```python
import torch

def _softmax(x, axis=-1):
    return torch.softmax(x, dim=axis)
def _gelu(x):
    return torch.nn.functional.gelu(x, approximate="tanh")
def _silu(x):
    return torch.nn.functional.silu(x)
def _sigmoid(x):
    return torch.sigmoid(x)
def _layernorm(x, axis=-1, eps=1e-5):
    m = x.mean(dim=axis, keepdim=True)
    v = x.var(dim=axis, keepdim=True, unbiased=False)
    return (x - m) / torch.sqrt(v + eps)
def _scatter_add(tgt, idx, src, axis=0):
    return tgt.clone().index_add_(axis, idx, src)
def _segment_sum(src, seg, n):
    out = torch.zeros((n,) + src.shape[1:], dtype=src.dtype)
    return out.index_add_(0, seg, src)
def _pad(x, pw):
    flat = tuple(v for pair in reversed(pw) for v in pair)
    return torch.nn.functional.pad(x, flat)

kernel_name = 'standalone__w8a8_smoothquant_per_tensor_per_channel_int8_activations_weights__prompt'

def setup_inputs():

    _w = (lambda a: (lambda t: t.to(torch.bfloat16) if t.is_floating_point() else t)(torch.as_tensor(a)))
    B, S, D, F = 1, 2048, 1024, 3584
    x = abs((lambda *s: torch.randn(*s, dtype=torch.bfloat16) * 0.05)(B, S, D))
    w = (lambda *s: torch.randn(*s, dtype=torch.bfloat16) * 0.05)(D, F)
    x_min, x_max = float(x.min()), float(x.max())
    sx = (x_max - x_min) / 255.0 + 1e-6
    zx = int(torch.as_tensor(-128.0 - x_min / sx).round())
    qx = torch.clamp(torch.as_tensor(x / sx).round() + zx, -128, 127).to(torch.int32)
    sw = (torch.abs(w).amax(dim=0) / 127.0 + 1e-6).to(float)
    qw = torch.clamp(torch.as_tensor(w / sw[None, :]).round(), -127, 127).to(torch.int32)
    return {
        "qx": _w(qx),
        "qw": _w(qw),
        "scale_x": _w(torch.tensor(sx, dtype=float)),
        "zero_x": _w(torch.tensor(zx, dtype=torch.int32)),
        "scale_w": _w(sw),
        "bias": _w((lambda *s: torch.randn(*s, dtype=torch.bfloat16) * 0.05)(F)),
    }

def reference(qx, qw, scale_x, zero_x, scale_w, bias):
    dt = scale_w.dtype
    acc = torch.einsum("bsd,df->bsf", (lambda x, dt: x.to(dt))(qx, dt), (lambda x, dt: x.to(dt))(qw, dt))
    w_colsum = torch.sum((lambda x, dt: x.to(dt))(qw, dt), axis=0)
    acc = acc - (lambda x, dt: x.to(dt))(zero_x, dt) * w_colsum[None, None, :]
    return acc * scale_x * scale_w[None, None, :] + bias[None, None, :]
```

```cpp
#define DIAG 0
#include <hip/hip_runtime.h>
#include <hip/hip_bf16.h>
#include <stdint.h>

typedef int i32x4 __attribute__((ext_vector_type(4)));
typedef unsigned int u32x4 __attribute__((ext_vector_type(4)));
typedef unsigned int u32x2 __attribute__((ext_vector_type(2)));
typedef float f32x2 __attribute__((ext_vector_type(2)));
typedef float f32x4 __attribute__((ext_vector_type(4)));
typedef __bf16 bf16x2_t __attribute__((ext_vector_type(2)));
typedef unsigned long long u64;
#define GAS __attribute__((address_space(1)))
#define LAS __attribute__((address_space(3)))

constexpr int M_ = 2048, K_ = 1024, N_ = 3584;
constexpr int BM = 256, BN = 112;
constexpr int BLK_STRIDE = 1056;
constexpr int KB_STRIDE = 7 * BLK_STRIDE;
constexpr int BIMG_BYTES = 16 * KB_STRIDE;
constexpr int OFF_READY = BIMG_BYTES;
constexpr int OFF_AREADY = OFF_READY + 32;
constexpr int OFF_COLSUM = OFF_AREADY + 32;
constexpr int OFF_SCALE = OFF_COLSUM + 448;
constexpr int OFF_BIAS = OFF_SCALE + 448;
constexpr int SMEM_BYTES = OFF_BIAS + 448;
constexpr size_t WS_EPOCH = 0;
constexpr size_t WS_FLAGS = 4096;
constexpr size_t WS_A8 = 65536;
constexpr size_t WS_DBG = WS_A8 + (size_t)M_ * K_;
constexpr int AUX_SC1 = 16;
constexpr int RSRC_W3 = 0x00020000;

__device__ __forceinline__ unsigned pack4(int a, int b, int c, int d) {
    const unsigned lo = __builtin_amdgcn_perm((unsigned)b, (unsigned)a, 0x0c0c0400u);
    const unsigned hi = __builtin_amdgcn_perm((unsigned)d, (unsigned)c, 0x0c0c0400u);
    return __builtin_amdgcn_perm(hi, lo, 0x05040100u);
}
__device__ __forceinline__ unsigned lds_read_u32(unsigned addr) {
    unsigned v;
    asm volatile("ds_read_b32 %0, %1\n\ts_waitcnt lgkmcnt(0)" : "=v"(v) : "v"(addr) : "memory");
    return v;
}
__device__ __forceinline__ void lds_wait_ge(unsigned addr, unsigned want) {
    while ((unsigned)__builtin_amdgcn_readfirstlane(lds_read_u32(addr)) < want) __builtin_amdgcn_s_sleep(1);
}

#define TS(i) do { } while (0)

#define DSR(DST, ADDR, OFF) asm volatile("ds_read_b128 %0, %1 offset:%2" : "=v"(DST) : "v"(ADDR), "n"(OFF) : "memory")
#define MF(I, J, SET, KB) asm volatile("v_mfma_i32_16x16x64_i8 %0, %1, %2, %0" : "+v"(acc[I][J]) : "v"(bfr[SET][J]), "v"(af[I][KB]))

template <bool EARLY>
__device__ __forceinline__ void compute_and_store(const i32x4 (&af)[2][16], const unsigned smem_base, const int lane,
                                                  const int zx, __hip_bfloat16* __restrict__ outp
) {
    i32x4 acc[2][7];
    i32x4 bfr[2][7];
#pragma unroll
    for (int i = 0; i < 2; ++i)
#pragma unroll
        for (int j = 0; j < 7; ++j) {
            acc[i][j] = (i32x4){0, 0, 0, 0};
            asm volatile("" : "+v"(acc[i][j]));
        }
    const unsigned fB = smem_base + lane * 16;
    if (EARLY) lds_wait_ge(smem_base + OFF_READY, 4);
#pragma unroll
    for (int j = 0; j < 7; ++j) DSR(bfr[0][j], fB, j * BLK_STRIDE);
#pragma unroll
    for (int kb = 0; kb < 16; ++kb) {
        const int S = kb & 1;
        if (EARLY && (kb & 1) && kb + 1 < 16) lds_wait_ge(smem_base + OFF_READY + ((kb + 1) >> 1) * 4, 4);
        asm volatile("s_waitcnt lgkmcnt(0)" ::: "memory");
        __builtin_amdgcn_sched_barrier(0);
        if (kb + 1 < 16) {
            const unsigned a = fB + ((kb + 1) >> 3) * (8 * KB_STRIDE);
            constexpr int dummy = 0; (void)dummy;
#define OFFS(J) ((((kb + 1) & 7) * KB_STRIDE) + (J) * BLK_STRIDE)
            MF(0, 0, S, kb);
            DSR(bfr[S ^ 1][0], a, OFFS(0)); MF(1, 0, S, kb);
            DSR(bfr[S ^ 1][1], a, OFFS(1)); MF(0, 1, S, kb);
            DSR(bfr[S ^ 1][2], a, OFFS(2)); MF(1, 1, S, kb);
            DSR(bfr[S ^ 1][3], a, OFFS(3)); MF(0, 2, S, kb);
            DSR(bfr[S ^ 1][4], a, OFFS(4)); MF(1, 2, S, kb);
            DSR(bfr[S ^ 1][5], a, OFFS(5)); MF(0, 3, S, kb);
            DSR(bfr[S ^ 1][6], a, OFFS(6)); MF(1, 3, S, kb);
        } else {
#pragma unroll
            for (int j = 0; j < 4; ++j) { MF(0, j, S, kb); MF(1, j, S, kb); }
        }
#pragma unroll
        for (int j = 4; j < 7; ++j) { MF(0, j, S, kb); MF(1, j, S, kb); }
    }
    TS(6);
    const int kq = lane >> 4;
    const LAS uint8_t* sm = (const LAS uint8_t*)(uintptr_t)smem_base;
    asm volatile("s_nop 7\n\ts_nop 7" ::: "memory");
#pragma unroll
    for (int j = 0; j < 7; ++j) asm volatile("" : "+v"(acc[0][j]), "+v"(acc[1][j]));
    uint8_t* o0 = reinterpret_cast<uint8_t*>(outp) + (size_t)(lane & 15) * (N_ * 2);
#pragma unroll
    for (int p = 0; p < 4; ++p) {
        const int nh = p < 3 ? 2 : 1;
        const int c0 = p < 3 ? p * 32 + kq * 8 : 96 + kq * 4;
        unsigned o[2][4];
#pragma unroll
        for (int h = 0; h < nh; ++h) {
            const i32x4 cs = *reinterpret_cast<const LAS i32x4*>(sm + OFF_COLSUM + (c0 + 4 * h) * 4);
            const f32x4 sc = *reinterpret_cast<const LAS f32x4*>(sm + OFF_SCALE + (c0 + 4 * h) * 4);
            const f32x4 bi = *reinterpret_cast<const LAS f32x4*>(sm + OFF_BIAS + (c0 + 4 * h) * 4);
#pragma unroll
            for (int i = 0; i < 2; ++i) {
                const i32x4 a = acc[i][2 * p + h];
                f32x2 v0 = {(float)(a[0] - zx * cs[0]) * sc[0] + bi[0], (float)(a[1] - zx * cs[1]) * sc[1] + bi[1]};
                f32x2 v1 = {(float)(a[2] - zx * cs[2]) * sc[2] + bi[2], (float)(a[3] - zx * cs[3]) * sc[3] + bi[3]};
                o[i][2 * h] = __builtin_bit_cast(unsigned, __builtin_convertvector(v0, bf16x2_t));
                o[i][2 * h + 1] = __builtin_bit_cast(unsigned, __builtin_convertvector(v1, bf16x2_t));
            }
        }
#pragma unroll
        for (int i = 0; i < 2; ++i) {
            uint8_t* dst = o0 + (size_t)i * 16 * (N_ * 2) + c0 * 2;
            if (p < 3) {
                const u32x4 v = {o[i][0], o[i][1], o[i][2], o[i][3]};
                *reinterpret_cast<u32x4*>(dst) = v;
            } else {
                const u32x2 v = {o[i][0], o[i][1]};
                *reinterpret_cast<u32x2*>(dst) = v;
            }
        }
    }
    TS(7);
}

__global__ __launch_bounds__(512, 2) void fused_kernel(const int* __restrict__ qx, const int* __restrict__ qw,
                                                       const __hip_bfloat16* __restrict__ scale_x,
                                                       const int* __restrict__ zero_x,
                                                       const __hip_bfloat16* __restrict__ scale_w,
                                                       const __hip_bfloat16* __restrict__ bias,
                                                       __hip_bfloat16* __restrict__ out, uint8_t* __restrict__ ws) {
    extern __shared__ __attribute__((aligned(16))) uint8_t smem[];
    const int tid = threadIdx.x, lane = tid & 63;
    const int wid = __builtin_amdgcn_readfirstlane(tid >> 6);
    const int bid = blockIdx.x;
    const int tm = bid >> 5;
    const int tn = (bid & 7) * 4 + ((bid >> 3) & 3);
    const unsigned smem_base = (unsigned)(uintptr_t)smem;

    u64 dg[10];
    {
        const u64 tstart = __builtin_amdgcn_s_memrealtime();
        if (bid == 0 && wid == 0) {
            auto ld = [&](const void* p) {
                unsigned v;
                asm volatile("global_load_dword %0, %1, off\n\ts_waitcnt vmcnt(0)" : "=v"(v) : "v"(p) : "memory");
                return v;
            };
            unsigned x = 0;
            dg[0] = __builtin_amdgcn_s_memtime();
            x += ld(qx);                                   dg[1] = __builtin_amdgcn_s_memtime();
            x += ld(qx + 1024);                            dg[2] = __builtin_amdgcn_s_memtime();
            x += ld(qx + 16 * 1024);                       dg[3] = __builtin_amdgcn_s_memtime();
            x += ld(qx + 1024 * 1024);                     dg[4] = __builtin_amdgcn_s_memtime();
            x += ld(qw);                                   dg[5] = __builtin_amdgcn_s_memtime();
            x += ld(out);                                  dg[6] = __builtin_amdgcn_s_memtime();
            x += ld(ws + (64u << 20));                     dg[7] = __builtin_amdgcn_s_memtime();
            x += ld(qx + 64);                              dg[8] = __builtin_amdgcn_s_memtime();
            asm volatile("" :: "v"(x));
        }
        while (__builtin_amdgcn_s_memrealtime() - tstart < 2000) __builtin_amdgcn_s_sleep(4);
    }
    const __amdgpu_buffer_rsrc_t rsA8 = __builtin_amdgcn_make_buffer_rsrc((void*)(ws + WS_A8), 0, M_ * K_, RSRC_W3);
    i32x4 af[2][16];

    if (wid >= 4) {
        const int lw = wid - 4;
        const __amdgpu_buffer_rsrc_t rsW = __builtin_amdgcn_make_buffer_rsrc((void*)qw, 0, K_ * N_ * 4, RSRC_W3);
        const int le = lane < 56 ? lane : lane - 8;
        const int gs = le >= 28 ? 1 : 0;
        const int q = le - 28 * gs;
        const int g = 2 * (lw & 1) + gs;
        const unsigned voff = (unsigned)((tn * BN + q * 4) * 4 + (g * 4 + (lw >> 1) * 64) * (N_ * 4));
        const int cb = q < 24 ? 2 * (q >> 3) + (q & 1) : 6;
        const int sl = q < 24 ? ((q & 7) >> 1) * 4 : (q - 24) * 4;
        LAS uint8_t* dst0 = (LAS uint8_t*)(uintptr_t)(smem_base + (lw >> 1) * KB_STRIDE + cb * BLK_STRIDE + (sl + 16 * g) * 16);
        i32x4 st[3][16];
        int cs[4] = {0, 0, 0, 0};
#define LOAD_ROUND(R)                                                                                      \
    do {                                                                                                   \
        _Pragma("unroll") for (int x = 0; x < 16; ++x)                                                     \
            st[(R) % 3][x] = __builtin_bit_cast(i32x4, __builtin_amdgcn_raw_buffer_load_b128(              \
                rsW, voff, ((R) * 128 + (x >> 2) * 16 + (x & 3)) * (N_ * 4), 0));                          \
    } while (0)
        LOAD_ROUND(0);
        LOAD_ROUND(1);
        LOAD_ROUND(2);
        __builtin_amdgcn_sched_barrier(0);
        __builtin_amdgcn_s_barrier();
        __builtin_amdgcn_sched_barrier(0);
        TS(1);
#pragma unroll
        for (int R = 0; R < 8; ++R) {
            u32x4 pk[4];
#pragma unroll
            for (int c = 0; c < 4; ++c)
#pragma unroll
                for (int i = 0; i < 4; ++i) {
                    pk[c][i] = pack4(st[R % 3][4 * i][c], st[R % 3][4 * i + 1][c], st[R % 3][4 * i + 2][c], st[R % 3][4 * i + 3][c]);
                    cs[c] = __builtin_amdgcn_sdot4((int)pk[c][i], 0x01010101, cs[c], false);
                }
#pragma unroll
            for (int c = 0; c < 4; ++c) asm volatile("" : "+v"(pk[c]), "+v"(cs[c]));
            __builtin_amdgcn_sched_barrier(0);
            if (R + 3 < 8) LOAD_ROUND(R + 3);
            __builtin_amdgcn_sched_barrier(0);
#pragma unroll
            for (int c = 0; c < 4; ++c) *reinterpret_cast<LAS u32x4*>(dst0 + R * 2 * KB_STRIDE + c * 16) = pk[c];
            if (R == 7) {
#pragma unroll
                for (int c = 0; c < 4; ++c)
                    __hip_atomic_fetch_add((LAS int*)(uintptr_t)(smem_base + OFF_COLSUM + (q * 4 + c) * 4), lane < 56 ? cs[c] : 0,
                                           __ATOMIC_RELAXED, __HIP_MEMORY_SCOPE_WORKGROUP);
            }
            asm volatile("s_waitcnt lgkmcnt(0)" ::: "memory");
            if (lane == 0)
                __hip_atomic_fetch_add((LAS unsigned*)(uintptr_t)(smem_base + OFF_READY + R * 4), 1u, __ATOMIC_RELAXED,
                                       __HIP_MEMORY_SCOPE_WORKGROUP);
            __builtin_amdgcn_sched_barrier(0);
            if (R == 5 || R == 6) {
                if (R == 5) lds_wait_ge(smem_base + OFF_AREADY + lw * 4, 1);
#pragma unroll
                for (int kb = 0; kb < 16; ++kb)
                    af[R - 5][kb] = __builtin_bit_cast(i32x4, __builtin_amdgcn_raw_buffer_load_b128(
                        rsA8, lane * 16, ((tm * 16 + 8 + 2 * lw + (R - 5)) * 16 + kb) * 1024, AUX_SC1));
            }
            __builtin_amdgcn_sched_barrier(0);
            if (R == 0) TS(2);
            if (R == 3) TS(3);
        }
        TS(4);
        const int zx = zero_x[0];
#pragma unroll
        for (int R = 0; R < 8; ++R) lds_wait_ge(smem_base + OFF_READY + R * 4, 4);
        TS(5);
        compute_and_store<false>(af, smem_base, lane, zx, out + (size_t)(tm * BM + 128 + lw * 32) * N_ + tn * BN
        );
    } else {
        const int rbs = tm * 16 + ((bid & 31) >> 1);
        const int kbs = (bid & 1) * 8 + 2 * wid;
        const int* src = qx + (size_t)(rbs * 16 + (lane & 15)) * K_ + kbs * 64 + (lane >> 4) * 4;
        i32x4 v[8];
#pragma unroll
        for (int x = 0; x < 8; ++x) v[x] = *reinterpret_cast<const i32x4*>(src + x * 16);
        const u64 ep = __hip_atomic_load((GAS u64*)(ws + WS_EPOCH), __ATOMIC_RELAXED, __HIP_MEMORY_SCOPE_AGENT);
        const int cl = wid * 64 + lane;
        const int clc = cl < BN ? cl : 0;
        const unsigned short sw_b = reinterpret_cast<const unsigned short*>(scale_w)[tn * BN + clc];
        const unsigned short bi_b = reinterpret_cast<const unsigned short*>(bias)[tn * BN + clc];
        const unsigned short sx_b = reinterpret_cast<const unsigned short*>(scale_x)[0];
        const int zx = zero_x[0];
        if (wid >= 2) *(LAS unsigned*)(uintptr_t)(smem_base + OFF_READY + ((wid - 2) * 64 + lane) * 4) = 0u;
        __builtin_amdgcn_sched_barrier(0);
        asm volatile("s_waitcnt lgkmcnt(0)" ::: "memory");
        __builtin_amdgcn_s_barrier();
        __builtin_amdgcn_sched_barrier(0);
        TS(1);
        const u64 tag = ep + 0x9E3779B97F4A7C15ull;
#pragma unroll
        for (int b = 0; b < 2; ++b) {
            u32x4 o;
#pragma unroll
            for (int i = 0; i < 4; ++i) o[i] = pack4(v[4 * b + i][0], v[4 * b + i][1], v[4 * b + i][2], v[4 * b + i][3]);
            __builtin_amdgcn_raw_buffer_store_b128(o, rsA8, lane * 16, (rbs * 16 + kbs + b) * 1024, AUX_SC1);
        }
        TS(2);
        asm volatile("s_waitcnt vmcnt(0)" ::: "memory");
        GAS u64* flags = (GAS u64*)(ws + WS_FLAGS);
        if (lane < 2) __hip_atomic_store(flags + rbs * 16 + kbs + lane, tag, __ATOMIC_RELAXED, __HIP_MEMORY_SCOPE_AGENT);
        TS(3);
        if (cl < BN) {
            *(LAS float*)(uintptr_t)(smem_base + OFF_SCALE + cl * 4) =
                __uint_as_float((unsigned)sx_b << 16) * __uint_as_float((unsigned)sw_b << 16);
            *(LAS float*)(uintptr_t)(smem_base + OFF_BIAS + cl * 4) = __uint_as_float((unsigned)bi_b << 16);
        }
        {
            const int rb = tm * 16 + 2 * wid + ((lane >> 4) & 1) + (lane >> 5) * 8;
            GAS u64* f = flags + rb * 16 + (lane & 15);
            for (;;) {
                const u64 x = __hip_atomic_load(f, __ATOMIC_RELAXED, __HIP_MEMORY_SCOPE_AGENT);
                if (__all(x == tag)) break;
                __builtin_amdgcn_s_sleep(2);
            }
            asm volatile("" ::: "memory");
        }
        TS(4);
        if (lane == 0) *(volatile LAS unsigned*)(uintptr_t)(smem_base + OFF_AREADY + wid * 4) = 1u;
#pragma unroll
        for (int kb = 0; kb < 16; ++kb)
#pragma unroll
            for (int i = 0; i < 2; ++i)
                af[i][kb] = __builtin_bit_cast(i32x4, __builtin_amdgcn_raw_buffer_load_b128(
                    rsA8, lane * 16, ((tm * 16 + 2 * wid + i) * 16 + kb) * 1024, AUX_SC1));
        __builtin_amdgcn_sched_barrier(0);
        TS(5);
        compute_and_store<true>(af, smem_base, lane, zx, out + (size_t)(tm * BM + wid * 32) * N_ + tn * BN
        );
        if (bid == 0 && wid == 0) {
            for (;;) {
                bool ok = true;
#pragma unroll
                for (int i = 0; i < 32; ++i)
                    ok &= __hip_atomic_load(flags + i * 64 + lane, __ATOMIC_RELAXED, __HIP_MEMORY_SCOPE_AGENT) == tag;
                if (__all(ok)) break;
                __builtin_amdgcn_s_sleep(2);
            }
            if (lane == 0) __hip_atomic_store((GAS u64*)(ws + WS_EPOCH), tag, __ATOMIC_RELAXED, __HIP_MEMORY_SCOPE_AGENT);
        }
    }

    if (bid == 0 && wid == 0 && DIAG > 0) {
        const u64 d = dg[DIAG] - dg[DIAG - 1];
        const u64 t = __builtin_amdgcn_s_memrealtime();
        while (__builtin_amdgcn_s_memrealtime() - t < d) __builtin_amdgcn_s_sleep(4);
    }
}

extern "C" void kernel_launch(void* const* d_in, const int* in_sizes, int n_in,
                              void* d_out, int out_size, void* d_ws, size_t ws_size,
                              hipStream_t stream) {
    const int* qx = (const int*)d_in[0];
    const int* qw = (const int*)d_in[1];
    const __hip_bfloat16* scale_x = (const __hip_bfloat16*)d_in[2];
    const int* zero_x = (const int*)d_in[3];
    const __hip_bfloat16* scale_w = (const __hip_bfloat16*)d_in[4];
    const __hip_bfloat16* bias = (const __hip_bfloat16*)d_in[5];
    (void)hipFuncSetAttribute((const void*)fused_kernel, hipFuncAttributeMaxDynamicSharedMemorySize, SMEM_BYTES);
    fused_kernel<<<256, 512, SMEM_BYTES, stream>>>(qx, qw, scale_x, zero_x, scale_w, bias, (__hip_bfloat16*)d_out,
                                                   (uint8_t*)d_ws);
}
```

```cpp
#define PROF_TS
#define DIAG2 0
#define SC1_OUT
#include <hip/hip_runtime.h>
#include <hip/hip_bf16.h>
#include <stdint.h>

typedef int i32x4 __attribute__((ext_vector_type(4)));
typedef unsigned int u32x4 __attribute__((ext_vector_type(4)));
typedef unsigned int u32x2 __attribute__((ext_vector_type(2)));
typedef float f32x2 __attribute__((ext_vector_type(2)));
typedef float f32x4 __attribute__((ext_vector_type(4)));
typedef __bf16 bf16x2_t __attribute__((ext_vector_type(2)));
typedef unsigned long long u64;
#define GAS __attribute__((address_space(1)))
#define LAS __attribute__((address_space(3)))

constexpr int M_ = 2048, K_ = 1024, N_ = 3584;
constexpr int BM = 256, BN = 112;
constexpr int BLK_STRIDE = 1056;
constexpr int KB_STRIDE = 7 * BLK_STRIDE;
constexpr int BIMG_BYTES = 16 * KB_STRIDE;
constexpr int OFF_READY = BIMG_BYTES;
constexpr int OFF_AREADY = OFF_READY + 32;
constexpr int OFF_COLSUM = OFF_AREADY + 32;
constexpr int OFF_SCALE = OFF_COLSUM + 448;
constexpr int OFF_BIAS = OFF_SCALE + 448;
constexpr int SMEM_BYTES = OFF_BIAS + 448;
constexpr size_t WS_EPOCH = 0;
constexpr size_t WS_FLAGS = 4096;
constexpr size_t WS_A8 = 65536;
constexpr size_t WS_DBG = WS_A8 + (size_t)M_ * K_;
constexpr int AUX_SC1 = 16;
constexpr int RSRC_W3 = 0x00020000;

__device__ __forceinline__ unsigned pack4(int a, int b, int c, int d) {
    const unsigned lo = __builtin_amdgcn_perm((unsigned)b, (unsigned)a, 0x0c0c0400u);
    const unsigned hi = __builtin_amdgcn_perm((unsigned)d, (unsigned)c, 0x0c0c0400u);
    return __builtin_amdgcn_perm(hi, lo, 0x05040100u);
}
__device__ __forceinline__ unsigned lds_read_u32(unsigned addr) {
    unsigned v;
    asm volatile("ds_read_b32 %0, %1\n\ts_waitcnt lgkmcnt(0)" : "=v"(v) : "v"(addr) : "memory");
    return v;
}
__device__ __forceinline__ void lds_wait_ge(unsigned addr, unsigned want) {
    while ((unsigned)__builtin_amdgcn_readfirstlane(lds_read_u32(addr)) < want) __builtin_amdgcn_s_sleep(1);
}

#define TS(i) do { ts[i] = __builtin_amdgcn_s_memrealtime(); } while (0)

#define DSR(DST, ADDR, OFF) asm volatile("ds_read_b128 %0, %1 offset:%2" : "=v"(DST) : "v"(ADDR), "n"(OFF) : "memory")
#define MF(I, J, SET, KB) asm volatile("v_mfma_i32_16x16x64_i8 %0, %1, %2, %0" : "+v"(acc[I][J]) : "v"(bfr[SET][J]), "v"(af[I][KB]))

template <bool EARLY>
__device__ __forceinline__ void compute_and_store(const i32x4 (&af)[2][16], const unsigned smem_base, const int lane,
                                                  const int zx, __hip_bfloat16* __restrict__ outp
                                                  , u64 (&ts)[12]
) {
    i32x4 acc[2][7];
    i32x4 bfr[2][7];
#pragma unroll
    for (int i = 0; i < 2; ++i)
#pragma unroll
        for (int j = 0; j < 7; ++j) {
            acc[i][j] = (i32x4){0, 0, 0, 0};
            asm volatile("" : "+v"(acc[i][j]));
        }
    const unsigned fB = smem_base + lane * 16;
    if (EARLY) lds_wait_ge(smem_base + OFF_READY, 4);
#pragma unroll
    for (int j = 0; j < 7; ++j) DSR(bfr[0][j], fB, j * BLK_STRIDE);
#pragma unroll
    for (int kb = 0; kb < 16; ++kb) {
        const int S = kb & 1;
        if (EARLY && (kb & 1) && kb + 1 < 16) lds_wait_ge(smem_base + OFF_READY + ((kb + 1) >> 1) * 4, 4);
        asm volatile("s_waitcnt lgkmcnt(0)" ::: "memory");
        __builtin_amdgcn_sched_barrier(0);
        if (kb + 1 < 16) {
            const unsigned a = fB + ((kb + 1) >> 3) * (8 * KB_STRIDE);
            constexpr int dummy = 0; (void)dummy;
#define OFFS(J) ((((kb + 1) & 7) * KB_STRIDE) + (J) * BLK_STRIDE)
            MF(0, 0, S, kb);
            DSR(bfr[S ^ 1][0], a, OFFS(0)); MF(1, 0, S, kb);
            DSR(bfr[S ^ 1][1], a, OFFS(1)); MF(0, 1, S, kb);
            DSR(bfr[S ^ 1][2], a, OFFS(2)); MF(1, 1, S, kb);
            DSR(bfr[S ^ 1][3], a, OFFS(3)); MF(0, 2, S, kb);
            DSR(bfr[S ^ 1][4], a, OFFS(4)); MF(1, 2, S, kb);
            DSR(bfr[S ^ 1][5], a, OFFS(5)); MF(0, 3, S, kb);
            DSR(bfr[S ^ 1][6], a, OFFS(6)); MF(1, 3, S, kb);
        } else {
#pragma unroll
            for (int j = 0; j < 4; ++j) { MF(0, j, S, kb); MF(1, j, S, kb); }
        }
#pragma unroll
        for (int j = 4; j < 7; ++j) { MF(0, j, S, kb); MF(1, j, S, kb); }
    }
    TS(6);
    const int kq = lane >> 4;
    const LAS uint8_t* sm = (const LAS uint8_t*)(uintptr_t)smem_base;
    asm volatile("s_nop 7\n\ts_nop 7" ::: "memory");
#pragma unroll
    for (int j = 0; j < 7; ++j) asm volatile("" : "+v"(acc[0][j]), "+v"(acc[1][j]));
    uint8_t* o0 = reinterpret_cast<uint8_t*>(outp) + (size_t)(lane & 15) * (N_ * 2);
#pragma unroll
    for (int p = 0; p < 4; ++p) {
        const int nh = p < 3 ? 2 : 1;
        const int c0 = p < 3 ? p * 32 + kq * 8 : 96 + kq * 4;
        unsigned o[2][4];
#pragma unroll
        for (int h = 0; h < nh; ++h) {
            const i32x4 cs = *reinterpret_cast<const LAS i32x4*>(sm + OFF_COLSUM + (c0 + 4 * h) * 4);
            const f32x4 sc = *reinterpret_cast<const LAS f32x4*>(sm + OFF_SCALE + (c0 + 4 * h) * 4);
            const f32x4 bi = *reinterpret_cast<const LAS f32x4*>(sm + OFF_BIAS + (c0 + 4 * h) * 4);
#pragma unroll
            for (int i = 0; i < 2; ++i) {
                const i32x4 a = acc[i][2 * p + h];
                f32x2 v0 = {(float)(a[0] - zx * cs[0]) * sc[0] + bi[0], (float)(a[1] - zx * cs[1]) * sc[1] + bi[1]};
                f32x2 v1 = {(float)(a[2] - zx * cs[2]) * sc[2] + bi[2], (float)(a[3] - zx * cs[3]) * sc[3] + bi[3]};
                o[i][2 * h] = __builtin_bit_cast(unsigned, __builtin_convertvector(v0, bf16x2_t));
                o[i][2 * h + 1] = __builtin_bit_cast(unsigned, __builtin_convertvector(v1, bf16x2_t));
            }
        }
#pragma unroll
        for (int i = 0; i < 2; ++i) {
            uint8_t* dst = o0 + (size_t)i * 16 * (N_ * 2) + c0 * 2;
            if (p < 3) {
                const u32x4 v = {o[i][0], o[i][1], o[i][2], o[i][3]};
                asm volatile("global_store_dwordx4 %0, %1, off sc0 sc1\n\ts_nop 1" :: "v"(dst), "v"(v) : "memory");
            } else {
                const u32x2 v = {o[i][0], o[i][1]};
                asm volatile("global_store_dwordx2 %0, %1, off sc0 sc1\n\ts_nop 1" :: "v"(dst), "v"(v) : "memory");
            }
        }
    }
    TS(7);
}

__global__ __launch_bounds__(512, 2) void fused_kernel(const int* __restrict__ qx, const int* __restrict__ qw,
                                                       const __hip_bfloat16* __restrict__ scale_x,
                                                       const int* __restrict__ zero_x,
                                                       const __hip_bfloat16* __restrict__ scale_w,
                                                       const __hip_bfloat16* __restrict__ bias,
                                                       __hip_bfloat16* __restrict__ out, uint8_t* __restrict__ ws) {
    extern __shared__ __attribute__((aligned(16))) uint8_t smem[];
    const int tid = threadIdx.x, lane = tid & 63;
    const int wid = __builtin_amdgcn_readfirstlane(tid >> 6);
    const int bid = blockIdx.x;
    const int tm = bid >> 5;
    const int tn = (bid & 7) * 4 + ((bid >> 3) & 3);
    const unsigned smem_base = (unsigned)(uintptr_t)smem;
    u64 ts[12];
#pragma unroll
    for (int i = 0; i < 12; ++i) ts[i] = 0;
    TS(0);
    const __amdgpu_buffer_rsrc_t rsA8 = __builtin_amdgcn_make_buffer_rsrc((void*)(ws + WS_A8), 0, M_ * K_, RSRC_W3);
    i32x4 af[2][16];

    if (wid >= 4) {
        const int lw = wid - 4;
        const __amdgpu_buffer_rsrc_t rsW = __builtin_amdgcn_make_buffer_rsrc((void*)qw, 0, K_ * N_ * 4, RSRC_W3);
        const int le = lane < 56 ? lane : lane - 8;
        const int gs = le >= 28 ? 1 : 0;
        const int q = le - 28 * gs;
        const int g = 2 * (lw & 1) + gs;
        const unsigned voff = (unsigned)((tn * BN + q * 4) * 4 + (g * 4 + (lw >> 1) * 64) * (N_ * 4));
        const int cb = q < 24 ? 2 * (q >> 3) + (q & 1) : 6;
        const int sl = q < 24 ? ((q & 7) >> 1) * 4 : (q - 24) * 4;
        LAS uint8_t* dst0 = (LAS uint8_t*)(uintptr_t)(smem_base + (lw >> 1) * KB_STRIDE + cb * BLK_STRIDE + (sl + 16 * g) * 16);
        i32x4 st[3][16];
        int cs[4] = {0, 0, 0, 0};
#define LOAD_ROUND(R)                                                                                      \
    do {                                                                                                   \
        _Pragma("unroll") for (int x = 0; x < 16; ++x)                                                     \
            st[(R) % 3][x] = __builtin_bit_cast(i32x4, __builtin_amdgcn_raw_buffer_load_b128(              \
                rsW, voff, ((R) * 128 + (x >> 2) * 16 + (x & 3)) * (N_ * 4), 0));                          \
    } while (0)
        LOAD_ROUND(0);
        LOAD_ROUND(1);
        LOAD_ROUND(2);
        __builtin_amdgcn_sched_barrier(0);
        __builtin_amdgcn_s_barrier();
        __builtin_amdgcn_sched_barrier(0);
        TS(1);
#pragma unroll
        for (int R = 0; R < 8; ++R) {
            u32x4 pk[4];
#pragma unroll
            for (int c = 0; c < 4; ++c)
#pragma unroll
                for (int i = 0; i < 4; ++i) {
                    pk[c][i] = pack4(st[R % 3][4 * i][c], st[R % 3][4 * i + 1][c], st[R % 3][4 * i + 2][c], st[R % 3][4 * i + 3][c]);
                    cs[c] = __builtin_amdgcn_sdot4((int)pk[c][i], 0x01010101, cs[c], false);
                }
#pragma unroll
            for (int c = 0; c < 4; ++c) asm volatile("" : "+v"(pk[c]), "+v"(cs[c]));
            __builtin_amdgcn_sched_barrier(0);
            if (R + 3 < 8) LOAD_ROUND(R + 3);
            __builtin_amdgcn_sched_barrier(0);
#pragma unroll
            for (int c = 0; c < 4; ++c) *reinterpret_cast<LAS u32x4*>(dst0 + R * 2 * KB_STRIDE + c * 16) = pk[c];
            if (R == 7) {
#pragma unroll
                for (int c = 0; c < 4; ++c)
                    __hip_atomic_fetch_add((LAS int*)(uintptr_t)(smem_base + OFF_COLSUM + (q * 4 + c) * 4), lane < 56 ? cs[c] : 0,
                                           __ATOMIC_RELAXED, __HIP_MEMORY_SCOPE_WORKGROUP);
            }
            asm volatile("s_waitcnt lgkmcnt(0)" ::: "memory");
            if (lane == 0)
                __hip_atomic_fetch_add((LAS unsigned*)(uintptr_t)(smem_base + OFF_READY + R * 4), 1u, __ATOMIC_RELAXED,
                                       __HIP_MEMORY_SCOPE_WORKGROUP);
            __builtin_amdgcn_sched_barrier(0);
            if (R == 5 || R == 6) {
                if (R == 5) lds_wait_ge(smem_base + OFF_AREADY + lw * 4, 1);
#pragma unroll
                for (int kb = 0; kb < 16; ++kb)
                    af[R - 5][kb] = __builtin_bit_cast(i32x4, __builtin_amdgcn_raw_buffer_load_b128(
                        rsA8, lane * 16, ((tm * 16 + 8 + 2 * lw + (R - 5)) * 16 + kb) * 1024, AUX_SC1));
            }
            __builtin_amdgcn_sched_barrier(0);
            if (R == 0) TS(2);
            if (R == 3) TS(3);
        }
        TS(4);
        const int zx = zero_x[0];
#pragma unroll
        for (int R = 0; R < 8; ++R) lds_wait_ge(smem_base + OFF_READY + R * 4, 4);
        TS(5);
        compute_and_store<false>(af, smem_base, lane, zx, out + (size_t)(tm * BM + 128 + lw * 32) * N_ + tn * BN
                                 , ts
        );
    } else {
        const int rbs = tm * 16 + ((bid & 31) >> 1);
        const int kbs = (bid & 1) * 8 + 2 * wid;
        const int* src = qx + (size_t)(rbs * 16 + (lane & 15)) * K_ + kbs * 64 + (lane >> 4) * 4;
        i32x4 v[8];
#pragma unroll
        for (int x = 0; x < 8; ++x) v[x] = *reinterpret_cast<const i32x4*>(src + x * 16);
        const u64 ep = __hip_atomic_load((GAS u64*)(ws + WS_EPOCH), __ATOMIC_RELAXED, __HIP_MEMORY_SCOPE_AGENT);
        const int cl = wid * 64 + lane;
        const int clc = cl < BN ? cl : 0;
        const unsigned short sw_b = reinterpret_cast<const unsigned short*>(scale_w)[tn * BN + clc];
        const unsigned short bi_b = reinterpret_cast<const unsigned short*>(bias)[tn * BN + clc];
        const unsigned short sx_b = reinterpret_cast<const unsigned short*>(scale_x)[0];
        const int zx = zero_x[0];
        if (wid >= 2) *(LAS unsigned*)(uintptr_t)(smem_base + OFF_READY + ((wid - 2) * 64 + lane) * 4) = 0u;
        __builtin_amdgcn_sched_barrier(0);
        asm volatile("s_waitcnt lgkmcnt(0)" ::: "memory");
        __builtin_amdgcn_s_barrier();
        __builtin_amdgcn_sched_barrier(0);
        TS(1);
        const u64 tag = ep + 0x9E3779B97F4A7C15ull;
#pragma unroll
        for (int b = 0; b < 2; ++b) {
            u32x4 o;
#pragma unroll
            for (int i = 0; i < 4; ++i) o[i] = pack4(v[4 * b + i][0], v[4 * b + i][1], v[4 * b + i][2], v[4 * b + i][3]);
            __builtin_amdgcn_raw_buffer_store_b128(o, rsA8, lane * 16, (rbs * 16 + kbs + b) * 1024, AUX_SC1);
        }
        TS(2);
        asm volatile("s_waitcnt vmcnt(0)" ::: "memory");
        GAS u64* flags = (GAS u64*)(ws + WS_FLAGS);
        if (lane < 2) __hip_atomic_store(flags + rbs * 16 + kbs + lane, tag, __ATOMIC_RELAXED, __HIP_MEMORY_SCOPE_AGENT);
        TS(3);
        if (cl < BN) {
            *(LAS float*)(uintptr_t)(smem_base + OFF_SCALE + cl * 4) =
                __uint_as_float((unsigned)sx_b << 16) * __uint_as_float((unsigned)sw_b << 16);
            *(LAS float*)(uintptr_t)(smem_base + OFF_BIAS + cl * 4) = __uint_as_float((unsigned)bi_b << 16);
        }
        {
            const int rb = tm * 16 + 2 * wid + ((lane >> 4) & 1) + (lane >> 5) * 8;
            GAS u64* f = flags + rb * 16 + (lane & 15);
            for (;;) {
                const u64 x = __hip_atomic_load(f, __ATOMIC_RELAXED, __HIP_MEMORY_SCOPE_AGENT);
                if (__all(x == tag)) break;
                __builtin_amdgcn_s_sleep(2);
            }
            asm volatile("" ::: "memory");
        }
        TS(4);
        if (lane == 0) *(volatile LAS unsigned*)(uintptr_t)(smem_base + OFF_AREADY + wid * 4) = 1u;
#pragma unroll
        for (int kb = 0; kb < 16; ++kb)
#pragma unroll
            for (int i = 0; i < 2; ++i)
                af[i][kb] = __builtin_bit_cast(i32x4, __builtin_amdgcn_raw_buffer_load_b128(
                    rsA8, lane * 16, ((tm * 16 + 2 * wid + i) * 16 + kb) * 1024, AUX_SC1));
        __builtin_amdgcn_sched_barrier(0);
        TS(5);
        compute_and_store<true>(af, smem_base, lane, zx, out + (size_t)(tm * BM + wid * 32) * N_ + tn * BN
                                , ts
        );
        if (bid == 0 && wid == 0) {
            for (;;) {
                bool ok = true;
#pragma unroll
                for (int i = 0; i < 32; ++i)
                    ok &= __hip_atomic_load(flags + i * 64 + lane, __ATOMIC_RELAXED, __HIP_MEMORY_SCOPE_AGENT) == tag;
                if (__all(ok)) break;
                __builtin_amdgcn_s_sleep(2);
            }
            if (lane == 0) __hip_atomic_store((GAS u64*)(ws + WS_EPOCH), tag, __ATOMIC_RELAXED, __HIP_MEMORY_SCOPE_AGENT);
        }
    }
    asm volatile("s_waitcnt vmcnt(0)" ::: "memory");
    TS(8);
    if (wid == (DIAG2 / 16) * 4 && (DIAG2 % 16) > 0 && (bid & 31) == 5) {
        const u64 d = (ts[DIAG2 % 16] - ts[0]) * 5;
        const u64 t = __builtin_amdgcn_s_memrealtime();
        while (__builtin_amdgcn_s_memrealtime() - t < d) __builtin_amdgcn_s_sleep(4);
    }
}

extern "C" void kernel_launch(void* const* d_in, const int* in_sizes, int n_in,
                              void* d_out, int out_size, void* d_ws, size_t ws_size,
                              hipStream_t stream) {
    const int* qx = (const int*)d_in[0];
    const int* qw = (const int*)d_in[1];
    const __hip_bfloat16* scale_x = (const __hip_bfloat16*)d_in[2];
    const int* zero_x = (const int*)d_in[3];
    const __hip_bfloat16* scale_w = (const __hip_bfloat16*)d_in[4];
    const __hip_bfloat16* bias = (const __hip_bfloat16*)d_in[5];
    (void)hipFuncSetAttribute((const void*)fused_kernel, hipFuncAttributeMaxDynamicSharedMemorySize, SMEM_BYTES);
    fused_kernel<<<256, 512, SMEM_BYTES, stream>>>(qx, qw, scale_x, zero_x, scale_w, bias, (__hip_bfloat16*)d_out,
                                                   (uint8_t*)d_ws);
}
```

```cpp
#include <hip/hip_runtime.h>
#include <hip/hip_bf16.h>
#include <stdint.h>

typedef int i32x4 __attribute__((ext_vector_type(4)));
typedef unsigned int u32x4 __attribute__((ext_vector_type(4)));
typedef unsigned int u32x2 __attribute__((ext_vector_type(2)));
typedef float f32x2 __attribute__((ext_vector_type(2)));
typedef float f32x4 __attribute__((ext_vector_type(4)));
typedef __bf16 bf16x2_t __attribute__((ext_vector_type(2)));
typedef unsigned long long u64;
#define GAS __attribute__((address_space(1)))
#define LAS __attribute__((address_space(3)))

constexpr int M_ = 2048, K_ = 1024, N_ = 3584;
constexpr int BM = 256, BN = 112;
constexpr int G_STRIDE = 1792;
constexpr int KB_STRIDE = 4 * G_STRIDE;
constexpr int CH_STRIDE = 4 * KB_STRIDE;
constexpr int BIMG_BYTES = 4 * CH_STRIDE;
constexpr int OFF_SCR = BIMG_BYTES;
constexpr int OFF_CTL = OFF_SCR + 4 * 3584;
constexpr int OFF_CORR = OFF_CTL + 64;
constexpr int OFF_SCALE = OFF_CORR + 448;
constexpr int OFF_BIAS = OFF_SCALE + 448;
constexpr int SMEM_BYTES = OFF_BIAS + 448;
constexpr size_t WS_EPOCH = 0;
constexpr size_t WS_FLAGS = 4096;
constexpr size_t WS_A8 = 65536;
constexpr size_t WS_B8 = WS_A8 + (size_t)M_ * K_;
constexpr size_t WS_DBG = WS_B8 + (size_t)N_ * K_;
constexpr int AUX_SC1 = 16;
constexpr int RSRC_W3 = 0x00020000;
#define STAG 4
#define PSLEEP 16
#define OUT_AUX 0
#define TOKN 0
#define TOKPOS 2
#define RAW_AUX 2

__device__ __forceinline__ unsigned pack4(int a, int b, int c, int d) {
    const unsigned lo = __builtin_amdgcn_perm((unsigned)b, (unsigned)a, 0x0c0c0400u);
    const unsigned hi = __builtin_amdgcn_perm((unsigned)d, (unsigned)c, 0x0c0c0400u);
    return __builtin_amdgcn_perm(hi, lo, 0x05040100u);
}

#define TS(i) do { } while (0)

#define DSR(DST, ADDR, OFF) asm volatile("ds_read_b128 %0, %1 offset:%2" : "=v"(DST) : "v"(ADDR), "n"(OFF) : "memory")
#define MF(I, J, KK) asm volatile("v_mfma_i32_16x16x64_i8 %0, %1, %2, %0" : "+v"(acc[I][J]) : "v"(bfr[J]), "v"(af[KK][I]))

__device__ __forceinline__ u32x4 lds_rd128(unsigned addr) {
    u32x4 v;
    asm volatile("ds_read_b128 %0, %1\n\ts_waitcnt lgkmcnt(0)" : "=v"(v) : "v"(addr) : "memory");
    return v;
}

__global__ __launch_bounds__(512, 2) void fused_kernel(const int* __restrict__ qx, const int* __restrict__ qw,
                                                       const __hip_bfloat16* __restrict__ scale_x,
                                                       const int* __restrict__ zero_x,
                                                       const __hip_bfloat16* __restrict__ scale_w,
                                                       const __hip_bfloat16* __restrict__ bias,
                                                       __hip_bfloat16* __restrict__ out, uint8_t* __restrict__ ws) {
    extern __shared__ __attribute__((aligned(16))) uint8_t smem[];
    const int tid = threadIdx.x, lane = tid & 63;
    const int wid = __builtin_amdgcn_readfirstlane(tid >> 6);
    const int bid = blockIdx.x;
    const int tm = (bid & 1) * 4 + ((bid >> 3) & 3);
    const int tn = ((bid >> 1) & 3) * 8 + (bid >> 5);
    const unsigned smem_base = (unsigned)(uintptr_t)smem;
    const unsigned ctl = smem_base + OFF_CTL;
    const __amdgpu_buffer_rsrc_t rsA8 = __builtin_amdgcn_make_buffer_rsrc((void*)(ws + WS_A8), 0, M_ * K_, RSRC_W3);
    const __amdgpu_buffer_rsrc_t rsB8 = __builtin_amdgcn_make_buffer_rsrc((void*)(ws + WS_B8), 0, N_ * K_, RSRC_W3);
    GAS u64* flags = (GAS u64*)(ws + WS_FLAGS);
    u64 tag = 0;
    auto get_tag = [&]() -> u64 {
        for (;;) {
            const u32x4 v = lds_rd128(ctl + 16);
            if (__builtin_amdgcn_readfirstlane(v[2]) != 0) return ((u64)v[1] << 32) | v[0];
            __builtin_amdgcn_s_sleep(1);
        }
    };

    if (wid == 4) {
        if (lane < 16) *(LAS unsigned*)(uintptr_t)(ctl + lane * 4) = 0u;
        if (lane < 56) *(LAS u64*)(uintptr_t)(smem_base + OFF_CORR + lane * 8) = 0ull;
        asm volatile("s_waitcnt lgkmcnt(0)" ::: "memory");
    }
    if (wid >= 2) __builtin_amdgcn_s_barrier();

    if (wid == 4 || wid == 5) {
        const int h = wid - 4;
        if (h == 0) {
            const int l2 = lane < 56 ? lane : 0;
            const u64 ep = __hip_atomic_load((GAS u64*)(ws + WS_EPOCH), __ATOMIC_RELAXED, __HIP_MEMORY_SCOPE_AGENT);
            const unsigned sw2 = reinterpret_cast<const unsigned*>(scale_w)[tn * (BN / 2) + l2];
            const unsigned bi2 = reinterpret_cast<const unsigned*>(bias)[tn * (BN / 2) + l2];
            const float sx = __uint_as_float((unsigned)reinterpret_cast<const unsigned short*>(scale_x)[0] << 16);
            if (lane < 56) {
                const int q = lane >> 1;
                const int idx = (q < 24 ? (2 * (q >> 3) + (q & 1)) * 16 + ((q & 7) >> 1) * 4 : 96 + (q - 24) * 4) + (lane & 1) * 2;
                const f32x2 sc = {sx * __uint_as_float(sw2 << 16), sx * __uint_as_float(sw2 & 0xffff0000u)};
                const f32x2 bi = {__uint_as_float(bi2 << 16), __uint_as_float(bi2 & 0xffff0000u)};
                *(LAS f32x2*)(uintptr_t)(smem_base + OFF_SCALE + idx * 4) = sc;
                *(LAS f32x2*)(uintptr_t)(smem_base + OFF_BIAS + idx * 4) = bi;
            }
            tag = ep + 0x9E3779B97F4A7C15ull;
            asm volatile("s_waitcnt lgkmcnt(0)" ::: "memory");
            if (lane == 0) {
                const u32x4 t = {(unsigned)tag, (unsigned)(tag >> 32), 1u, 0u};
                *(volatile LAS u32x4*)(uintptr_t)(ctl + 16) = t;
            }
        } else {
            tag = get_tag();
        }
        TS(1);
        const int nzx = -zero_x[0];
        GAS u64* fl = lane < 32 ? flags + tm * 32 + lane : flags + 1024 + (tn >> 1) * 16 + (lane & 15);
        int cs[7] = {0, 0, 0, 0, 0, 0, 0};
        const unsigned pd0 = smem_base + h * KB_STRIDE + lane * 16;
#pragma unroll 1
        for (int c = 0; c < 4; ++c) {
            for (;;) {
                const u64 x = __hip_atomic_load(fl + c * 256, __ATOMIC_RELAXED, __HIP_MEMORY_SCOPE_AGENT);
                if (__all(x == tag)) break;
                __builtin_amdgcn_s_sleep(PSLEEP);
            }
            asm volatile("" ::: "memory");
            if (h == 0) *(volatile LAS unsigned*)(uintptr_t)(ctl + 8) = (unsigned)(c + 1);
            u32x4 bp[14];
            const int goff = tn * BIMG_BYTES + c * CH_STRIDE + h * KB_STRIDE;
#pragma unroll
            for (int x = 0; x < 14; ++x)
                bp[x] = __builtin_amdgcn_raw_buffer_load_b128(rsB8, lane * 16, goff + (x / 7) * (2 * KB_STRIDE) + (x % 7) * 1024, AUX_SC1);
            LAS uint8_t* pd = (LAS uint8_t*)(uintptr_t)(pd0 + c * CH_STRIDE);
#pragma unroll
            for (int kk = 0; kk < 2; ++kk) {
#pragma unroll
                for (int y = 0; y < 7; ++y) *reinterpret_cast<LAS u32x4*>(pd + kk * (2 * KB_STRIDE) + y * 1024) = bp[kk * 7 + y];
                asm volatile("s_waitcnt lgkmcnt(0)" ::: "memory");
                *(volatile LAS unsigned*)(uintptr_t)(ctl + h * 4) = (unsigned)(2 * c + kk + 1);
                __builtin_amdgcn_sched_barrier(0);
            }
#pragma unroll
            for (int x = 0; x < 14; ++x)
#pragma unroll
                for (int d = 0; d < 4; ++d) cs[(4 * x) % 7] = __builtin_amdgcn_sdot4((int)bp[x][d], 0x01010101, cs[(4 * x) % 7], false);
        }
#pragma unroll
        for (int i = 0; i < 7; ++i) {
            int cb = i + (lane >> 4);
            cb = cb >= 7 ? cb - 7 : cb;
            __hip_atomic_fetch_add((LAS int*)(uintptr_t)(smem_base + OFF_CORR + (cb * 16 + (lane & 15)) * 4), nzx * cs[i],
                                   __ATOMIC_RELAXED, __HIP_MEMORY_SCOPE_WORKGROUP);
        }
        asm volatile("s_waitcnt lgkmcnt(0)" ::: "memory");
        *(volatile LAS unsigned*)(uintptr_t)(ctl + h * 4) = 9u;
        TS(8);
    } else {
        const int c = wid < 4 ? wid >> 1 : wid - 4;
        const bool hasB = wid >= 4 || (wid & 1) == 0, hasA = wid >= 4 || (wid & 1) == 1;
        if (c == 1) __builtin_amdgcn_s_sleep(STAG);
        if (c >= 2) {
            for (;;) {
                const u32x4 v = lds_rd128(ctl + 32);
                if (__builtin_amdgcn_readfirstlane(c == 2 ? v[0] : v[1]) != 0) break;
                __builtin_amdgcn_s_sleep(1);
            }
        }
        const int kg = bid >> 4, tn2 = bid & 15;
        const int kb = 4 * c + (kg >> 2), g = kg & 3;
        const int q = lane < 56 ? lane : lane - 8;
        const int rb = (bid >> 5) * 16 + ((bid & 31) >> 1);
        const int kba = 4 * c + 2 * (bid & 1);
        i32x4 st[16], v[8];
        if (hasB) {
            const __amdgpu_buffer_rsrc_t rsW = __builtin_amdgcn_make_buffer_rsrc((void*)qw, 0, K_ * N_ * 4, RSRC_W3);
            const unsigned voff = (unsigned)((tn2 * 224 + q * 4) * 4);
            const int soff = (kb * 64 + g * 4) * (N_ * 4);
#pragma unroll
            for (int x = 0; x < 16; ++x)
                st[x] = __builtin_bit_cast(i32x4, __builtin_amdgcn_raw_buffer_load_b128(rsW, voff, soff + ((x >> 2) * 16 + (x & 3)) * (N_ * 4), RAW_AUX));
        }
        if (hasA) {
            const __amdgpu_buffer_rsrc_t rsX = __builtin_amdgcn_make_buffer_rsrc((void*)qx, 0, M_ * K_ * 4, RSRC_W3);
            const unsigned voff = (unsigned)(((rb * 16 + (lane & 15)) * K_ + kba * 64 + (lane >> 4) * 4) * 4);
#pragma unroll
            for (int x = 0; x < 8; ++x)
                v[x] = __builtin_bit_cast(i32x4, __builtin_amdgcn_raw_buffer_load_b128(rsX, voff + x * 64, 0, RAW_AUX));
        }
        __builtin_amdgcn_sched_barrier(0);
        if (wid < 2) __builtin_amdgcn_s_barrier();
        TS(1);
        if (TOKPOS == 0 && (wid == 0 || wid == 2)) {
            asm volatile("s_waitcnt vmcnt(%0)" ::"n"(TOKN) : "memory");
            *(volatile LAS unsigned*)(uintptr_t)(ctl + 32 + c * 4) = 1u;
        }
        __builtin_amdgcn_sched_barrier(0);
        if (hasB) {
            const int th = q >= 28 ? 1 : 0;
            const int qq = q - 28 * th;
            const int cb = qq < 24 ? 2 * (qq >> 3) + (qq & 1) : 6;
            const int sl = qq < 24 ? ((qq & 7) >> 1) * 4 : (qq - 24) * 4;
            const unsigned pbase = smem_base + OFF_SCR + c * 3584;
            LAS uint8_t* dst = (LAS uint8_t*)(uintptr_t)(pbase + th * G_STRIDE + cb * 256 + sl * 16);
#pragma unroll
            for (int cc = 0; cc < 4; ++cc) {
                u32x4 pk;
#pragma unroll
                for (int i = 0; i < 4; ++i) pk[i] = pack4(st[4 * i][cc], st[4 * i + 1][cc], st[4 * i + 2][cc], st[4 * i + 3][cc]);
                *reinterpret_cast<LAS u32x4*>(dst + cc * 16) = pk;
            }
            u32x4 lin[4];
#pragma unroll
            for (int x = 0; x < 3; ++x) lin[x] = *reinterpret_cast<const LAS u32x4*>((const LAS uint8_t*)(uintptr_t)(pbase + lane * 16) + x * 1024);
            lin[3] = *reinterpret_cast<const LAS u32x4*>((const LAS uint8_t*)(uintptr_t)(pbase + (lane & 31) * 16) + 3072);
            const unsigned vo = (unsigned)((2 * tn2 * 64 + kb * 4 + g) * G_STRIDE + lane * 16);
            constexpr int JUMP = 64 * G_STRIDE - G_STRIDE;
            __builtin_amdgcn_raw_buffer_store_b128(lin[0], rsB8, vo, 0, AUX_SC1);
            __builtin_amdgcn_raw_buffer_store_b128(lin[1], rsB8, vo + 1024 + (lane >= 48 ? JUMP : 0), 0, AUX_SC1);
            __builtin_amdgcn_raw_buffer_store_b128(lin[2], rsB8, vo + 2048 + JUMP, 0, AUX_SC1);
            if (lane < 32) __builtin_amdgcn_raw_buffer_store_b128(lin[3], rsB8, vo + 3072 + JUMP, 0, AUX_SC1);
        }
        if (hasA) {
            const unsigned vo = (unsigned)((rb * 16 + kba) * 1024 + lane * 16);
#pragma unroll
            for (int b = 0; b < 2; ++b) {
                u32x4 o;
#pragma unroll
                for (int i = 0; i < 4; ++i) o[i] = pack4(v[4 * b + i][0], v[4 * b + i][1], v[4 * b + i][2], v[4 * b + i][3]);
                __builtin_amdgcn_raw_buffer_store_b128(o, rsA8, vo + b * 1024, 0, AUX_SC1);
            }
        }
        TS(2);
        if (TOKPOS == 1 && (wid == 0 || wid == 2)) *(volatile LAS unsigned*)(uintptr_t)(ctl + 32 + c * 4) = 1u;
        tag = get_tag();
        asm volatile("s_waitcnt vmcnt(0)" ::: "memory");
        if (TOKPOS == 2 && (wid == 0 || wid == 2)) *(volatile LAS unsigned*)(uintptr_t)(ctl + 32 + c * 4) = 1u;
        if (lane == 0 && hasA) __hip_atomic_store(flags + c * 256 + bid, tag, __ATOMIC_RELAXED, __HIP_MEMORY_SCOPE_AGENT);
        if (lane == 0 && hasB) __hip_atomic_store(flags + 1024 + c * 256 + tn2 * 16 + kg, tag, __ATOMIC_RELAXED, __HIP_MEMORY_SCOPE_AGENT);
        TS(3);
    }

    if (wid < 4) {
        i32x4 af[4][4];
        i32x4 acc[4][7];
        i32x4 bfr[7];
#pragma unroll
        for (int i = 0; i < 4; ++i)
#pragma unroll
            for (int j = 0; j < 7; ++j) {
                acc[i][j] = (i32x4){0, 0, 0, 0};
                asm volatile("" : "+v"(acc[i][j]));
            }
        unsigned fB = smem_base + (lane >> 4) * G_STRIDE + (lane & 15) * 16;
        const int arb = (tm * 16 + 4 * wid) * 16;
        unsigned known = 0, aknown = 0;
        auto poll = [&]() {
            const u32x4 v = lds_rd128(ctl);
            const unsigned p0 = __builtin_amdgcn_readfirstlane(v[0]), p1 = __builtin_amdgcn_readfirstlane(v[1]);
            aknown = __builtin_amdgcn_readfirstlane(v[2]);
            known = p0 <= p1 ? 2 * p0 : 2 * p1 + 1;
        };
#define NEED(VAR, X)                                                                                           \
    do {                                                                                                       \
        if (VAR <= (unsigned)(X)) {                                                                            \
            for (;;) {                                                                                         \
                poll();                                                                                        \
                if (VAR > (unsigned)(X)) break;                                                                \
                __builtin_amdgcn_s_sleep(1);                                                                   \
            }                                                                                                  \
        }                                                                                                      \
    } while (0)
#define ISSUE_A(KK, KB)                                                                                        \
    do {                                                                                                       \
        _Pragma("unroll") for (int i = 0; i < 4; ++i)                                                          \
            af[KK][i] = __builtin_bit_cast(i32x4, __builtin_amdgcn_raw_buffer_load_b128(                       \
                rsA8, lane * 16, (arb + i * 16 + (KB)) * 1024, AUX_SC1));                                      \
    } while (0)
#define KSTEP(KK, NEXT)                                                                                        \
    do {                                                                                                       \
        _Pragma("unroll") for (int j = 0; j < 7; ++j) {                                                        \
            if (NEXT) asm volatile("s_waitcnt lgkmcnt(6)" ::: "memory");                                       \
            else asm volatile("s_waitcnt lgkmcnt(%0)" ::"n"(6 - j) : "memory");                                \
            MF(0, j, KK); MF(1, j, KK); MF(2, j, KK); MF(3, j, KK);                                            \
            if (NEXT) DSR(bfr[j], fB, ((KK) + 1) * KB_STRIDE + j * 256);                                       \
        }                                                                                                      \
    } while (0)
        NEED(aknown, 0);
        TS(4);
#pragma unroll
        for (int kk = 0; kk < 4; ++kk) ISSUE_A(kk, kk);
        __builtin_amdgcn_sched_barrier(0);
        NEED(known, 0);
        TS(5);
#pragma unroll
        for (int j = 0; j < 7; ++j) DSR(bfr[j], fB, j * 256);
#pragma unroll 1
        for (int c = 0; c < 3; ++c) {
#pragma unroll
            for (int kk = 0; kk < 4; ++kk) {
                NEED(known, 4 * c + kk + 1);
                __builtin_amdgcn_sched_barrier(0);
                KSTEP(kk, true);
                __builtin_amdgcn_sched_barrier(0);
                if (kk == 0) NEED(aknown, c + 1);
                ISSUE_A(kk, 4 * c + 4 + kk);
                __builtin_amdgcn_sched_barrier(0);
            }
            fB += CH_STRIDE;
        }
#pragma unroll
        for (int kk = 0; kk < 4; ++kk) {
            if (kk < 3) NEED(known, 12 + kk + 1);
            __builtin_amdgcn_sched_barrier(0);
            KSTEP(kk, kk < 3);
            __builtin_amdgcn_sched_barrier(0);
        }
        NEED(known, 17);
        TS(9);

        const int kq = lane >> 4;
        const LAS uint8_t* sm = (const LAS uint8_t*)(uintptr_t)smem_base;
        asm volatile("s_nop 7\n\ts_nop 7" ::: "memory");
#pragma unroll
        for (int j = 0; j < 7; ++j) asm volatile("" : "+v"(acc[0][j]), "+v"(acc[1][j]), "+v"(acc[2][j]), "+v"(acc[3][j]));
        const __amdgpu_buffer_rsrc_t rsO = __builtin_amdgcn_make_buffer_rsrc((void*)out, 0, M_ * N_ * 2, RSRC_W3);
        const unsigned ovo = (unsigned)(((tm * BM + wid * 64 + (lane & 15)) * N_ + tn * BN) * 2);
        i32x4 corr[7];
        f32x4 sc[7], bi[7];
#pragma unroll
        for (int j = 0; j < 7; ++j) {
            const int idx = j * 16 + kq * 4;
            corr[j] = *reinterpret_cast<const LAS i32x4*>(sm + OFF_CORR + idx * 4);
            sc[j] = *reinterpret_cast<const LAS f32x4*>(sm + OFF_SCALE + idx * 4);
            bi[j] = *reinterpret_cast<const LAS f32x4*>(sm + OFF_BIAS + idx * 4);
        }
#pragma unroll
        for (int p = 0; p < 4; ++p) {
            const int nh = p < 3 ? 2 : 1;
            const int c0 = p < 3 ? p * 32 + kq * 8 : 96 + kq * 4;
#pragma unroll
            for (int i = 0; i < 4; ++i) {
                unsigned o[4];
#pragma unroll
                for (int h = 0; h < nh; ++h) {
                    const int j = 2 * p + h;
                    const i32x4 a = acc[i][j] + corr[j];
                    f32x2 v0 = {(float)a[0] * sc[j][0] + bi[j][0], (float)a[1] * sc[j][1] + bi[j][1]};
                    f32x2 v1 = {(float)a[2] * sc[j][2] + bi[j][2], (float)a[3] * sc[j][3] + bi[j][3]};
                    o[2 * h] = __builtin_bit_cast(unsigned, __builtin_convertvector(v0, bf16x2_t));
                    o[2 * h + 1] = __builtin_bit_cast(unsigned, __builtin_convertvector(v1, bf16x2_t));
                }
                if (p < 3) {
                    const u32x4 v = {o[0], o[1], o[2], o[3]};
                    __builtin_amdgcn_raw_buffer_store_b128(v, rsO, ovo + c0 * 2 + i * 16 * (N_ * 2), 0, OUT_AUX);
                } else {
                    const u32x2 v = {o[0], o[1]};
                    __builtin_amdgcn_raw_buffer_store_b64(v, rsO, ovo + c0 * 2 + i * 16 * (N_ * 2), 0, OUT_AUX);
                }
            }
        }
        TS(10);
    }
    if (bid == 0 && wid == 7) {
        for (;;) {
            bool ok = true;
#pragma unroll
            for (int i = 0; i < 4; ++i)
                ok &= __hip_atomic_load(flags + 768 + i * 64 + lane, __ATOMIC_RELAXED, __HIP_MEMORY_SCOPE_AGENT) == tag;
            if (__all(ok)) break;
            __builtin_amdgcn_s_sleep(8);
        }
        if (lane == 0) __hip_atomic_store((GAS u64*)(ws + WS_EPOCH), tag, __ATOMIC_RELAXED, __HIP_MEMORY_SCOPE_AGENT);
    }
}

extern "C" void kernel_launch(void* const* d_in, const int* in_sizes, int n_in,
                              void* d_out, int out_size, void* d_ws, size_t ws_size,
                              hipStream_t stream) {
    const int* qx = (const int*)d_in[0];
    const int* qw = (const int*)d_in[1];
    const __hip_bfloat16* scale_x = (const __hip_bfloat16*)d_in[2];
    const int* zero_x = (const int*)d_in[3];
    const __hip_bfloat16* scale_w = (const __hip_bfloat16*)d_in[4];
    const __hip_bfloat16* bias = (const __hip_bfloat16*)d_in[5];
    (void)hipFuncSetAttribute((const void*)fused_kernel, hipFuncAttributeMaxDynamicSharedMemorySize, SMEM_BYTES);
    fused_kernel<<<256, 512, SMEM_BYTES, stream>>>(qx, qw, scale_x, zero_x, scale_w, bias, (__hip_bfloat16*)d_out,
                                                   (uint8_t*)d_ws);
}
```

```cpp
#include <hip/hip_runtime.h>
#include <hip/hip_bf16.h>
#include <stdint.h>

typedef int i32x4 __attribute__((ext_vector_type(4)));
typedef int i32x2 __attribute__((ext_vector_type(2)));
typedef unsigned int u32x4 __attribute__((ext_vector_type(4)));
typedef unsigned int u32x2 __attribute__((ext_vector_type(2)));
typedef float f32x2 __attribute__((ext_vector_type(2)));
typedef float f32x4 __attribute__((ext_vector_type(4)));
typedef short s16x2 __attribute__((ext_vector_type(2)));
typedef __bf16 bf16x2_t __attribute__((ext_vector_type(2)));
typedef unsigned long long u64;
#define GAS __attribute__((address_space(1)))
#define LAS __attribute__((address_space(3)))

constexpr int M_ = 2048, K_ = 1024, N_ = 3584;
constexpr int BM = 128, BN = 224;
constexpr int NSTAGE = 6;
constexpr int UNIT = 3584;
constexpr int A_STAGE = 8192, B_STAGE = 4 * UNIT;
constexpr int STAGE_BYTES = A_STAGE + B_STAGE;
constexpr int RING_BYTES = NSTAGE * STAGE_BYTES;
constexpr int ACC_PITCH = BN * 4 + 16;
constexpr int OFF_CTL = RING_BYTES;
constexpr int CT_FULL = 0, CT_FREE = 16, CT_TAG = 32, CT_ARR = 48, CT_GO = 56, CT_PH2 = 60;
constexpr int OFF_SCALE = OFF_CTL + 64;
constexpr int OFF_BIAS = OFF_SCALE + 896;
constexpr int OFF_CORR = OFF_BIAS + 896;
constexpr int SMEM_BYTES = OFF_CORR + 896;
static_assert(BM * ACC_PITCH <= RING_BYTES, "acc staging must fit in the ring");
constexpr int K1 = 12;
constexpr size_t WS_EPOCH = 0;
constexpr size_t WS_FLAGS = 4096;
constexpr size_t WS_A8 = 65536;
constexpr size_t WS_B8 = WS_A8 + (size_t)M_ * K_;
constexpr size_t WS_CP = WS_B8 + (size_t)N_ * K_;
constexpr size_t WS_DBG = WS_CP + 16 * 64 * 512;
constexpr int AUX_SC1 = 16;
constexpr int RSRC_W3 = 0x00020000;
#define PSLEEP 4
#define OUT_AUX 16
#define RAW_AUX 0
#define GO_KB 2

__device__ __forceinline__ unsigned pack4(int a, int b, int c, int d) {
    const unsigned lo = __builtin_amdgcn_perm((unsigned)b, (unsigned)a, 0x0c0c0400u);
    const unsigned hi = __builtin_amdgcn_perm((unsigned)d, (unsigned)c, 0x0c0c0400u);
    return __builtin_amdgcn_perm(hi, lo, 0x05040100u);
}
__device__ __forceinline__ u32x4 lds_rd128(unsigned addr) {
    u32x4 v;
    asm volatile("ds_read_b128 %0, %1\n\ts_waitcnt lgkmcnt(0)" : "=v"(v) : "v"(addr) : "memory");
    return v;
}
__device__ __forceinline__ void lds_wr32(unsigned addr, unsigned val) {
    asm volatile("ds_write_b32 %0, %1" ::"v"(addr), "v"(val) : "memory");
}

#define SPIN for (;;)
#define TS(i) do { } while (0)

__global__ __launch_bounds__(512, 2) void fused_kernel(const int* __restrict__ qx, const int* __restrict__ qw,
                                                       const __hip_bfloat16* __restrict__ scale_x,
                                                       const int* __restrict__ zero_x,
                                                       const __hip_bfloat16* __restrict__ scale_w,
                                                       const __hip_bfloat16* __restrict__ bias,
                                                       __hip_bfloat16* __restrict__ out, uint8_t* __restrict__ ws) {
    extern __shared__ __attribute__((aligned(16))) uint8_t smem[];
    const int tid = threadIdx.x, lane = tid & 63;
    const int wid = __builtin_amdgcn_readfirstlane(tid >> 6);
    const int bid = blockIdx.x;
    const int tm = (bid & 1) * 8 + ((bid >> 3) & 7);
    const int tn = ((bid >> 1) & 3) * 4 + (bid >> 6);
    const int px = bid >> 4, py = bid & 15;
    const unsigned smem_base = (unsigned)(uintptr_t)smem;
    const unsigned ctl = smem_base + OFF_CTL;
    GAS u64* flags = (GAS u64*)(ws + WS_FLAGS);
    u64 tag = 0;
    auto get_tag = [&]() -> u64 {
        SPIN {
            const u32x4 v = lds_rd128(ctl + CT_TAG);
            if (__builtin_amdgcn_readfirstlane(v[2]) != 0) return ((u64)v[1] << 32) | v[0];
            __builtin_amdgcn_s_sleep(1);
        }
        return 0;
    };
    auto task_B = [&](int u, bool bar) {
        const __amdgpu_buffer_rsrc_t rsW = __builtin_amdgcn_make_buffer_rsrc((void*)qw, 0, K_ * N_ * 4, RSRC_W3);
        const __amdgpu_buffer_rsrc_t rsB8 = __builtin_amdgcn_make_buffer_rsrc((void*)(ws + WS_B8), 0, N_ * K_ + 16 * 64 * 512, RSRC_W3);
        const int q = lane < 56 ? lane : lane - 8;
        const unsigned voff = (unsigned)((py * 224 + q * 4) * 4);
        const int soff = ((u >> 2) * 64 + (u & 3) * 4) * (N_ * 4);
        i32x4 st[16];
#pragma unroll
        for (int x = 0; x < 16; ++x)
            st[x] = __builtin_bit_cast(i32x4, __builtin_amdgcn_raw_buffer_load_b128(rsW, voff, soff + ((x >> 2) * 16 + (x & 3)) * (N_ * 4), RAW_AUX));
        __builtin_amdgcn_sched_barrier(0);
        if (bar) __builtin_amdgcn_s_barrier();
        TS(1);
        __builtin_amdgcn_sched_barrier(0);
        const unsigned vo = (unsigned)((py * 64 + u) * UNIT + lane * 16);
        int cs[4];
#pragma unroll
        for (int c = 0; c < 4; ++c) {
            u32x4 pk;
            int s = 0;
#pragma unroll
            for (int i = 0; i < 4; ++i) {
                pk[i] = pack4(st[4 * i][c], st[4 * i + 1][c], st[4 * i + 2][c], st[4 * i + 3][c]);
                s = __builtin_amdgcn_sdot4((int)pk[i], 0x01010101, s, false);
            }
            cs[c] = s;
            if (lane < 56) __builtin_amdgcn_raw_buffer_store_b128(pk, rsB8, vo + c * 896, 0, AUX_SC1);
        }
        const u32x2 cp = {__builtin_amdgcn_perm((unsigned)cs[1], (unsigned)cs[0], 0x05040100u),
                          __builtin_amdgcn_perm((unsigned)cs[3], (unsigned)cs[2], 0x05040100u)};
        __builtin_amdgcn_raw_buffer_store_b64(cp, rsB8, (unsigned)(N_ * K_ + (py * 64 + u) * 512 + lane * 8), 0, AUX_SC1);
    };
    auto task_A = [&](int kb, bool bar) {
        const __amdgpu_buffer_rsrc_t rsX = __builtin_amdgcn_make_buffer_rsrc((void*)qx, 0, M_ * K_ * 4, RSRC_W3);
        const __amdgpu_buffer_rsrc_t rsA8 = __builtin_amdgcn_make_buffer_rsrc((void*)(ws + WS_A8), 0, M_ * K_, RSRC_W3);
        const int rb = px * 8 + (py >> 1);
        const unsigned voff = (unsigned)(((rb * 16 + (lane & 15)) * K_ + kb * 64 + (lane >> 4) * 4) * 4);
        i32x4 v[8];
#pragma unroll
        for (int x = 0; x < 8; ++x)
            v[x] = __builtin_bit_cast(i32x4, __builtin_amdgcn_raw_buffer_load_b128(rsX, voff + x * 64, 0, RAW_AUX));
        __builtin_amdgcn_sched_barrier(0);
        if (bar) __builtin_amdgcn_s_barrier();
        TS(1);
        __builtin_amdgcn_sched_barrier(0);
        const unsigned vo = (unsigned)((rb * 16 + kb) * 1024 + lane * 16);
#pragma unroll
        for (int b = 0; b < 2; ++b) {
            u32x4 o;
#pragma unroll
            for (int i = 0; i < 4; ++i) o[i] = pack4(v[4 * b + i][0], v[4 * b + i][1], v[4 * b + i][2], v[4 * b + i][3]);
            __builtin_amdgcn_raw_buffer_store_b128(o, rsA8, vo + b * 1024, 0, AUX_SC1);
        }
    };
    auto publish = [&](int ph, unsigned ntask) {
        asm volatile("s_waitcnt vmcnt(0)" ::: "memory");
        unsigned old = 0;
        if (lane == 0)
            old = __hip_atomic_fetch_add((LAS unsigned*)(uintptr_t)(ctl + CT_ARR + ph * 4), 1u, __ATOMIC_RELAXED, __HIP_MEMORY_SCOPE_WORKGROUP);
        if ((unsigned)__builtin_amdgcn_readfirstlane(old) == ntask - 1 && lane < 2)
            __hip_atomic_store(flags + ph * 256 + (lane == 0 ? px * 16 + py : 512 + py * 16 + px), tag, __ATOMIC_RELAXED,
                               __HIP_MEMORY_SCOPE_AGENT);
    };

    if (wid < 6) {
        if (wid < 3) task_B(px + 16 * wid, true);
        else task_A((py & 1) * (K1 / 2) + 2 * (wid - 3), true);
        TS(2);
        tag = get_tag();
        publish(0, 6);
        TS(3);
    } else {
        if (wid == 6) {
            if (lane < 16) lds_wr32(ctl + lane * 4, 0u);
            asm volatile("s_waitcnt lgkmcnt(0)" ::: "memory");
        }
        __builtin_amdgcn_s_barrier();
    }

    if (wid >= 6) {
        const int lw = wid - 6;
        if (lw == 0) {
            const u64 ep = __hip_atomic_load((GAS u64*)(ws + WS_EPOCH), __ATOMIC_RELAXED, __HIP_MEMORY_SCOPE_AGENT);
            tag = ep + 0x9E3779B97F4A7C15ull;
            if (lane == 0) {
                const u32x4 t = {(unsigned)tag, (unsigned)(tag >> 32), 1u, 0u};
                *(volatile LAS u32x4*)(uintptr_t)(ctl + CT_TAG) = t;
            }
        } else {
            const int l4 = lane < 56 ? lane : 0;
            const u32x2 sw4 = reinterpret_cast<const u32x2*>(scale_w)[tn * (BN / 4) + l4];
            const u32x2 bi4 = reinterpret_cast<const u32x2*>(bias)[tn * (BN / 4) + l4];
            const float sx = __uint_as_float((unsigned)reinterpret_cast<const unsigned short*>(scale_x)[0] << 16);
            if (lane < 56) {
                const f32x4 sc = {sx * __uint_as_float(sw4[0] << 16), sx * __uint_as_float(sw4[0] & 0xffff0000u),
                                  sx * __uint_as_float(sw4[1] << 16), sx * __uint_as_float(sw4[1] & 0xffff0000u)};
                const f32x4 bi = {__uint_as_float(bi4[0] << 16), __uint_as_float(bi4[0] & 0xffff0000u),
                                  __uint_as_float(bi4[1] << 16), __uint_as_float(bi4[1] & 0xffff0000u)};
                *(LAS f32x4*)(uintptr_t)(smem_base + OFF_SCALE + lane * 16) = sc;
                *(LAS f32x4*)(uintptr_t)(smem_base + OFF_BIAS + lane * 16) = bi;
                *(LAS i32x4*)(uintptr_t)(smem_base + OFF_CORR + lane * 16) = (i32x4){0, 0, 0, 0};
            }
            tag = get_tag();
        }
        TS(1);
        GAS u64* fl = flags + ((lane & 16) ? 512 + tn * 16 : tm * 16) + (lane & 15);
        const uint8_t* gA = ws + WS_A8 + (size_t)(tm * 8) * 16384 + lane * 16;
        const uint8_t* gB = ws + WS_B8 + (size_t)tn * (64 * UNIT) + lane * 16;
        const unsigned full = ctl + CT_FULL + lw * 4;
#pragma unroll
        for (int kb = 0; kb < 16; ++kb) {
            if (kb == K1) {
                asm volatile("s_waitcnt vmcnt(11)" ::: "memory");
                lds_wr32(full, K1 - 1);
                asm volatile("s_waitcnt vmcnt(0)" ::: "memory");
                lds_wr32(full, K1);
            }
            if (kb == 0 || kb == K1) {
                SPIN {
                    const u64 x = __hip_atomic_load(fl + (kb ? 256 : 0), __ATOMIC_RELAXED, __HIP_MEMORY_SCOPE_AGENT);
                    if (__all(x == tag)) break;
                    __builtin_amdgcn_s_sleep(PSLEEP);
                }
                asm volatile("" ::: "memory");
                if (kb == K1 && lw == 0) lds_wr32(ctl + CT_PH2, 1u);
                if (kb == 0) TS(4); else TS(6);
            }
            if (kb >= NSTAGE) {
                SPIN {
                    const u32x4 v = lds_rd128(ctl + CT_FREE);
                    const unsigned m = min(min(v[0], v[1]), min(v[2], v[3]));
                    if ((unsigned)__builtin_amdgcn_readfirstlane(m) > (unsigned)(kb - NSTAGE)) break;
                    __builtin_amdgcn_s_sleep(1);
                }
            }
            uint8_t* base = smem + (kb % NSTAGE) * STAGE_BYTES;
#pragma unroll
            for (int i = 0; i < 11; ++i) {
                const int blk = lw + 2 * i;
                const uint8_t* g = i < 4 ? gA + (size_t)blk * 16384 + kb * 1024 : gB + kb * B_STAGE + (blk - 8) * 1024;
                __builtin_amdgcn_global_load_lds((const unsigned*)g, (unsigned*)(base + blk * 1024), 16, 0, AUX_SC1);
            }
            if (kb == GO_KB && lw == 0) lds_wr32(ctl + CT_GO, 1u);
            if (kb >= 2 && kb != K1 && kb != K1 + 1) {
                asm volatile("s_waitcnt vmcnt(22)" ::: "memory");
                lds_wr32(full, kb - 1);
            }
            if (kb == 2) TS(5);
        }
        asm volatile("s_waitcnt vmcnt(11)" ::: "memory");
        lds_wr32(full, 15);
        asm volatile("s_waitcnt vmcnt(0)" ::: "memory");
        lds_wr32(full, 16);
        TS(7);
        asm volatile("s_waitcnt lgkmcnt(0)" ::: "memory");
        __builtin_amdgcn_s_barrier();
    } else if (wid >= 4) {
        SPIN {
            const u32x4 v = lds_rd128(ctl + CT_ARR);
            if (__builtin_amdgcn_readfirstlane(v[2]) != 0) break;
            __builtin_amdgcn_s_sleep(2);
        }
        TS(4);
        if (wid == 4) task_B(4 * K1 + px, false);
        else task_A(K1 + 2 * (py & 1), false);
        TS(5);
        publish(1, 2);
        TS(6);
        SPIN {
            const u32x4 v = lds_rd128(ctl + CT_ARR);
            if (__builtin_amdgcn_readfirstlane(v[3]) != 0) break;
            __builtin_amdgcn_s_sleep(2);
        }
        {
            const __amdgpu_buffer_rsrc_t rsC = __builtin_amdgcn_make_buffer_rsrc((void*)(ws + WS_CP), 0, 16 * 64 * 512, RSRC_W3);
            u32x4 cp[16];
#pragma unroll
            for (int x = 0; x < 16; ++x)
                cp[x] = __builtin_amdgcn_raw_buffer_load_b128(rsC, lane * 16, tn * 32768 + (wid - 4) * 16384 + x * 1024, AUX_SC1);
            const int nzx = -zero_x[0];
            int s[8] = {0, 0, 0, 0, 0, 0, 0, 0};
#pragma unroll
            for (int hh = 0; hh < 2; ++hh) {
#pragma unroll
                for (int d = 0; d < 4; ++d) {
                    s16x2 a = {0, 0};
#pragma unroll
                    for (int x = 0; x < 8; ++x) {
                        const unsigned w_ = cp[8 * hh + x][d];
                        a += __builtin_bit_cast(s16x2, w_);
                    }
                    s[2 * d] += a[0];
                    s[2 * d + 1] += a[1];
                }
            }
            if ((lane & 31) < 28) {
#pragma unroll
                for (int e = 0; e < 8; ++e)
                    __hip_atomic_fetch_add((LAS int*)(uintptr_t)(smem_base + OFF_CORR + ((lane & 31) * 8 + e) * 4), nzx * s[e],
                                           __ATOMIC_RELAXED, __HIP_MEMORY_SCOPE_WORKGROUP);
            }
        }
        TS(7);
        if (bid == 0 && wid == 5) {
            SPIN {
                bool ok = true;
#pragma unroll
                for (int i = 0; i < 4; ++i)
                    ok &= __hip_atomic_load(flags + 256 + i * 64 + lane, __ATOMIC_RELAXED, __HIP_MEMORY_SCOPE_AGENT) == tag;
                if (__all(ok)) break;
                __builtin_amdgcn_s_sleep(8);
            }
            if (lane == 0) __hip_atomic_store((GAS u64*)(ws + WS_EPOCH), tag, __ATOMIC_RELAXED, __HIP_MEMORY_SCOPE_AGENT);
        }
        asm volatile("s_waitcnt lgkmcnt(0)" ::: "memory");
        __builtin_amdgcn_s_barrier();
    } else {
        const int wm = wid >> 1, wn = wid & 1;
        i32x4 acc[4][7];
        i32x4 af[2][4], bfr[2][7];
#pragma unroll
        for (int i = 0; i < 4; ++i)
#pragma unroll
            for (int j = 0; j < 7; ++j) {
                acc[i][j] = (i32x4){0, 0, 0, 0};
                asm volatile("" : "+v"(acc[i][j]));
            }
        const unsigned fA = smem_base + wm * 4096 + lane * 16;
        const unsigned fB = smem_base + A_STAGE + (lane >> 4) * UNIT + wn * 1792 + (lane & 15) * 16;
        const unsigned freew = ctl + CT_FREE + wid * 4;
        unsigned known = 0;
#define DSR(DST, ADDR, OFF) asm volatile("ds_read_b128 %0, %1 offset:%2" : "=v"(DST) : "v"(ADDR), "n"(OFF) : "memory")
#define MF(I, J, SET) asm volatile("v_mfma_i32_16x16x64_i8 %0, %1, %2, %0" : "+v"(acc[I][J]) : "v"(bfr[SET][J]), "v"(af[SET][I]))
#define NEED(X)                                                                                                \
    do {                                                                                                       \
        if (known <= (unsigned)(X)) {                                                                          \
            SPIN {                                                                                         \
                const u32x4 v_ = lds_rd128(ctl + CT_FULL);                                                     \
                known = __builtin_amdgcn_readfirstlane(min(v_[0], v_[1]));                                     \
                if (known > (unsigned)(X)) break;                                                              \
                __builtin_amdgcn_s_sleep(1);                                                                   \
            }                                                                                                  \
        }                                                                                                      \
    } while (0)
#define STEP(SET, KB)                                                                        \
    do {                                                                                     \
        asm volatile("s_waitcnt lgkmcnt(0)" ::: "memory");                                   \
        if ((KB) + 1 < 16) {                                                                 \
            if ((KB) + NSTAGE < 16) lds_wr32(freew, (KB) + 1);                               \
            NEED((KB) + 1);                                                                  \
            __builtin_amdgcn_sched_barrier(0);                                               \
            const unsigned aA_ = fA + (((KB) + 1) % NSTAGE) * STAGE_BYTES;                   \
            const unsigned aB_ = fB + (((KB) + 1) % NSTAGE) * STAGE_BYTES;                   \
            MF(0, 0, SET);                                                                   \
            DSR(af[(SET) ^ 1][0], aA_, 0);        MF(1, 0, SET);                             \
            DSR(bfr[(SET) ^ 1][0], aB_, 0);       MF(2, 0, SET);                             \
            DSR(af[(SET) ^ 1][1], aA_, 1024);     MF(3, 0, SET);                             \
            DSR(af[(SET) ^ 1][2], aA_, 2048);     MF(0, 1, SET);                             \
            DSR(af[(SET) ^ 1][3], aA_, 3072);     MF(1, 1, SET);                             \
            DSR(bfr[(SET) ^ 1][1], aB_, 256);     MF(2, 1, SET);                             \
            DSR(bfr[(SET) ^ 1][2], aB_, 512);     MF(3, 1, SET);                             \
            DSR(bfr[(SET) ^ 1][3], aB_, 768);     MF(0, 2, SET);                             \
            DSR(bfr[(SET) ^ 1][4], aB_, 1024);    MF(1, 2, SET);                             \
            DSR(bfr[(SET) ^ 1][5], aB_, 1280);    MF(2, 2, SET);                             \
            DSR(bfr[(SET) ^ 1][6], aB_, 1536);    MF(3, 2, SET);                             \
        } else {                                                                             \
            __builtin_amdgcn_sched_barrier(0);                                               \
            _Pragma("unroll") for (int j = 0; j < 3; ++j)                                    \
            _Pragma("unroll") for (int i = 0; i < 4; ++i) MF(i, j, SET);                     \
        }                                                                                    \
        _Pragma("unroll") for (int j = 3; j < 7; ++j)                                        \
        _Pragma("unroll") for (int i = 0; i < 4; ++i) MF(i, j, SET);                         \
    } while (0)
        NEED(0);
        TS(4);
#pragma unroll
        for (int j = 0; j < 7; ++j) DSR(bfr[0][j], fB, j * 256);
#pragma unroll
        for (int i = 0; i < 4; ++i) DSR(af[0][i], fA, i * 1024);
#pragma unroll
        for (int kb = 0; kb < 16; kb += 2) {
            STEP(0, kb);
            STEP(1, kb + 1);
            if (kb == 2) TS(5);
            if (kb == 6) TS(6);
            if (kb == 10) TS(7);
        }
        TS(8);
        __builtin_amdgcn_sched_barrier(0);
        __builtin_amdgcn_s_barrier();
        asm volatile("s_nop 7\n\ts_nop 7" ::: "memory");
#pragma unroll
        for (int j = 0; j < 7; ++j) asm volatile("" : "+v"(acc[0][j]), "+v"(acc[1][j]), "+v"(acc[2][j]), "+v"(acc[3][j]));
        LAS uint8_t* dst = (LAS uint8_t*)(uintptr_t)(smem_base + (wm * 64 + (lane & 15)) * ACC_PITCH + (wn * 112 + (lane >> 4) * 4) * 4);
#pragma unroll
        for (int j = 0; j < 7; ++j)
#pragma unroll
            for (int i = 0; i < 4; ++i) *reinterpret_cast<LAS i32x4*>(dst + i * 16 * ACC_PITCH + j * 64) = acc[i][j];
    }
    asm volatile("s_waitcnt lgkmcnt(0)" ::: "memory");
    __builtin_amdgcn_s_barrier();
    TS(9);
    if (tid < 448) {
        const int cc = tid % 28, r0 = tid / 28;
        const LAS uint8_t* sm = (const LAS uint8_t*)(uintptr_t)smem_base;
        const f32x4 s0 = *reinterpret_cast<const LAS f32x4*>(sm + OFF_SCALE + cc * 32), s1 = *reinterpret_cast<const LAS f32x4*>(sm + OFF_SCALE + cc * 32 + 16);
        const f32x4 b0 = *reinterpret_cast<const LAS f32x4*>(sm + OFF_BIAS + cc * 32), b1 = *reinterpret_cast<const LAS f32x4*>(sm + OFF_BIAS + cc * 32 + 16);
        const i32x4 c0 = *reinterpret_cast<const LAS i32x4*>(sm + OFF_CORR + cc * 32), c1 = *reinterpret_cast<const LAS i32x4*>(sm + OFF_CORR + cc * 32 + 16);
        const __amdgpu_buffer_rsrc_t rsO = __builtin_amdgcn_make_buffer_rsrc((void*)out, 0, M_ * N_ * 2, RSRC_W3);
        const unsigned ovo = (unsigned)(((tm * BM + r0) * N_ + tn * BN + cc * 8) * 2);
#pragma unroll
        for (int i = 0; i < 8; ++i) {
            const LAS uint8_t* src = sm + (r0 + 16 * i) * ACC_PITCH + cc * 8;
            i32x2 a[4];
#pragma unroll
            for (int c = 0; c < 4; ++c) a[c] = *reinterpret_cast<const LAS i32x2*>(src + c * 224);
            f32x2 v0 = {(float)(a[0][0] + c0[0]) * s0[0] + b0[0], (float)(a[1][0] + c0[1]) * s0[1] + b0[1]};
            f32x2 v1 = {(float)(a[2][0] + c0[2]) * s0[2] + b0[2], (float)(a[3][0] + c0[3]) * s0[3] + b0[3]};
            f32x2 v2 = {(float)(a[0][1] + c1[0]) * s1[0] + b1[0], (float)(a[1][1] + c1[1]) * s1[1] + b1[1]};
            f32x2 v3 = {(float)(a[2][1] + c1[2]) * s1[2] + b1[2], (float)(a[3][1] + c1[3]) * s1[3] + b1[3]};
            u32x4 o;
            o[0] = __builtin_bit_cast(unsigned, __builtin_convertvector(v0, bf16x2_t));
            o[1] = __builtin_bit_cast(unsigned, __builtin_convertvector(v1, bf16x2_t));
            o[2] = __builtin_bit_cast(unsigned, __builtin_convertvector(v2, bf16x2_t));
            o[3] = __builtin_bit_cast(unsigned, __builtin_convertvector(v3, bf16x2_t));
            __builtin_amdgcn_raw_buffer_store_b128(o, rsO, ovo + i * 16 * (N_ * 2), 0, OUT_AUX);
        }
    }
    TS(10);
}

extern "C" void kernel_launch(void* const* d_in, const int* in_sizes, int n_in,
                              void* d_out, int out_size, void* d_ws, size_t ws_size,
                              hipStream_t stream) {
    const int* qx = (const int*)d_in[0];
    const int* qw = (const int*)d_in[1];
    const __hip_bfloat16* scale_x = (const __hip_bfloat16*)d_in[2];
    const int* zero_x = (const int*)d_in[3];
    const __hip_bfloat16* scale_w = (const __hip_bfloat16*)d_in[4];
    const __hip_bfloat16* bias = (const __hip_bfloat16*)d_in[5];
    (void)hipFuncSetAttribute((const void*)fused_kernel, hipFuncAttributeMaxDynamicSharedMemorySize, SMEM_BYTES);
    fused_kernel<<<256, 512, SMEM_BYTES, stream>>>(qx, qw, scale_x, zero_x, scale_w, bias, (__hip_bfloat16*)d_out,
                                                   (uint8_t*)d_ws);
}
```

```cpp
#define LAG 1
#include <hip/hip_runtime.h>
#include <hip/hip_bf16.h>
#include <stdint.h>

typedef int i32x4 __attribute__((ext_vector_type(4)));
typedef int i32x2 __attribute__((ext_vector_type(2)));
typedef unsigned int u32x4 __attribute__((ext_vector_type(4)));
typedef unsigned int u32x2 __attribute__((ext_vector_type(2)));
typedef float f32x2 __attribute__((ext_vector_type(2)));
typedef float f32x4 __attribute__((ext_vector_type(4)));
typedef short s16x2 __attribute__((ext_vector_type(2)));
typedef __bf16 bf16x2_t __attribute__((ext_vector_type(2)));
typedef unsigned long long u64;
#define GAS __attribute__((address_space(1)))
#define LAS __attribute__((address_space(3)))

constexpr int M_ = 2048, K_ = 1024, N_ = 3584;
constexpr int BM = 128, BN = 224;
constexpr int NSTAGE = 6;
constexpr int UNIT = 3584;
constexpr int A_STAGE = 8192, B_STAGE = 4 * UNIT;
constexpr int STAGE_BYTES = A_STAGE + B_STAGE;
constexpr int RING_BYTES = NSTAGE * STAGE_BYTES;
constexpr int ACC_PITCH = BN * 4 + 16;
constexpr int OFF_CTL = RING_BYTES;
constexpr int CT_FULL = 0, CT_FREE = 16, CT_TAG = 32, CT_ARR = 48, CT_GO = 56, CT_PH2 = 60;
constexpr int OFF_SCALE = OFF_CTL + 64;
constexpr int OFF_BIAS = OFF_SCALE + 896;
constexpr int OFF_CORR = OFF_BIAS + 896;
constexpr int SMEM_BYTES = OFF_CORR + 896;
static_assert(BM * ACC_PITCH <= RING_BYTES, "acc staging must fit in the ring");
constexpr int NPH = 4;
constexpr size_t WS_EPOCH = 0;
#define FSTR 544
constexpr size_t WS_FLAGS = 8u << 20;
constexpr size_t WS_A8 = 65536;
constexpr size_t WS_B8 = WS_A8 + (size_t)M_ * K_;
constexpr size_t WS_CP = WS_B8 + (size_t)N_ * K_;
constexpr size_t WS_DBG = WS_CP + 16 * 64 * 512;
constexpr int AUX_SC1 = 16;
constexpr int RSRC_W3 = 0x00020000;
#define WB 4
#define WA 4
#define FLAGI 1
#define PSLEEP 4
#define OUT_AUX 16
#define RAW_AUX 0
#define GO_KB 2

__device__ __forceinline__ unsigned pack4(int a, int b, int c, int d) {
    const unsigned lo = __builtin_amdgcn_perm((unsigned)b, (unsigned)a, 0x0c0c0400u);
    const unsigned hi = __builtin_amdgcn_perm((unsigned)d, (unsigned)c, 0x0c0c0400u);
    return __builtin_amdgcn_perm(hi, lo, 0x05040100u);
}
__device__ __forceinline__ u32x4 lds_rd128(unsigned addr) {
    u32x4 v;
    asm volatile("ds_read_b128 %0, %1\n\ts_waitcnt lgkmcnt(0)" : "=v"(v) : "v"(addr) : "memory");
    return v;
}
__device__ __forceinline__ void lds_wr32_max(unsigned addr, unsigned val) {
    asm volatile("ds_max_u32 %0, %1" ::"v"(addr), "v"(val) : "memory");
}
__device__ __forceinline__ void lds_wr32(unsigned addr, unsigned val) {
    asm volatile("ds_write_b32 %0, %1" ::"v"(addr), "v"(val) : "memory");
}

#define SPIN for (;;)
#define TS(i) do { } while (0)

__global__ __launch_bounds__(512, 2) void fused_kernel(const int* __restrict__ qx, const int* __restrict__ qw,
                                                       const __hip_bfloat16* __restrict__ scale_x,
                                                       const int* __restrict__ zero_x,
                                                       const __hip_bfloat16* __restrict__ scale_w,
                                                       const __hip_bfloat16* __restrict__ bias,
                                                       __hip_bfloat16* __restrict__ out, uint8_t* __restrict__ ws) {
    extern __shared__ __attribute__((aligned(16))) uint8_t smem[];
    const int tid = threadIdx.x, lane = tid & 63;
    const int wid = __builtin_amdgcn_readfirstlane(tid >> 6);
    const int bid = blockIdx.x;
    const int tm = (bid & 1) * 8 + ((bid >> 3) & 7);
    const int tn = ((bid >> 1) & 3) * 4 + (bid >> 6);
    const int tmA = bid >> 4, subA = bid & 15;
    const int tnB = bid & 15, subB = bid >> 4;
    const unsigned smem_base = (unsigned)(uintptr_t)smem;
    const unsigned ctl = smem_base + OFF_CTL;
    GAS u64* flags = (GAS u64*)(ws + WS_FLAGS);
    u64 tag;
#define TAGWAIT()                                                                                                        \
    do {                                                                                                                 \
        asm volatile("s_load_dwordx2 %0, %1, 0x0\n\ts_waitcnt lgkmcnt(0)" : "=s"(tag) : "s"(ws + WS_EPOCH) : "memory"); \
        tag += 0x9E3779B97F4A7C15ull;                                                                                    \
    } while (0)

    const __amdgpu_buffer_rsrc_t rsF = __builtin_amdgcn_make_buffer_rsrc((void*)(ws + WS_FLAGS), 0, 128 * FSTR * 8, RSRC_W3);
    if (wid == 4) {
        const __amdgpu_buffer_rsrc_t rsW = __builtin_amdgcn_make_buffer_rsrc((void*)qw, 0, K_ * N_ * 4, RSRC_W3);
        const __amdgpu_buffer_rsrc_t rsB8 = __builtin_amdgcn_make_buffer_rsrc((void*)(ws + WS_B8), 0, N_ * K_ + 16 * 64 * 512, RSRC_W3);
        const int q = lane < 56 ? lane : lane - 8;
        const unsigned voff = (unsigned)((tnB * 224 + q * 4) * 4);
        const int soff0 = ((subB >> 2) * 64 + (subB & 3) * 4) * (N_ * 4);
        const unsigned vo0 = lane < 56 ? (unsigned)((tnB * 64 + subB) * UNIT + lane * 16) : 0x80000000u;
        const unsigned vc0 = (unsigned)(N_ * K_ + (tnB * 64 + subB) * 512 + lane * 8);
        const unsigned vf0 = lane == 0 ? (unsigned)(((16 + tnB) * FSTR + subB) * 8) : 0x80000000u;
        i32x4 st[WB][4];
        auto ld = [&](int t) {
#pragma unroll
            for (int x = 0; x < 4; ++x)
                st[t % WB][x] = __builtin_bit_cast(i32x4, __builtin_amdgcn_raw_buffer_load_b128(rsW, voff, soff0 + ((t >> 2) * 256 + (t & 3) * 16 + x) * (N_ * 4), RAW_AUX));
        };
#pragma unroll
        for (int t = 0; t < WB; ++t) ld(t);
        __builtin_amdgcn_sched_barrier(0);
        TAGWAIT();
        __builtin_amdgcn_s_barrier();
        __builtin_amdgcn_sched_barrier(0);
        const u32x2 tg = {(unsigned)tag, (unsigned)(tag >> 32)};
        int cs[4];
        u32x4 pk[4];
#pragma unroll
        for (int t = 0; t < 16; ++t) {
            const int p = t >> 2, i = t & 3;
#pragma unroll
            for (int c = 0; c < 4; ++c) {
                pk[c][i] = pack4(st[t % WB][0][c], st[t % WB][1][c], st[t % WB][2][c], st[t % WB][3][c]);
                cs[c] = __builtin_amdgcn_sdot4((int)pk[c][i], 0x01010101, i ? cs[c] : 0, false);
            }
            if (i == 3) {
                if (p == 0) TS(1);
#pragma unroll
                for (int c = 0; c < 4; ++c) __builtin_amdgcn_raw_buffer_store_b128(pk[c], rsB8, vo0 + p * 16 * UNIT + c * 896, 0, AUX_SC1);
                const u32x2 cp = {__builtin_amdgcn_perm((unsigned)cs[1], (unsigned)cs[0], 0x05040100u),
                                  __builtin_amdgcn_perm((unsigned)cs[3], (unsigned)cs[2], 0x05040100u)};
                __builtin_amdgcn_raw_buffer_store_b64(cp, rsB8, vc0 + p * 16 * 512, 0, AUX_SC1);
            }
            __builtin_amdgcn_sched_barrier(0);
            if (t + WB < 16) ld(t + WB);
            __builtin_amdgcn_sched_barrier(0);
            if ((i == FLAGI && t >= 4) || t == 15) {
                if (i == FLAGI && t >= 4) {
                    constexpr int dummy = 0; (void)dummy;
                    const int pp = p - 1;
                    const int last = t + WB < 16 ? t : 15 - WB;
                    const int after = last - (4 * pp + 3) + 1;
                    const int extra = t == 15 ? 5 : 0;
                    asm volatile("s_waitcnt vmcnt(%0)" ::"n"((after > 0 ? after : 0) * 4 + extra) : "memory");
                    __builtin_amdgcn_raw_buffer_store_b64(tg, rsF, vf0 + pp * 32 * FSTR * 8, 0, AUX_SC1);
                    lds_wr32(ctl + CT_GO, pp + 1);
                    TS(2 + pp);
                }
                if (t == 15) {
                    asm volatile("s_waitcnt vmcnt(0)" ::: "memory");
                    __builtin_amdgcn_raw_buffer_store_b64(tg, rsF, vf0 + 3 * 32 * FSTR * 8, 0, AUX_SC1);
                    lds_wr32(ctl + CT_GO, 4);
                    TS(5);
                }
                __builtin_amdgcn_sched_barrier(0);
            }
        }
    } else if (wid == 5) {
        const __amdgpu_buffer_rsrc_t rsX = __builtin_amdgcn_make_buffer_rsrc((void*)qx, 0, M_ * K_ * 4, RSRC_W3);
        const __amdgpu_buffer_rsrc_t rsA8 = __builtin_amdgcn_make_buffer_rsrc((void*)(ws + WS_A8), 0, M_ * K_, RSRC_W3);
        const int rb = tmA * 8 + (subA >> 1), kb0 = 2 * (subA & 1);
        const unsigned voff = (unsigned)(((rb * 16 + (lane & 15)) * K_ + kb0 * 64 + (lane >> 4) * 4) * 4);
        const unsigned vo0 = (unsigned)((rb * 16 + kb0) * 1024 + lane * 16);
        const unsigned vf0 = lane == 0 ? (unsigned)((tmA * FSTR + subA) * 8) : 0x80000000u;
        i32x4 v[WA][2];
        auto ld = [&](int t) {
#pragma unroll
            for (int x = 0; x < 2; ++x)
                v[t % WA][x] = __builtin_bit_cast(i32x4, __builtin_amdgcn_raw_buffer_load_b128(rsX, voff + (t >> 2) * 1024 + (t & 3) * 128 + x * 64, 0, RAW_AUX));
        };
#pragma unroll
        for (int t = 0; t < WA; ++t) ld(t);
        __builtin_amdgcn_sched_barrier(0);
        TAGWAIT();
        __builtin_amdgcn_s_barrier();
        __builtin_amdgcn_sched_barrier(0);
        const u32x2 tg = {(unsigned)tag, (unsigned)(tag >> 32)};
        u32x4 o;
#pragma unroll
        for (int t = 0; t < 16; ++t) {
            const int p = t >> 2, i = t & 3;
#pragma unroll
            for (int x = 0; x < 2; ++x) o[(i & 1) * 2 + x] = pack4(v[t % WA][x][0], v[t % WA][x][1], v[t % WA][x][2], v[t % WA][x][3]);
            if (i & 1) {
                if (t == 3) TS(1);
                __builtin_amdgcn_raw_buffer_store_b128(o, rsA8, vo0 + p * 4096 + (i >> 1) * 1024, 0, AUX_SC1);
            }
            __builtin_amdgcn_sched_barrier(0);
            if (t + WA < 16) ld(t + WA);
            __builtin_amdgcn_sched_barrier(0);
            if ((i == FLAGI && t >= 4) || t == 15) {
                if (i == FLAGI && t >= 4) {
                    const int pp = p - 1;
                    const int last = t + WA < 16 ? t : 15 - WA;
                    const int after = last - (4 * pp + 3) + 1;
                    const int extra = i == 3 ? 2 : (i >= 1 ? 1 : 0);
                    asm volatile("s_waitcnt vmcnt(%0)" ::"n"((after > 0 ? after : 0) * 2 + extra) : "memory");
                    __builtin_amdgcn_raw_buffer_store_b64(tg, rsF, vf0 + pp * 32 * FSTR * 8, 0, AUX_SC1);
                    TS(2 + pp);
                }
                if (t == 15) {
                    asm volatile("s_waitcnt vmcnt(0)" ::: "memory");
                    __builtin_amdgcn_raw_buffer_store_b64(tg, rsF, vf0 + 3 * 32 * FSTR * 8, 0, AUX_SC1);
                    TS(5);
                }
                __builtin_amdgcn_sched_barrier(0);
            }
        }
    } else {
        if (wid == 6 && lane < 16) lds_wr32(ctl + lane * 4, 0u);
        if (wid == 7 && lane < 56) *(volatile LAS i32x4*)(uintptr_t)(smem_base + OFF_CORR + lane * 16) = (i32x4){0, 0, 0, 0};
        TAGWAIT();
        __builtin_amdgcn_s_barrier();
    }

    if (wid >= 6) {
        const int lw = wid - 6;
        if (lw == 1) {
            const int l4 = lane < 56 ? lane : 0;
            const u32x2 sw4 = reinterpret_cast<const u32x2*>(scale_w)[tn * (BN / 4) + l4];
            const u32x2 bi4 = reinterpret_cast<const u32x2*>(bias)[tn * (BN / 4) + l4];
            const float sx = __uint_as_float((unsigned)reinterpret_cast<const unsigned short*>(scale_x)[0] << 16);
            if (lane < 56) {
                const f32x4 sc = {sx * __uint_as_float(sw4[0] << 16), sx * __uint_as_float(sw4[0] & 0xffff0000u),
                                  sx * __uint_as_float(sw4[1] << 16), sx * __uint_as_float(sw4[1] & 0xffff0000u)};
                const f32x4 bi = {__uint_as_float(bi4[0] << 16), __uint_as_float(bi4[0] & 0xffff0000u),
                                  __uint_as_float(bi4[1] << 16), __uint_as_float(bi4[1] & 0xffff0000u)};
                *(LAS f32x4*)(uintptr_t)(smem_base + OFF_SCALE + lane * 16) = sc;
                *(LAS f32x4*)(uintptr_t)(smem_base + OFF_BIAS + lane * 16) = bi;
            }
        }
        TS(1);
        GAS u64* fl = flags + ((lane & 16) ? 16 + tn : tm) * FSTR + (lane & 15);
        unsigned pub = 0;
        u64 pf = 0;
        const __amdgpu_buffer_rsrc_t rsL = __builtin_amdgcn_make_buffer_rsrc((void*)(ws + WS_A8), 0, (M_ + N_) * K_, RSRC_W3);
        const int soA = (tm * 8 + lw) * 16384, soB = M_ * K_ + tn * (64 * UNIT) + lw * 1024;
        const unsigned full = ctl + CT_FULL + lw * 4;
        unsigned kfree = 0;
#define PUBLISH(V) do { pub = max(pub, (unsigned)(V)); lds_wr32(full, pub); } while (0)
#pragma unroll
        for (int kb = 0; kb < 16; ++kb) {
            if ((kb & 3) == 0) {
                bool ok = false;
                if (kb) {
                    asm volatile("s_waitcnt vmcnt(11)" : "+v"(pf)::"memory");
                    PUBLISH(kb - 1);
                    if (lw == 0) ok = __all(pf == tag);
                    else ok = (unsigned)__builtin_amdgcn_readfirstlane(lds_rd128(ctl + CT_ARR)[3]) > (unsigned)(kb >> 2);
                    if (!ok) {
                        asm volatile("s_waitcnt vmcnt(0)" ::: "memory");
                        PUBLISH(kb);
                    }
                }
                if (!ok) {
                    if (lw == 0) {
                        SPIN {
                            const u32x4 v = lds_rd128(ctl + CT_ARR);
                            if ((unsigned)__builtin_amdgcn_readfirstlane(v[2]) > (unsigned)(kb >> 2)) break;
                            __builtin_amdgcn_s_sleep(1);
                        }
                        SPIN {
                            u64 x;
                            asm volatile("global_load_dwordx2 %0, %1, off sc1\n\ts_waitcnt vmcnt(0)" : "=v"(x) : "v"(fl + (kb >> 2) * 32 * FSTR) : "memory");
                            if (__all(x == tag)) break;
                            __builtin_amdgcn_s_sleep(PSLEEP);
                        }
                    } else {
                        SPIN {
                            const u32x4 v = lds_rd128(ctl + CT_ARR);
                            if ((unsigned)__builtin_amdgcn_readfirstlane(v[3]) > (unsigned)(kb >> 2)) break;
                            __builtin_amdgcn_s_sleep(1);
                        }
                    }
                }
                if (lw == 0) lds_wr32(ctl + CT_PH2, (kb >> 2) + 1);
                TS(2 + (kb >> 2));
            }
            if (kb >= NSTAGE && kfree <= (unsigned)(kb - NSTAGE)) {
                u32x4 v = lds_rd128(ctl + CT_FREE);
                kfree = __builtin_amdgcn_readfirstlane(min(min(v[0], v[1]), min(v[2], v[3])));
                if (kfree <= (unsigned)(kb - NSTAGE)) {
                    if (LAG > 1) {
                        asm volatile("s_waitcnt vmcnt(11)" ::: "memory");
                        PUBLISH(kb - 1);
                    }
                    asm volatile("s_waitcnt vmcnt(0)" ::: "memory");
                    PUBLISH(kb);
                    SPIN {
                        v = lds_rd128(ctl + CT_FREE);
                        kfree = __builtin_amdgcn_readfirstlane(min(min(v[0], v[1]), min(v[2], v[3])));
                        if (kfree > (unsigned)(kb - NSTAGE)) break;
                        __builtin_amdgcn_s_sleep(1);
                    }
                }
            }
            LAS uint8_t* base = (LAS uint8_t*)(uintptr_t)(smem_base + (kb % NSTAGE) * STAGE_BYTES + lw * 1024);
#pragma unroll
            for (int i = 0; i < 11; ++i) {
                const int so = i < 4 ? soA + i * 32768 + kb * 1024 : soB + kb * B_STAGE + (i - 4) * 2048;
                __builtin_amdgcn_raw_ptr_buffer_load_lds(rsL, base + i * 2048, 16, lane * 16, so, 0, AUX_SC1);
            }
            if (kb >= LAG) {
                if ((kb & 3) == 3 && kb < 12 && lw == 0) asm volatile("s_waitcnt vmcnt(%0)" ::"n"(11 * LAG + 1) : "memory");
                else asm volatile("s_waitcnt vmcnt(%0)" ::"n"(11 * LAG) : "memory");
                PUBLISH(kb - LAG + 1);
            }
            if ((kb & 3) == 2 && kb < 12 && lw == 0)
                asm volatile("global_load_dwordx2 %0, %1, off sc1" : "=v"(pf) : "v"(fl + ((kb >> 2) + 1) * 32 * FSTR) : "memory");
        }
        asm volatile("s_waitcnt vmcnt(11)" ::: "memory");
        PUBLISH(15);
        asm volatile("s_waitcnt vmcnt(0)" ::: "memory");
        PUBLISH(16);
        TS(7);
        asm volatile("s_waitcnt lgkmcnt(0)" ::: "memory");
        __builtin_amdgcn_s_barrier();
    } else if (wid >= 4) {
        SPIN {
            const u32x4 v = lds_rd128(ctl + CT_ARR);
            if (__builtin_amdgcn_readfirstlane(v[3]) == NPH) break;
            __builtin_amdgcn_s_sleep(2);
        }
        {
            const __amdgpu_buffer_rsrc_t rsC = __builtin_amdgcn_make_buffer_rsrc((void*)(ws + WS_CP), 0, 16 * 64 * 512, RSRC_W3);
            u32x4 cp[16];
#pragma unroll
            for (int x = 0; x < 16; ++x)
                cp[x] = __builtin_amdgcn_raw_buffer_load_b128(rsC, lane * 16, tn * 32768 + (wid - 4) * 16384 + x * 1024, AUX_SC1);
            const int nzx = -zero_x[0];
            int s[8] = {0, 0, 0, 0, 0, 0, 0, 0};
#pragma unroll
            for (int hh = 0; hh < 2; ++hh) {
#pragma unroll
                for (int d = 0; d < 4; ++d) {
                    s16x2 a = {0, 0};
#pragma unroll
                    for (int x = 0; x < 8; ++x) {
                        const unsigned w_ = cp[8 * hh + x][d];
                        a += __builtin_bit_cast(s16x2, w_);
                    }
                    s[2 * d] += a[0];
                    s[2 * d + 1] += a[1];
                }
            }
            if ((lane & 31) < 28) {
#pragma unroll
                for (int e = 0; e < 8; ++e)
                    __hip_atomic_fetch_add((LAS int*)(uintptr_t)(smem_base + OFF_CORR + ((lane & 31) * 8 + e) * 4), nzx * s[e],
                                           __ATOMIC_RELAXED, __HIP_MEMORY_SCOPE_WORKGROUP);
            }
        }
        TS(8);
        if (bid == 0 && wid == 5) {
            SPIN {
                bool ok = true;
#pragma unroll
                for (int i = 0; i < 4; ++i)
                    ok &= __hip_atomic_load(flags + (3 * 32 + i * 4 + (lane >> 4)) * FSTR + (lane & 15), __ATOMIC_RELAXED, __HIP_MEMORY_SCOPE_AGENT) == tag;
                if (__all(ok)) break;
                __builtin_amdgcn_s_sleep(8);
            }
            if (lane == 0) __hip_atomic_store((GAS u64*)(ws + WS_EPOCH), tag, __ATOMIC_RELAXED, __HIP_MEMORY_SCOPE_AGENT);
        }
        asm volatile("s_waitcnt lgkmcnt(0)" ::: "memory");
        __builtin_amdgcn_s_barrier();
    } else {
        const int wm = wid >> 1, wn = wid & 1;
        i32x4 acc[4][7];
        i32x4 af[2][4], bfr[2][7];
#pragma unroll
        for (int i = 0; i < 4; ++i)
#pragma unroll
            for (int j = 0; j < 7; ++j) {
                acc[i][j] = (i32x4){0, 0, 0, 0};
                asm volatile("" : "+v"(acc[i][j]));
            }
        const unsigned fA = smem_base + wm * 4096 + lane * 16;
        const unsigned fB = smem_base + A_STAGE + (lane >> 4) * UNIT + wn * 1792 + (lane & 15) * 16;
        const unsigned freew = ctl + CT_FREE + wid * 4;
        unsigned known = 0;
#define DSR(DST, ADDR, OFF) asm volatile("ds_read_b128 %0, %1 offset:%2" : "=v"(DST) : "v"(ADDR), "n"(OFF) : "memory")
#define MF(I, J, SET) asm volatile("v_mfma_i32_16x16x64_i8 %0, %1, %2, %0" : "+v"(acc[I][J]) : "v"(bfr[SET][J]), "v"(af[SET][I]))
#define NEED(X)                                                                                                \
    do {                                                                                                       \
        if (__builtin_expect(known <= (unsigned)(X), 0)) {                                                                          \
            SPIN {                                                                                         \
                const u32x4 v_ = lds_rd128(ctl + CT_FULL);                                                     \
                known = __builtin_amdgcn_readfirstlane(min(v_[0], v_[1]));                                     \
                if (known > (unsigned)(X)) break;                                                              \
                __builtin_amdgcn_s_sleep(1);                                                                   \
            }                                                                                                  \
        }                                                                                                      \
    } while (0)
#define STEP(SET, KB)                                                                        \
    do {                                                                                     \
        MF(0, 0, SET);                                                                       \
        if ((KB) + 1 < 16) {                                                                 \
            NEED((KB) + 1);                                                                  \
            __builtin_amdgcn_sched_barrier(0);                                               \
            const unsigned aA_ = fA + (((KB) + 1) % NSTAGE) * STAGE_BYTES;                   \
            const unsigned aB_ = fB + (((KB) + 1) % NSTAGE) * STAGE_BYTES;                   \
            MF(1, 0, SET);                                                                   \
            DSR(bfr[(SET) ^ 1][0], aB_, 0);       MF(2, 0, SET);                             \
            DSR(af[(SET) ^ 1][0], aA_, 0);        MF(3, 0, SET);                             \
            DSR(af[(SET) ^ 1][1], aA_, 1024);     MF(0, 1, SET);                             \
            DSR(af[(SET) ^ 1][2], aA_, 2048);     MF(1, 1, SET);                             \
            DSR(af[(SET) ^ 1][3], aA_, 3072);     MF(2, 1, SET);                             \
            DSR(bfr[(SET) ^ 1][1], aB_, 256);     MF(3, 1, SET);                             \
            DSR(bfr[(SET) ^ 1][2], aB_, 512);     MF(0, 2, SET);                             \
            DSR(bfr[(SET) ^ 1][3], aB_, 768);     MF(1, 2, SET);                             \
            DSR(bfr[(SET) ^ 1][4], aB_, 1024);    MF(2, 2, SET);                             \
            DSR(bfr[(SET) ^ 1][5], aB_, 1280);    MF(3, 2, SET);                             \
            DSR(bfr[(SET) ^ 1][6], aB_, 1536);                                               \
            MF(0, 3, SET); MF(1, 3, SET); MF(2, 3, SET); MF(3, 3, SET);                      \
            MF(0, 4, SET); MF(1, 4, SET);                                                    \
            if ((KB) + 2 < 16) asm volatile("ds_read_b64 %0, %1" : "=v"(fv) : "v"(fullv) : "memory"); \
            MF(2, 4, SET); MF(3, 4, SET);                                                    \
            MF(0, 5, SET); MF(1, 5, SET); MF(2, 5, SET); MF(3, 5, SET);                      \
            MF(0, 6, SET);                                                                   \
            asm volatile("s_waitcnt lgkmcnt(0)" : "+v"(fv)::"memory");                       \
            if ((KB) + 1 + NSTAGE < 16) lds_wr32(freew, (KB) + 2);                           \
            MF(1, 6, SET);                                                                   \
            if ((KB) + 2 < 16) known = max(known, (unsigned)__builtin_amdgcn_readfirstlane(min(fv[0], fv[1]))); \
            __builtin_amdgcn_sched_barrier(0);                                               \
            MF(2, 6, SET); MF(3, 6, SET);                                                    \
        } else {                                                                             \
            MF(1, 0, SET); MF(2, 0, SET); MF(3, 0, SET);                                     \
            _Pragma("unroll") for (int j = 1; j < 7; ++j)                                    \
            _Pragma("unroll") for (int i = 0; i < 4; ++i) MF(i, j, SET);                     \
        }                                                                                    \
        __builtin_amdgcn_sched_barrier(0);                                                   \
    } while (0)
        u32x2 fv = {0, 0};
        unsigned fullv = ctl + CT_FULL;
        asm volatile("" : "+v"(fullv));
        NEED(0);
        TS(4);
#pragma unroll
        for (int j = 0; j < 7; ++j) DSR(bfr[0][j], fB, j * 256);
#pragma unroll
        for (int i = 0; i < 4; ++i) DSR(af[0][i], fA, i * 1024);
        asm volatile("s_waitcnt lgkmcnt(0)" ::: "memory");
        lds_wr32(freew, 1);
        __builtin_amdgcn_sched_barrier(0);
#pragma unroll
        for (int kb = 0; kb < 16; kb += 2) {
            STEP(0, kb);
            STEP(1, kb + 1);
            if (kb == 2) TS(5);
            if (kb == 6) TS(6);
            if (kb == 10) TS(7);
        }
        TS(8);
        __builtin_amdgcn_sched_barrier(0);
        __builtin_amdgcn_s_barrier();
        asm volatile("s_nop 7\n\ts_nop 7" ::: "memory");
#pragma unroll
        for (int j = 0; j < 7; ++j) asm volatile("" : "+v"(acc[0][j]), "+v"(acc[1][j]), "+v"(acc[2][j]), "+v"(acc[3][j]));
        LAS uint8_t* dst = (LAS uint8_t*)(uintptr_t)(smem_base + (wm * 64 + (lane & 15)) * ACC_PITCH + (wn * 112 + (lane >> 4) * 4) * 4);
#pragma unroll
        for (int j = 0; j < 7; ++j)
#pragma unroll
            for (int i = 0; i < 4; ++i) *reinterpret_cast<LAS i32x4*>(dst + i * 16 * ACC_PITCH + j * 64) = acc[i][j];
    }
    asm volatile("s_waitcnt lgkmcnt(0)" ::: "memory");
    __builtin_amdgcn_s_barrier();
    TS(9);
    if (tid < 448) {
        const int cc = tid % 28, r0 = tid / 28;
        const LAS uint8_t* sm = (const LAS uint8_t*)(uintptr_t)smem_base;
        const f32x4 s0 = *reinterpret_cast<const LAS f32x4*>(sm + OFF_SCALE + cc * 32), s1 = *reinterpret_cast<const LAS f32x4*>(sm + OFF_SCALE + cc * 32 + 16);
        const f32x4 b0 = *reinterpret_cast<const LAS f32x4*>(sm + OFF_BIAS + cc * 32), b1 = *reinterpret_cast<const LAS f32x4*>(sm + OFF_BIAS + cc * 32 + 16);
        const i32x4 c0 = *reinterpret_cast<const LAS i32x4*>(sm + OFF_CORR + cc * 32), c1 = *reinterpret_cast<const LAS i32x4*>(sm + OFF_CORR + cc * 32 + 16);
        const __amdgpu_buffer_rsrc_t rsO = __builtin_amdgcn_make_buffer_rsrc((void*)out, 0, M_ * N_ * 2, RSRC_W3);
        const unsigned ovo = (unsigned)(((tm * BM + r0) * N_ + tn * BN + cc * 8) * 2);
#pragma unroll
        for (int i = 0; i < 8; ++i) {
            const LAS uint8_t* src = sm + (r0 + 16 * i) * ACC_PITCH + cc * 8;
            i32x2 a[4];
#pragma unroll
            for (int c = 0; c < 4; ++c) a[c] = *reinterpret_cast<const LAS i32x2*>(src + c * 224);
            f32x2 v0 = {(float)(a[0][0] + c0[0]) * s0[0] + b0[0], (float)(a[1][0] + c0[1]) * s0[1] + b0[1]};
            f32x2 v1 = {(float)(a[2][0] + c0[2]) * s0[2] + b0[2], (float)(a[3][0] + c0[3]) * s0[3] + b0[3]};
            f32x2 v2 = {(float)(a[0][1] + c1[0]) * s1[0] + b1[0], (float)(a[1][1] + c1[1]) * s1[1] + b1[1]};
            f32x2 v3 = {(float)(a[2][1] + c1[2]) * s1[2] + b1[2], (float)(a[3][1] + c1[3]) * s1[3] + b1[3]};
            u32x4 o;
            o[0] = __builtin_bit_cast(unsigned, __builtin_convertvector(v0, bf16x2_t));
            o[1] = __builtin_bit_cast(unsigned, __builtin_convertvector(v1, bf16x2_t));
            o[2] = __builtin_bit_cast(unsigned, __builtin_convertvector(v2, bf16x2_t));
            o[3] = __builtin_bit_cast(unsigned, __builtin_convertvector(v3, bf16x2_t));
            __builtin_amdgcn_raw_buffer_store_b128(o, rsO, ovo + i * 16 * (N_ * 2), 0, OUT_AUX);
        }
    }
    TS(10);
}

extern "C" void kernel_launch(void* const* d_in, const int* in_sizes, int n_in,
                              void* d_out, int out_size, void* d_ws, size_t ws_size,
                              hipStream_t stream) {
    const int* qx = (const int*)d_in[0];
    const int* qw = (const int*)d_in[1];
    const __hip_bfloat16* scale_x = (const __hip_bfloat16*)d_in[2];
    const int* zero_x = (const int*)d_in[3];
    const __hip_bfloat16* scale_w = (const __hip_bfloat16*)d_in[4];
    const __hip_bfloat16* bias = (const __hip_bfloat16*)d_in[5];
    (void)hipFuncSetAttribute((const void*)fused_kernel, hipFuncAttributeMaxDynamicSharedMemorySize, SMEM_BYTES);
    fused_kernel<<<256, 512, SMEM_BYTES, stream>>>(qx, qw, scale_x, zero_x, scale_w, bias, (__hip_bfloat16*)d_out,
                                                   (uint8_t*)d_ws);
}
```

```cpp
#include <hip/hip_runtime.h>
#include <hip/hip_bf16.h>
#include <stdint.h>

typedef int i32x4 __attribute__((ext_vector_type(4)));
typedef int i32x2 __attribute__((ext_vector_type(2)));
typedef unsigned int u32x4 __attribute__((ext_vector_type(4)));
typedef unsigned int u32x2 __attribute__((ext_vector_type(2)));
typedef float f32x2 __attribute__((ext_vector_type(2)));
typedef float f32x4 __attribute__((ext_vector_type(4)));
typedef short s16x2 __attribute__((ext_vector_type(2)));
typedef __bf16 bf16x2_t __attribute__((ext_vector_type(2)));
typedef unsigned long long u64;
#define GAS __attribute__((address_space(1)))
#define LAS __attribute__((address_space(3)))

constexpr int M_ = 2048, K_ = 1024, N_ = 3584;
constexpr int BM = 128, BN = 224;
constexpr int NSTAGE = 6;
constexpr int UNIT = 3584;
constexpr int A_STAGE = 8192, B_STAGE = 4 * UNIT;
constexpr int STAGE_BYTES = A_STAGE + B_STAGE;
constexpr int RING_BYTES = NSTAGE * STAGE_BYTES;
constexpr int ACC_PITCH = BN * 4 + 16;
constexpr int OFF_CTL = RING_BYTES;
constexpr int CT_FULL = 0, CT_FREE = 16, CT_TAG = 32, CT_ARR = 48, CT_GO = 56, CT_PH2 = 60;
constexpr int OFF_SCALE = OFF_CTL + 64;
constexpr int OFF_BIAS = OFF_SCALE + 896;
constexpr int OFF_CORR = OFF_BIAS + 896;
constexpr int OFF_TR = OFF_CORR + 896;
constexpr int SMEM_BYTES = OFF_TR + 2048;
static_assert(BM * ACC_PITCH <= RING_BYTES, "acc staging must fit in the ring");
constexpr int NPH = 4;
constexpr size_t WS_EPOCH = 0;
#define FSUB 16
#define FSTR 544
constexpr size_t WS_FLAGS = 8u << 20;
constexpr size_t WS_A8 = 65536;
constexpr size_t WS_B8 = WS_A8 + (size_t)M_ * K_;
constexpr size_t WS_CP = WS_B8 + (size_t)N_ * K_;
constexpr size_t WS_DBG = WS_CP + 16 * 64 * 512;
constexpr int AUX_SC1 = 16;
constexpr int RSRC_W3 = 0x00020000;
#define WB 4
#define WA 2
#define FLAGI 1
#define LAG 1
#define PIPE 0
#define PGAP 6
#define PSLEEP 4
#define OUT_AUX 16
#define RAW_AUX 2
#define GO_KB 1

__device__ __forceinline__ unsigned pack4(int a, int b, int c, int d) {
    const unsigned lo = __builtin_amdgcn_perm((unsigned)b, (unsigned)a, 0x0c0c0400u);
    const unsigned hi = __builtin_amdgcn_perm((unsigned)d, (unsigned)c, 0x0c0c0400u);
    return __builtin_amdgcn_perm(hi, lo, 0x05040100u);
}
__device__ __forceinline__ u32x4 lds_rd128(unsigned addr) {
    u32x4 v;
    asm volatile("ds_read_b128 %0, %1\n\ts_waitcnt lgkmcnt(0)" : "=v"(v) : "v"(addr) : "memory");
    return v;
}
__device__ __forceinline__ void lds_wr32_max(unsigned addr, unsigned val) {
    asm volatile("ds_max_u32 %0, %1" ::"v"(addr), "v"(val) : "memory");
}
__device__ __forceinline__ void lds_wr32(unsigned addr, unsigned val) {
    asm volatile("ds_write_b32 %0, %1" ::"v"(addr), "v"(val) : "memory");
}

#define SPIN for (;;)
#define TS(i) do { } while (0)

__global__ __launch_bounds__(512, 2) void fused_kernel(const int* __restrict__ qx, const int* __restrict__ qw,
                                                       const __hip_bfloat16* __restrict__ scale_x,
                                                       const int* __restrict__ zero_x,
                                                       const __hip_bfloat16* __restrict__ scale_w,
                                                       const __hip_bfloat16* __restrict__ bias,
                                                       __hip_bfloat16* __restrict__ out, uint8_t* __restrict__ ws) {
    extern __shared__ __attribute__((aligned(16))) uint8_t smem[];
    const int tid = threadIdx.x, lane = tid & 63;
    const int wid = __builtin_amdgcn_readfirstlane(tid >> 6);
    const int bid = blockIdx.x;
    const int tm = (bid & 1) * 8 + ((bid >> 3) & 7);
    const int tn = ((bid >> 1) & 3) * 4 + (bid >> 6);
    const int tmA = bid >> 4, subA = bid & 15;
    const int tnB = bid & 15, subB = bid >> 4;
    const unsigned smem_base = (unsigned)(uintptr_t)smem;
    const unsigned ctl = smem_base + OFF_CTL;
    GAS u64* flags = (GAS u64*)(ws + WS_FLAGS);
    u64 tag;
#define TAGWAIT()                                                                                                        \
    do {                                                                                                                 \
        asm volatile("s_load_dwordx2 %0, %1, 0x0\n\ts_waitcnt lgkmcnt(0)" : "=s"(tag) : "s"(ws + WS_EPOCH) : "memory"); \
        tag += 0x9E3779B97F4A7C15ull;                                                                                    \
    } while (0)

    const __amdgpu_buffer_rsrc_t rsF = __builtin_amdgcn_make_buffer_rsrc((void*)(ws + WS_FLAGS), 0, 128 * FSTR * 8, RSRC_W3);
    if (wid == 4) {
        const __amdgpu_buffer_rsrc_t rsW = __builtin_amdgcn_make_buffer_rsrc((void*)qw, 0, K_ * N_ * 4, RSRC_W3);
        const __amdgpu_buffer_rsrc_t rsB8 = __builtin_amdgcn_make_buffer_rsrc((void*)(ws + WS_B8), 0, N_ * K_ + 16 * 16 * 512, RSRC_W3);
        const int q = lane < 56 ? lane : lane - 8;
        const unsigned voff = (unsigned)((tnB * 224 + q * 4) * 4);
        const int soff0 = ((subB >> 3) * 128 + (subB & 7) * 4) * (N_ * 4);
        const unsigned vo0 = lane < 56 ? (unsigned)((tnB * 64 + subB) * UNIT + lane * 16) : 0x80000000u;
        const unsigned vf0 = lane == 0 ? (unsigned)(((16 + tnB) * FSTR + subB * FSUB) * 8) : 0x80000000u;
        i32x4 st[WB][4];
        auto ld = [&](int t) {
#pragma unroll
            for (int x = 0; x < 4; ++x)
                st[t % WB][x] = __builtin_bit_cast(i32x4, __builtin_amdgcn_raw_buffer_load_b128(rsW, voff, soff0 + ((t >> 2) * 256 + (t & 3) * 32 + x) * (N_ * 4), RAW_AUX));
        };
#pragma unroll
        for (int t = 0; t < WB; ++t) ld(t);
        __builtin_amdgcn_sched_barrier(0);
        TAGWAIT();
        __builtin_amdgcn_s_barrier();
        __builtin_amdgcn_sched_barrier(0);
        const u32x2 tg = {(unsigned)tag, (unsigned)(tag >> 32)};
        int cs[4] = {0, 0, 0, 0};
        u32x4 pk[4];
#pragma unroll
        for (int t = 0; t < 16; ++t) {
            const int p = t >> 2, i = t & 3;
#pragma unroll
            for (int c = 0; c < 4; ++c) {
                pk[c][i] = pack4(st[t % WB][0][c], st[t % WB][1][c], st[t % WB][2][c], st[t % WB][3][c]);
                cs[c] = __builtin_amdgcn_sdot4((int)pk[c][i], 0x01010101, cs[c], false);
            }
            if (i == 3) {
                if (p == 0) TS(1);
#pragma unroll
                for (int c = 0; c < 4; ++c) __builtin_amdgcn_raw_buffer_store_b128(pk[c], rsB8, vo0 + p * 16 * UNIT + c * 896, 0, AUX_SC1);
                if (t == 15) {
                    const u32x2 cp = {__builtin_amdgcn_perm((unsigned)cs[1], (unsigned)cs[0], 0x05040100u),
                                      __builtin_amdgcn_perm((unsigned)cs[3], (unsigned)cs[2], 0x05040100u)};
                    __builtin_amdgcn_raw_buffer_store_b64(cp, rsB8, (unsigned)(N_ * K_ + (tnB * 16 + subB) * 512 + lane * 8), 0, AUX_SC1);
                }
            }
            __builtin_amdgcn_sched_barrier(0);
            if (t + WB < 16) ld(t + WB);
            __builtin_amdgcn_sched_barrier(0);
            if (i == FLAGI && t >= 4) {
                const int pp = p - 1;
                const int last = t + WB < 16 ? t : 15 - WB;
                const int after = last - (4 * pp + 3) + 1;
                asm volatile("s_waitcnt vmcnt(%0)" ::"n"((after > 0 ? after : 0) * 4) : "memory");
                __builtin_amdgcn_raw_buffer_store_b64(tg, rsF, vf0 + pp * 32 * FSTR * 8, 0, AUX_SC1);
                lds_wr32(ctl + CT_GO, pp + 1);
                TS(2 + pp);
                __builtin_amdgcn_sched_barrier(0);
            }
            if (t == 15) {
                asm volatile("s_waitcnt vmcnt(0)" ::: "memory");
                __builtin_amdgcn_raw_buffer_store_b64(tg, rsF, vf0 + 3 * 32 * FSTR * 8, 0, AUX_SC1);
                lds_wr32(ctl + CT_GO, 4);
                TS(5);
            }
        }
    } else if (wid == 5) {
        const __amdgpu_buffer_rsrc_t rsX = __builtin_amdgcn_make_buffer_rsrc((void*)qx, 0, M_ * K_ * 4, RSRC_W3);
        const __amdgpu_buffer_rsrc_t rsA8 = __builtin_amdgcn_make_buffer_rsrc((void*)(ws + WS_A8), 0, M_ * K_, RSRC_W3);
        const int rb = tmA * 8 + (subA >> 1), kb0 = 2 * (subA & 1);
        const unsigned voff = (unsigned)(((rb * 16 + (lane >> 3)) * K_ + kb0 * 64 + (lane & 7) * 4) * 4);
        const unsigned vo0 = (unsigned)((rb * 16 + kb0) * 1024 + lane * 16);
        const unsigned vf0 = lane == 0 ? (unsigned)((tmA * FSTR + subA * FSUB) * 8) : 0x80000000u;
        const unsigned tw = smem_base + OFF_TR + (lane & 7) * 256 + (lane >> 3) * 16, tr = smem_base + OFF_TR + lane * 16;
        i32x4 v[WA][4];
        auto ld = [&](int t) {
#pragma unroll
            for (int x = 0; x < 4; ++x)
                v[t % WA][x] = __builtin_bit_cast(i32x4, __builtin_amdgcn_raw_buffer_load_b128(rsX, voff + (t >> 1) * 1024 + x * 128, (t & 1) * (8 * K_ * 4), RAW_AUX));
        };
#pragma unroll
        for (int t = 0; t < WA; ++t) ld(t);
        __builtin_amdgcn_sched_barrier(0);
        TAGWAIT();
        __builtin_amdgcn_s_barrier();
        __builtin_amdgcn_sched_barrier(0);
        const u32x2 tg = {(unsigned)tag, (unsigned)(tag >> 32)};
#pragma unroll
        for (int t = 0; t < 8; ++t) {
            const int p = t >> 1, h = t & 1;
            u32x4 o;
#pragma unroll
            for (int x = 0; x < 4; ++x) o[x] = pack4(v[t % WA][x][0], v[t % WA][x][1], v[t % WA][x][2], v[t % WA][x][3]);
            asm volatile("ds_write_b128 %0, %1 offset:%2" ::"v"(tw), "v"(o), "n"(h * 128) : "memory");
            if (h) {
                u32x4 o0, o1;
                asm volatile("ds_read_b128 %0, %2\n\tds_read_b128 %1, %2 offset:1024\n\ts_waitcnt lgkmcnt(0)" : "=&v"(o0), "=&v"(o1) : "v"(tr) : "memory");
                if (t == 1) TS(1);
                __builtin_amdgcn_raw_buffer_store_b128(o0, rsA8, vo0 + p * 4096, 0, AUX_SC1);
                __builtin_amdgcn_raw_buffer_store_b128(o1, rsA8, vo0 + p * 4096 + 1024, 0, AUX_SC1);
            }
            __builtin_amdgcn_sched_barrier(0);
            if (t + WA < 8) ld(t + WA);
            __builtin_amdgcn_sched_barrier(0);
            if (h == 0 && t >= 2) {
                const int last = t + WA < 8 ? t : 7 - WA;
                const int after = last - (t - 1) + 1;
                asm volatile("s_waitcnt vmcnt(%0)" ::"n"((after > 0 ? after : 0) * 4) : "memory");
                __builtin_amdgcn_raw_buffer_store_b64(tg, rsF, vf0 + (p - 1) * 32 * FSTR * 8, 0, AUX_SC1);
                TS(1 + p);
                __builtin_amdgcn_sched_barrier(0);
            }
            if (t == 7) {
                asm volatile("s_waitcnt vmcnt(0)" ::: "memory");
                __builtin_amdgcn_raw_buffer_store_b64(tg, rsF, vf0 + 3 * 32 * FSTR * 8, 0, AUX_SC1);
                TS(5);
            }
        }
    } else {
        if (wid == 6 && lane < 16) lds_wr32(ctl + lane * 4, 0u);
        TAGWAIT();
        __builtin_amdgcn_s_barrier();
    }

    if (wid >= 6) {
        const int lw = wid - 6;
        if (lw == 1) {
            const int l4 = lane < 56 ? lane : 0;
            const u32x2 sw4 = reinterpret_cast<const u32x2*>(scale_w)[tn * (BN / 4) + l4];
            const u32x2 bi4 = reinterpret_cast<const u32x2*>(bias)[tn * (BN / 4) + l4];
            const float sx = __uint_as_float((unsigned)reinterpret_cast<const unsigned short*>(scale_x)[0] << 16);
            if (lane < 56) {
                const f32x4 sc = {sx * __uint_as_float(sw4[0] << 16), sx * __uint_as_float(sw4[0] & 0xffff0000u),
                                  sx * __uint_as_float(sw4[1] << 16), sx * __uint_as_float(sw4[1] & 0xffff0000u)};
                const f32x4 bi = {__uint_as_float(bi4[0] << 16), __uint_as_float(bi4[0] & 0xffff0000u),
                                  __uint_as_float(bi4[1] << 16), __uint_as_float(bi4[1] & 0xffff0000u)};
                *(LAS f32x4*)(uintptr_t)(smem_base + OFF_SCALE + lane * 16) = sc;
                *(LAS f32x4*)(uintptr_t)(smem_base + OFF_BIAS + lane * 16) = bi;
            }
        }
        TS(1);
        GAS u64* fl = flags + ((lane & 16) ? 16 + tn : tm) * FSTR + (lane & 15) * FSUB;
        unsigned pub = 0;
        u64 pf = 0;
        const __amdgpu_buffer_rsrc_t rsL = __builtin_amdgcn_make_buffer_rsrc((void*)(ws + WS_A8), 0, (M_ + N_) * K_, RSRC_W3);
        const int soA = (tm * 8 + lw) * 16384, soB = M_ * K_ + tn * (64 * UNIT) + lw * 1024;
        const unsigned full = ctl + CT_FULL + lw * 4;
        unsigned kfree = 0;
        u64 x0 = 0, x1 = 0, x2 = 0, x3 = 0;
#define PUBLISH(V) do { pub = max(pub, (unsigned)(V)); lds_wr32(full, pub); } while (0)
#pragma unroll
        for (int kb = 0; kb < 16; ++kb) {
            if ((kb & 3) == 0) {
                bool ok = false;
                if (kb) {
                    asm volatile("s_waitcnt vmcnt(11)" : "+v"(pf)::"memory");
                    PUBLISH(kb - 1);
                    ok = __all(pf == tag);
                    if (!ok) {
                        asm volatile("s_waitcnt vmcnt(0)" ::: "memory");
                        PUBLISH(kb);
                    }
                }
                if (!ok) {
                    if (lw == 0) {
                        SPIN {
                            const u32x4 v = lds_rd128(ctl + CT_ARR);
                            if ((unsigned)__builtin_amdgcn_readfirstlane(v[2]) > (unsigned)(kb >> 2)) break;
                            __builtin_amdgcn_s_sleep(1);
                        }
                        if (PIPE && kb == 0) {
#define PISS(X) asm volatile("global_load_dwordx2 %0, %1, off sc1" : "=v"(X) : "v"(fl) : "memory")
#define PCHK(X) asm volatile("s_waitcnt vmcnt(3)" : "+v"(X)::"memory"); if (__all(X == tag)) break; PISS(X); __builtin_amdgcn_s_sleep(PGAP)
                            PISS(x0); __builtin_amdgcn_s_sleep(PGAP);
                            PISS(x1); __builtin_amdgcn_s_sleep(PGAP);
                            PISS(x2); __builtin_amdgcn_s_sleep(PGAP);
                            PISS(x3);
                            SPIN {
                                PCHK(x0);
                                PCHK(x1);
                                PCHK(x2);
                                PCHK(x3);
                            }
                        } else
                        SPIN {
                            u64 x;
                            asm volatile("global_load_dwordx2 %0, %1, off sc1\n\ts_waitcnt vmcnt(0)" : "=v"(x) : "v"(fl + (kb >> 2) * 32 * FSTR) : "memory");
                            if (__all(x == tag)) break;
                            __builtin_amdgcn_s_sleep(PSLEEP);
                        }
                    } else {
                        SPIN {
                            const u32x4 v = lds_rd128(ctl + CT_ARR);
                            if ((unsigned)__builtin_amdgcn_readfirstlane(v[3]) > (unsigned)(kb >> 2)) break;
                            __builtin_amdgcn_s_sleep(1);
                        }
                    }
                }
                if (lw == 0) lds_wr32(ctl + CT_PH2, (kb >> 2) + 1);
                TS(2 + (kb >> 2));
            }
            if (kb >= NSTAGE && kfree <= (unsigned)(kb - NSTAGE)) {
                u32x4 v = lds_rd128(ctl + CT_FREE);
                kfree = __builtin_amdgcn_readfirstlane(min(min(v[0], v[1]), min(v[2], v[3])));
                if (kfree <= (unsigned)(kb - NSTAGE)) {
                    if (LAG > 1) {
                        asm volatile("s_waitcnt vmcnt(11)" ::: "memory");
                        PUBLISH(kb - 1);
                    }
                    asm volatile("s_waitcnt vmcnt(0)" ::: "memory");
                    PUBLISH(kb);
                    SPIN {
                        v = lds_rd128(ctl + CT_FREE);
                        kfree = __builtin_amdgcn_readfirstlane(min(min(v[0], v[1]), min(v[2], v[3])));
                        if (kfree > (unsigned)(kb - NSTAGE)) break;
                        __builtin_amdgcn_s_sleep(1);
                    }
                }
            }
            LAS uint8_t* base = (LAS uint8_t*)(uintptr_t)(smem_base + (kb % NSTAGE) * STAGE_BYTES + lw * 1024);
#pragma unroll
            for (int i = 0; i < 11; ++i) {
                const int so = i < 4 ? soA + i * 32768 + kb * 1024 : soB + kb * B_STAGE + (i - 4) * 2048;
                __builtin_amdgcn_raw_ptr_buffer_load_lds(rsL, base + i * 2048, 16, lane * 16, so, 0, AUX_SC1);
            }
            if (kb == 15 && lw == 0) lds_wr32(ctl + CT_ARR + 4, 1u);
            if (kb >= LAG) {
                if ((kb & 3) == 3 && kb < 12) asm volatile("s_waitcnt vmcnt(%0)" ::"n"(11 * LAG + 1) : "memory");
                else asm volatile("s_waitcnt vmcnt(%0)" ::"n"(11 * LAG) : "memory");
                PUBLISH(kb - LAG + 1);
                if (kb == LAG) asm volatile("" : "+v"(x0), "+v"(x1), "+v"(x2), "+v"(x3));
            }
            if ((kb & 3) == 2 && kb < 12)
                asm volatile("global_load_dwordx2 %0, %1, off sc1" : "=v"(pf) : "v"(fl + ((kb >> 2) + 1) * 32 * FSTR) : "memory");
        }
        asm volatile("s_waitcnt vmcnt(11)" ::: "memory");
        PUBLISH(15);
        asm volatile("s_waitcnt vmcnt(0)" ::: "memory");
        PUBLISH(16);
        TS(7);
        asm volatile("s_waitcnt lgkmcnt(0)" ::: "memory");
        __builtin_amdgcn_s_barrier();
    } else if (wid >= 4) {
        if (wid == 4) {
            SPIN {
                const u32x4 v = lds_rd128(ctl + CT_ARR);
                if (__builtin_amdgcn_readfirstlane(v[1]) != 0) break;
                __builtin_amdgcn_s_sleep(2);
            }
            const __amdgpu_buffer_rsrc_t rsC = __builtin_amdgcn_make_buffer_rsrc((void*)(ws + WS_CP), 0, 16 * 16 * 512, RSRC_W3);
            u32x4 cp[8];
#pragma unroll
            for (int x = 0; x < 8; ++x)
                cp[x] = __builtin_amdgcn_raw_buffer_load_b128(rsC, lane * 16, tn * 8192 + x * 1024, AUX_SC1);
            const int nzx = -zero_x[0];
            int s[8];
#pragma unroll
            for (int d = 0; d < 4; ++d) {
                int lo = 0, hi = 0;
#pragma unroll
                for (int x = 0; x < 8; ++x) {
                    const unsigned w_ = cp[x][d];
                    lo += (int)(short)(w_ & 0xffffu);
                    hi += (int)w_ >> 16;
                }
                s[2 * d] = lo + __shfl_xor(lo, 32);
                s[2 * d + 1] = hi + __shfl_xor(hi, 32);
            }
            if (lane < 28) {
                *(LAS i32x4*)(uintptr_t)(smem_base + OFF_CORR + lane * 32) = (i32x4){nzx * s[0], nzx * s[1], nzx * s[2], nzx * s[3]};
                *(LAS i32x4*)(uintptr_t)(smem_base + OFF_CORR + lane * 32 + 16) = (i32x4){nzx * s[4], nzx * s[5], nzx * s[6], nzx * s[7]};
            }
        }
        TS(8);
        if (bid == 0 && wid == 5) {
            SPIN {
                bool ok = true;
#pragma unroll
                for (int i = 0; i < 4; ++i)
                    ok &= __hip_atomic_load(flags + (3 * 32 + i * 4 + (lane >> 4)) * FSTR + (lane & 15) * FSUB, __ATOMIC_RELAXED, __HIP_MEMORY_SCOPE_AGENT) == tag;
                if (__all(ok)) break;
                __builtin_amdgcn_s_sleep(8);
            }
            if (lane == 0) __hip_atomic_store((GAS u64*)(ws + WS_EPOCH), tag, __ATOMIC_RELAXED, __HIP_MEMORY_SCOPE_AGENT);
        }
        asm volatile("s_waitcnt lgkmcnt(0)" ::: "memory");
        __builtin_amdgcn_s_barrier();
    } else {
        const int wm = wid >> 1, wn = wid & 1;
        i32x4 acc[4][7];
        i32x4 af[2][4], bfr[2][7];
#pragma unroll
        for (int i = 0; i < 4; ++i)
#pragma unroll
            for (int j = 0; j < 7; ++j) {
                acc[i][j] = (i32x4){0, 0, 0, 0};
                asm volatile("" : "+v"(acc[i][j]));
            }
        const unsigned fA = smem_base + wm * 4096 + lane * 16;
        const unsigned fB = smem_base + A_STAGE + (lane >> 4) * UNIT + wn * 1792 + (lane & 15) * 16;
        const unsigned freew = ctl + CT_FREE + wid * 4;
        unsigned known = 0;
#define DSR(DST, ADDR, OFF) asm volatile("ds_read_b128 %0, %1 offset:%2" : "=v"(DST) : "v"(ADDR), "n"(OFF) : "memory")
#define MF(I, J, SET) asm volatile("v_mfma_i32_16x16x64_i8 %0, %1, %2, %0" : "+v"(acc[I][J]) : "v"(bfr[SET][J]), "v"(af[SET][I]))
#define NEED(X)                                                                                                \
    do {                                                                                                       \
        if (__builtin_expect(known <= (unsigned)(X), 0)) {                                                                          \
            SPIN {                                                                                         \
                const u32x4 v_ = lds_rd128(ctl + CT_FULL);                                                     \
                known = __builtin_amdgcn_readfirstlane(min(v_[0], v_[1]));                                     \
                if (known > (unsigned)(X)) break;                                                              \
                __builtin_amdgcn_s_sleep(1);                                                                   \
            }                                                                                                  \
        }                                                                                                      \
    } while (0)
#define STEP(SET, KB)                                                                        \
    do {                                                                                     \
        MF(0, 0, SET);                                                                       \
        if ((KB) + 1 < 16) {                                                                 \
            NEED((KB) + 1);                                                                  \
            __builtin_amdgcn_sched_barrier(0);                                               \
            const unsigned aA_ = fA + (((KB) + 1) % NSTAGE) * STAGE_BYTES;                   \
            const unsigned aB_ = fB + (((KB) + 1) % NSTAGE) * STAGE_BYTES;                   \
            MF(1, 0, SET);                                                                   \
            DSR(bfr[(SET) ^ 1][0], aB_, 0);       MF(2, 0, SET);                             \
            DSR(af[(SET) ^ 1][0], aA_, 0);        MF(3, 0, SET);                             \
            DSR(af[(SET) ^ 1][1], aA_, 1024);     MF(0, 1, SET);                             \
            DSR(af[(SET) ^ 1][2], aA_, 2048);     MF(1, 1, SET);                             \
            DSR(af[(SET) ^ 1][3], aA_, 3072);     MF(2, 1, SET);                             \
            DSR(bfr[(SET) ^ 1][1], aB_, 256);     MF(3, 1, SET);                             \
            DSR(bfr[(SET) ^ 1][2], aB_, 512);     MF(0, 2, SET);                             \
            DSR(bfr[(SET) ^ 1][3], aB_, 768);     MF(1, 2, SET);                             \
            DSR(bfr[(SET) ^ 1][4], aB_, 1024);    MF(2, 2, SET);                             \
            DSR(bfr[(SET) ^ 1][5], aB_, 1280);    MF(3, 2, SET);                             \
            DSR(bfr[(SET) ^ 1][6], aB_, 1536);                                               \
            MF(0, 3, SET); MF(1, 3, SET); MF(2, 3, SET); MF(3, 3, SET);                      \
            MF(0, 4, SET); MF(1, 4, SET);                                                    \
            if ((KB) + 2 < 16) asm volatile("ds_read_b64 %0, %1" : "=v"(fv) : "v"(fullv) : "memory"); \
            MF(2, 4, SET); MF(3, 4, SET);                                                    \
            MF(0, 5, SET); MF(1, 5, SET); MF(2, 5, SET); MF(3, 5, SET);                      \
            MF(0, 6, SET);                                                                   \
            asm volatile("s_waitcnt lgkmcnt(0)" : "+v"(fv)::"memory");                       \
            if ((KB) + 1 + NSTAGE < 16) lds_wr32(freew, (KB) + 2);                           \
            MF(1, 6, SET);                                                                   \
            if ((KB) + 2 < 16) known = max(known, (unsigned)__builtin_amdgcn_readfirstlane(min(fv[0], fv[1]))); \
            __builtin_amdgcn_sched_barrier(0);                                               \
            MF(2, 6, SET); MF(3, 6, SET);                                                    \
        } else {                                                                             \
            MF(1, 0, SET); MF(2, 0, SET); MF(3, 0, SET);                                     \
            _Pragma("unroll") for (int j = 1; j < 7; ++j)                                    \
            _Pragma("unroll") for (int i = 0; i < 4; ++i) MF(i, j, SET);                     \
        }                                                                                    \
        __builtin_amdgcn_sched_barrier(0);                                                   \
    } while (0)
        u32x2 fv = {0, 0};
        unsigned fullv = ctl + CT_FULL;
        asm volatile("" : "+v"(fullv));
        NEED(0);
        TS(4);
#pragma unroll
        for (int j = 0; j < 7; ++j) DSR(bfr[0][j], fB, j * 256);
#pragma unroll
        for (int i = 0; i < 4; ++i) DSR(af[0][i], fA, i * 1024);
        asm volatile("s_waitcnt lgkmcnt(0)" ::: "memory");
        lds_wr32(freew, 1);
        __builtin_amdgcn_sched_barrier(0);
#pragma unroll
        for (int kb = 0; kb < 16; kb += 2) {
            STEP(0, kb);
            STEP(1, kb + 1);
            if (kb == 2) TS(5);
            if (kb == 6) TS(6);
            if (kb == 10) TS(7);
        }
        TS(8);
        __builtin_amdgcn_sched_barrier(0);
        __builtin_amdgcn_s_barrier();
        asm volatile("s_nop 7\n\ts_nop 7" ::: "memory");
#pragma unroll
        for (int j = 0; j < 7; ++j) asm volatile("" : "+v"(acc[0][j]), "+v"(acc[1][j]), "+v"(acc[2][j]), "+v"(acc[3][j]));
        LAS uint8_t* dst = (LAS uint8_t*)(uintptr_t)(smem_base + (wm * 64 + (lane & 15)) * ACC_PITCH + (wn * 112 + (lane >> 4) * 4) * 4);
#pragma unroll
        for (int j = 0; j < 7; ++j)
#pragma unroll
            for (int i = 0; i < 4; ++i) *reinterpret_cast<LAS i32x4*>(dst + i * 16 * ACC_PITCH + j * 64) = acc[i][j];
    }
    asm volatile("s_waitcnt lgkmcnt(0)" ::: "memory");
    __builtin_amdgcn_s_barrier();
    TS(9);
    if (tid < 448) {
        const int cc = tid % 28, r0 = tid / 28;
        const LAS uint8_t* sm = (const LAS uint8_t*)(uintptr_t)smem_base;
        const f32x4 s0 = *reinterpret_cast<const LAS f32x4*>(sm + OFF_SCALE + cc * 32), s1 = *reinterpret_cast<const LAS f32x4*>(sm + OFF_SCALE + cc * 32 + 16);
        const f32x4 b0 = *reinterpret_cast<const LAS f32x4*>(sm + OFF_BIAS + cc * 32), b1 = *reinterpret_cast<const LAS f32x4*>(sm + OFF_BIAS + cc * 32 + 16);
        const i32x4 c0 = *reinterpret_cast<const LAS i32x4*>(sm + OFF_CORR + cc * 32), c1 = *reinterpret_cast<const LAS i32x4*>(sm + OFF_CORR + cc * 32 + 16);
        const __amdgpu_buffer_rsrc_t rsO = __builtin_amdgcn_make_buffer_rsrc((void*)out, 0, M_ * N_ * 2, RSRC_W3);
        const unsigned ovo = (unsigned)(((tm * BM + r0) * N_ + tn * BN + cc * 8) * 2);
#pragma unroll
        for (int i = 0; i < 8; ++i) {
            const LAS uint8_t* src = sm + (r0 + 16 * i) * ACC_PITCH + cc * 8;
            i32x2 a[4];
#pragma unroll
            for (int c = 0; c < 4; ++c) a[c] = *reinterpret_cast<const LAS i32x2*>(src + c * 224);
            f32x2 v0 = {(float)(a[0][0] + c0[0]) * s0[0] + b0[0], (float)(a[1][0] + c0[1]) * s0[1] + b0[1]};
            f32x2 v1 = {(float)(a[2][0] + c0[2]) * s0[2] + b0[2], (float)(a[3][0] + c0[3]) * s0[3] + b0[3]};
            f32x2 v2 = {(float)(a[0][1] + c1[0]) * s1[0] + b1[0], (float)(a[1][1] + c1[1]) * s1[1] + b1[1]};
            f32x2 v3 = {(float)(a[2][1] + c1[2]) * s1[2] + b1[2], (float)(a[3][1] + c1[3]) * s1[3] + b1[3]};
            u32x4 o;
            o[0] = __builtin_bit_cast(unsigned, __builtin_convertvector(v0, bf16x2_t));
            o[1] = __builtin_bit_cast(unsigned, __builtin_convertvector(v1, bf16x2_t));
            o[2] = __builtin_bit_cast(unsigned, __builtin_convertvector(v2, bf16x2_t));
            o[3] = __builtin_bit_cast(unsigned, __builtin_convertvector(v3, bf16x2_t));
            __builtin_amdgcn_raw_buffer_store_b128(o, rsO, ovo + i * 16 * (N_ * 2), 0, OUT_AUX);
        }
    }
    TS(10);
}

extern "C" void kernel_launch(void* const* d_in, const int* in_sizes, int n_in,
                              void* d_out, int out_size, void* d_ws, size_t ws_size,
                              hipStream_t stream) {
    const int* qx = (const int*)d_in[0];
    const int* qw = (const int*)d_in[1];
    const __hip_bfloat16* scale_x = (const __hip_bfloat16*)d_in[2];
    const int* zero_x = (const int*)d_in[3];
    const __hip_bfloat16* scale_w = (const __hip_bfloat16*)d_in[4];
    const __hip_bfloat16* bias = (const __hip_bfloat16*)d_in[5];
    (void)hipFuncSetAttribute((const void*)fused_kernel, hipFuncAttributeMaxDynamicSharedMemorySize, SMEM_BYTES);
    fused_kernel<<<256, 512, SMEM_BYTES, stream>>>(qx, qw, scale_x, zero_x, scale_w, bias, (__hip_bfloat16*)d_out,
                                                   (uint8_t*)d_ws);
}
```

```cpp
#define PROF_TS
#define DIAG2 0
#include <hip/hip_runtime.h>
#include <hip/hip_bf16.h>
#include <stdint.h>

typedef int i32x4 __attribute__((ext_vector_type(4)));
typedef int i32x2 __attribute__((ext_vector_type(2)));
typedef unsigned int u32x4 __attribute__((ext_vector_type(4)));
typedef unsigned int u32x2 __attribute__((ext_vector_type(2)));
typedef float f32x2 __attribute__((ext_vector_type(2)));
typedef float f32x4 __attribute__((ext_vector_type(4)));
typedef short s16x2 __attribute__((ext_vector_type(2)));
typedef __bf16 bf16x2_t __attribute__((ext_vector_type(2)));
typedef unsigned long long u64;
#define GAS __attribute__((address_space(1)))
#define LAS __attribute__((address_space(3)))

constexpr int M_ = 2048, K_ = 1024, N_ = 3584;
constexpr int BM = 128, BN = 224;
constexpr int NSTAGE = 6;
constexpr int UNIT = 3584;
constexpr int A_STAGE = 8192, B_STAGE = 4 * UNIT;
constexpr int STAGE_BYTES = A_STAGE + B_STAGE;
constexpr int RING_BYTES = NSTAGE * STAGE_BYTES;
constexpr int ACC_PITCH = BN * 4 + 16;
constexpr int OFF_CTL = RING_BYTES;
constexpr int CT_FULL = 0, CT_FREE = 16, CT_TAG = 32, CT_ARR = 48, CT_GO = 56, CT_PH2 = 60;
constexpr int OFF_SCALE = OFF_CTL + 64;
constexpr int OFF_BIAS = OFF_SCALE + 896;
constexpr int OFF_CORR = OFF_BIAS + 896;
constexpr int OFF_TR = OFF_CORR + 896;
constexpr int SMEM_BYTES = OFF_TR + 2048;
static_assert(BM * ACC_PITCH <= RING_BYTES, "acc staging must fit in the ring");
constexpr int NPH = 4;
constexpr size_t WS_EPOCH = 0;
#define FSUB 16
#define FSTR 544
constexpr size_t WS_FLAGS = 8u << 20;
constexpr size_t WS_A8 = 65536;
constexpr size_t WS_B8 = WS_A8 + (size_t)M_ * K_;
constexpr size_t WS_CP = WS_B8 + (size_t)N_ * K_;
constexpr size_t WS_DBG = WS_CP + 16 * 64 * 512;
constexpr int AUX_SC1 = 16;
constexpr int RSRC_W3 = 0x00020000;
#define WB 4
#define WA 2
#define FLAGI 1
#define LAG 1
#define PIPE 0
#define PGAP 6
#define PSLEEP 4
#define OUT_AUX 16
#define RAW_AUX 2
#define GO_KB 1

__device__ __forceinline__ unsigned pack4(int a, int b, int c, int d) {
    const unsigned lo = __builtin_amdgcn_perm((unsigned)b, (unsigned)a, 0x0c0c0400u);
    const unsigned hi = __builtin_amdgcn_perm((unsigned)d, (unsigned)c, 0x0c0c0400u);
    return __builtin_amdgcn_perm(hi, lo, 0x05040100u);
}
__device__ __forceinline__ u32x4 lds_rd128(unsigned addr) {
    u32x4 v;
    asm volatile("ds_read_b128 %0, %1\n\ts_waitcnt lgkmcnt(0)" : "=v"(v) : "v"(addr) : "memory");
    return v;
}
__device__ __forceinline__ void lds_wr32_max(unsigned addr, unsigned val) {
    asm volatile("ds_max_u32 %0, %1" ::"v"(addr), "v"(val) : "memory");
}
__device__ __forceinline__ void lds_wr32(unsigned addr, unsigned val) {
    asm volatile("ds_write_b32 %0, %1" ::"v"(addr), "v"(val) : "memory");
}

#define SPIN for (;;)
#define TS(i) do { ts[i] = __builtin_amdgcn_s_memrealtime(); } while (0)

__global__ __launch_bounds__(512, 2) void fused_kernel(const int* __restrict__ qx, const int* __restrict__ qw,
                                                       const __hip_bfloat16* __restrict__ scale_x,
                                                       const int* __restrict__ zero_x,
                                                       const __hip_bfloat16* __restrict__ scale_w,
                                                       const __hip_bfloat16* __restrict__ bias,
                                                       __hip_bfloat16* __restrict__ out, uint8_t* __restrict__ ws) {
    extern __shared__ __attribute__((aligned(16))) uint8_t smem[];
    const int tid = threadIdx.x, lane = tid & 63;
    const int wid = __builtin_amdgcn_readfirstlane(tid >> 6);
    const int bid = blockIdx.x;
    const int tm = (bid & 1) * 8 + ((bid >> 3) & 7);
    const int tn = ((bid >> 1) & 3) * 4 + (bid >> 6);
    const int tmA = bid >> 4, subA = bid & 15;
    const int tnB = bid & 15, subB = bid >> 4;
    const unsigned smem_base = (unsigned)(uintptr_t)smem;
    const unsigned ctl = smem_base + OFF_CTL;
    u64 ts[16];
#pragma unroll
    for (int i = 0; i < 16; ++i) ts[i] = 0;
    TS(0);
    ts[12] = __builtin_amdgcn_s_memtime();
    GAS u64* flags = (GAS u64*)(ws + WS_FLAGS);
    u64 tag;
#define TAGWAIT()                                                                                                        \
    do {                                                                                                                 \
        asm volatile("s_load_dwordx2 %0, %1, 0x0\n\ts_waitcnt lgkmcnt(0)" : "=s"(tag) : "s"(ws + WS_EPOCH) : "memory"); \
        tag += 0x9E3779B97F4A7C15ull;                                                                                    \
    } while (0)

    const __amdgpu_buffer_rsrc_t rsF = __builtin_amdgcn_make_buffer_rsrc((void*)(ws + WS_FLAGS), 0, 128 * FSTR * 8, RSRC_W3);
    if (wid == 4) {
        const __amdgpu_buffer_rsrc_t rsW = __builtin_amdgcn_make_buffer_rsrc((void*)qw, 0, K_ * N_ * 4, RSRC_W3);
        const __amdgpu_buffer_rsrc_t rsB8 = __builtin_amdgcn_make_buffer_rsrc((void*)(ws + WS_B8), 0, N_ * K_ + 16 * 16 * 512, RSRC_W3);
        const int q = lane < 56 ? lane : lane - 8;
        const unsigned voff = (unsigned)((tnB * 224 + q * 4) * 4);
        const int soff0 = ((subB >> 3) * 128 + (subB & 7) * 4) * (N_ * 4);
        const unsigned vo0 = lane < 56 ? (unsigned)((tnB * 64 + subB) * UNIT + lane * 16) : 0x80000000u;
        const unsigned vf0 = lane == 0 ? (unsigned)(((16 + tnB) * FSTR + subB * FSUB) * 8) : 0x80000000u;
        i32x4 st[WB][4];
        auto ld = [&](int t) {
#pragma unroll
            for (int x = 0; x < 4; ++x)
                st[t % WB][x] = __builtin_bit_cast(i32x4, __builtin_amdgcn_raw_buffer_load_b128(rsW, voff, soff0 + ((t >> 2) * 256 + (t & 3) * 32 + x) * (N_ * 4), RAW_AUX));
        };
#pragma unroll
        for (int t = 0; t < WB; ++t) ld(t);
        __builtin_amdgcn_sched_barrier(0);
        TAGWAIT();
        __builtin_amdgcn_s_barrier();
        __builtin_amdgcn_sched_barrier(0);
        const u32x2 tg = {(unsigned)tag, (unsigned)(tag >> 32)};
        int cs[4] = {0, 0, 0, 0};
        u32x4 pk[4];
#pragma unroll
        for (int t = 0; t < 16; ++t) {
            const int p = t >> 2, i = t & 3;
#pragma unroll
            for (int c = 0; c < 4; ++c) {
                pk[c][i] = pack4(st[t % WB][0][c], st[t % WB][1][c], st[t % WB][2][c], st[t % WB][3][c]);
                cs[c] = __builtin_amdgcn_sdot4((int)pk[c][i], 0x01010101, cs[c], false);
            }
            if (i == 3) {
                if (p == 0) TS(1);
#pragma unroll
                for (int c = 0; c < 4; ++c) __builtin_amdgcn_raw_buffer_store_b128(pk[c], rsB8, vo0 + p * 16 * UNIT + c * 896, 0, AUX_SC1);
                if (t == 15) {
                    const u32x2 cp = {__builtin_amdgcn_perm((unsigned)cs[1], (unsigned)cs[0], 0x05040100u),
                                      __builtin_amdgcn_perm((unsigned)cs[3], (unsigned)cs[2], 0x05040100u)};
                    __builtin_amdgcn_raw_buffer_store_b64(cp, rsB8, (unsigned)(N_ * K_ + (tnB * 16 + subB) * 512 + lane * 8), 0, AUX_SC1);
                }
            }
            __builtin_amdgcn_sched_barrier(0);
            if (t + WB < 16) ld(t + WB);
            __builtin_amdgcn_sched_barrier(0);
            if (i == FLAGI && t >= 4) {
                const int pp = p - 1;
                const int last = t + WB < 16 ? t : 15 - WB;
                const int after = last - (4 * pp + 3) + 1;
                asm volatile("s_waitcnt vmcnt(%0)" ::"n"((after > 0 ? after : 0) * 4) : "memory");
                __builtin_amdgcn_raw_buffer_store_b64(tg, rsF, vf0 + pp * 32 * FSTR * 8, 0, AUX_SC1);
                lds_wr32(ctl + CT_GO, pp + 1);
                TS(2 + pp);
                __builtin_amdgcn_sched_barrier(0);
            }
            if (t == 15) {
                asm volatile("s_waitcnt vmcnt(0)" ::: "memory");
                __builtin_amdgcn_raw_buffer_store_b64(tg, rsF, vf0 + 3 * 32 * FSTR * 8, 0, AUX_SC1);
                lds_wr32(ctl + CT_GO, 4);
                TS(5);
            }
        }
    } else if (wid == 5) {
        const __amdgpu_buffer_rsrc_t rsX = __builtin_amdgcn_make_buffer_rsrc((void*)qx, 0, M_ * K_ * 4, RSRC_W3);
        const __amdgpu_buffer_rsrc_t rsA8 = __builtin_amdgcn_make_buffer_rsrc((void*)(ws + WS_A8), 0, M_ * K_, RSRC_W3);
        const int rb = tmA * 8 + (subA >> 1), kb0 = 2 * (subA & 1);
        const unsigned voff = (unsigned)(((rb * 16 + (lane >> 3)) * K_ + kb0 * 64 + (lane & 7) * 4) * 4);
        const unsigned vo0 = (unsigned)((rb * 16 + kb0) * 1024 + lane * 16);
        const unsigned vf0 = lane == 0 ? (unsigned)((tmA * FSTR + subA * FSUB) * 8) : 0x80000000u;
        const unsigned tw = smem_base + OFF_TR + (lane & 7) * 256 + (lane >> 3) * 16, tr = smem_base + OFF_TR + lane * 16;
        i32x4 v[WA][4];
        auto ld = [&](int t) {
#pragma unroll
            for (int x = 0; x < 4; ++x)
                v[t % WA][x] = __builtin_bit_cast(i32x4, __builtin_amdgcn_raw_buffer_load_b128(rsX, voff + (t >> 1) * 1024 + x * 128, (t & 1) * (8 * K_ * 4), RAW_AUX));
        };
#pragma unroll
        for (int t = 0; t < WA; ++t) ld(t);
        __builtin_amdgcn_sched_barrier(0);
        TAGWAIT();
        __builtin_amdgcn_s_barrier();
        __builtin_amdgcn_sched_barrier(0);
        const u32x2 tg = {(unsigned)tag, (unsigned)(tag >> 32)};
#pragma unroll
        for (int t = 0; t < 8; ++t) {
            const int p = t >> 1, h = t & 1;
            u32x4 o;
#pragma unroll
            for (int x = 0; x < 4; ++x) o[x] = pack4(v[t % WA][x][0], v[t % WA][x][1], v[t % WA][x][2], v[t % WA][x][3]);
            asm volatile("ds_write_b128 %0, %1 offset:%2" ::"v"(tw), "v"(o), "n"(h * 128) : "memory");
            if (h) {
                u32x4 o0, o1;
                asm volatile("ds_read_b128 %0, %2\n\tds_read_b128 %1, %2 offset:1024\n\ts_waitcnt lgkmcnt(0)" : "=&v"(o0), "=&v"(o1) : "v"(tr) : "memory");
                if (t == 1) TS(1);
                __builtin_amdgcn_raw_buffer_store_b128(o0, rsA8, vo0 + p * 4096, 0, AUX_SC1);
                __builtin_amdgcn_raw_buffer_store_b128(o1, rsA8, vo0 + p * 4096 + 1024, 0, AUX_SC1);
            }
            __builtin_amdgcn_sched_barrier(0);
            if (t + WA < 8) ld(t + WA);
            __builtin_amdgcn_sched_barrier(0);
            if (h == 0 && t >= 2) {
                const int last = t + WA < 8 ? t : 7 - WA;
                const int after = last - (t - 1) + 1;
                asm volatile("s_waitcnt vmcnt(%0)" ::"n"((after > 0 ? after : 0) * 4) : "memory");
                __builtin_amdgcn_raw_buffer_store_b64(tg, rsF, vf0 + (p - 1) * 32 * FSTR * 8, 0, AUX_SC1);
                TS(1 + p);
                __builtin_amdgcn_sched_barrier(0);
            }
            if (t == 7) {
                asm volatile("s_waitcnt vmcnt(0)" ::: "memory");
                __builtin_amdgcn_raw_buffer_store_b64(tg, rsF, vf0 + 3 * 32 * FSTR * 8, 0, AUX_SC1);
                TS(5);
            }
        }
    } else {
        if (wid == 6 && lane < 16) lds_wr32(ctl + lane * 4, 0u);
        TAGWAIT();
        __builtin_amdgcn_s_barrier();
    }

    if (wid >= 6) {
        const int lw = wid - 6;
        if (lw == 1) {
            const int l4 = lane < 56 ? lane : 0;
            const u32x2 sw4 = reinterpret_cast<const u32x2*>(scale_w)[tn * (BN / 4) + l4];
            const u32x2 bi4 = reinterpret_cast<const u32x2*>(bias)[tn * (BN / 4) + l4];
            const float sx = __uint_as_float((unsigned)reinterpret_cast<const unsigned short*>(scale_x)[0] << 16);
            if (lane < 56) {
                const f32x4 sc = {sx * __uint_as_float(sw4[0] << 16), sx * __uint_as_float(sw4[0] & 0xffff0000u),
                                  sx * __uint_as_float(sw4[1] << 16), sx * __uint_as_float(sw4[1] & 0xffff0000u)};
                const f32x4 bi = {__uint_as_float(bi4[0] << 16), __uint_as_float(bi4[0] & 0xffff0000u),
                                  __uint_as_float(bi4[1] << 16), __uint_as_float(bi4[1] & 0xffff0000u)};
                *(LAS f32x4*)(uintptr_t)(smem_base + OFF_SCALE + lane * 16) = sc;
                *(LAS f32x4*)(uintptr_t)(smem_base + OFF_BIAS + lane * 16) = bi;
            }
        }
        TS(1);
        GAS u64* fl = flags + ((lane & 16) ? 16 + tn : tm) * FSTR + (lane & 15) * FSUB;
        unsigned pub = 0;
        u64 pf = 0;
        const __amdgpu_buffer_rsrc_t rsL = __builtin_amdgcn_make_buffer_rsrc((void*)(ws + WS_A8), 0, (M_ + N_) * K_, RSRC_W3);
        const int soA = (tm * 8 + lw) * 16384, soB = M_ * K_ + tn * (64 * UNIT) + lw * 1024;
        const unsigned full = ctl + CT_FULL + lw * 4;
        unsigned kfree = 0;
        u64 x0 = 0, x1 = 0, x2 = 0, x3 = 0;
#define PUBLISH(V) do { pub = max(pub, (unsigned)(V)); lds_wr32(full, pub); } while (0)
#pragma unroll
        for (int kb = 0; kb < 16; ++kb) {
            if ((kb & 3) == 0) {
                bool ok = false;
                if (kb) {
                    asm volatile("s_waitcnt vmcnt(11)" : "+v"(pf)::"memory");
                    PUBLISH(kb - 1);
                    ok = __all(pf == tag);
                    if (!ok) {
                        asm volatile("s_waitcnt vmcnt(0)" ::: "memory");
                        PUBLISH(kb);
                    }
                }
                if (!ok) {
                    if (lw == 0) {
                        SPIN {
                            const u32x4 v = lds_rd128(ctl + CT_ARR);
                            if ((unsigned)__builtin_amdgcn_readfirstlane(v[2]) > (unsigned)(kb >> 2)) break;
                            __builtin_amdgcn_s_sleep(1);
                        }
                        if (PIPE && kb == 0) {
#define PISS(X) asm volatile("global_load_dwordx2 %0, %1, off sc1" : "=v"(X) : "v"(fl) : "memory")
#define PCHK(X) asm volatile("s_waitcnt vmcnt(3)" : "+v"(X)::"memory"); if (__all(X == tag)) break; PISS(X); __builtin_amdgcn_s_sleep(PGAP)
                            PISS(x0); __builtin_amdgcn_s_sleep(PGAP);
                            PISS(x1); __builtin_amdgcn_s_sleep(PGAP);
                            PISS(x2); __builtin_amdgcn_s_sleep(PGAP);
                            PISS(x3);
                            SPIN {
                                PCHK(x0);
                                PCHK(x1);
                                PCHK(x2);
                                PCHK(x3);
                            }
                        } else
                        SPIN {
                            u64 x;
                            asm volatile("global_load_dwordx2 %0, %1, off sc1\n\ts_waitcnt vmcnt(0)" : "=v"(x) : "v"(fl + (kb >> 2) * 32 * FSTR) : "memory");
                            if (__all(x == tag)) break;
                            __builtin_amdgcn_s_sleep(PSLEEP);
                        }
                    } else {
                        SPIN {
                            const u32x4 v = lds_rd128(ctl + CT_ARR);
                            if ((unsigned)__builtin_amdgcn_readfirstlane(v[3]) > (unsigned)(kb >> 2)) break;
                            __builtin_amdgcn_s_sleep(1);
                        }
                    }
                }
                if (lw == 0) lds_wr32(ctl + CT_PH2, (kb >> 2) + 1);
                TS(2 + (kb >> 2));
            }
            if (kb >= NSTAGE && kfree <= (unsigned)(kb - NSTAGE)) {
                u32x4 v = lds_rd128(ctl + CT_FREE);
                kfree = __builtin_amdgcn_readfirstlane(min(min(v[0], v[1]), min(v[2], v[3])));
                if (kfree <= (unsigned)(kb - NSTAGE)) {
                    if (LAG > 1) {
                        asm volatile("s_waitcnt vmcnt(11)" ::: "memory");
                        PUBLISH(kb - 1);
                    }
                    asm volatile("s_waitcnt vmcnt(0)" ::: "memory");
                    PUBLISH(kb);
                    SPIN {
                        v = lds_rd128(ctl + CT_FREE);
                        kfree = __builtin_amdgcn_readfirstlane(min(min(v[0], v[1]), min(v[2], v[3])));
                        if (kfree > (unsigned)(kb - NSTAGE)) break;
                        __builtin_amdgcn_s_sleep(1);
                    }
                }
            }
            LAS uint8_t* base = (LAS uint8_t*)(uintptr_t)(smem_base + (kb % NSTAGE) * STAGE_BYTES + lw * 1024);
#pragma unroll
            for (int i = 0; i < 11; ++i) {
                const int so = i < 4 ? soA + i * 32768 + kb * 1024 : soB + kb * B_STAGE + (i - 4) * 2048;
                __builtin_amdgcn_raw_ptr_buffer_load_lds(rsL, base + i * 2048, 16, lane * 16, so, 0, AUX_SC1);
            }
            if (kb == 15 && lw == 0) lds_wr32(ctl + CT_ARR + 4, 1u);
            if (kb >= LAG) {
                if ((kb & 3) == 3 && kb < 12) asm volatile("s_waitcnt vmcnt(%0)" ::"n"(11 * LAG + 1) : "memory");
                else asm volatile("s_waitcnt vmcnt(%0)" ::"n"(11 * LAG) : "memory");
                PUBLISH(kb - LAG + 1);
                if (kb == LAG) asm volatile("" : "+v"(x0), "+v"(x1), "+v"(x2), "+v"(x3));
            }
            if ((kb & 3) == 2 && kb < 12)
                asm volatile("global_load_dwordx2 %0, %1, off sc1" : "=v"(pf) : "v"(fl + ((kb >> 2) + 1) * 32 * FSTR) : "memory");
        }
        asm volatile("s_waitcnt vmcnt(11)" ::: "memory");
        PUBLISH(15);
        asm volatile("s_waitcnt vmcnt(0)" ::: "memory");
        PUBLISH(16);
        TS(7);
        asm volatile("s_waitcnt lgkmcnt(0)" ::: "memory");
        __builtin_amdgcn_s_barrier();
    } else if (wid >= 4) {
        if (wid == 4) {
            SPIN {
                const u32x4 v = lds_rd128(ctl + CT_ARR);
                if (__builtin_amdgcn_readfirstlane(v[1]) != 0) break;
                __builtin_amdgcn_s_sleep(2);
            }
            const __amdgpu_buffer_rsrc_t rsC = __builtin_amdgcn_make_buffer_rsrc((void*)(ws + WS_CP), 0, 16 * 16 * 512, RSRC_W3);
            u32x4 cp[8];
#pragma unroll
            for (int x = 0; x < 8; ++x)
                cp[x] = __builtin_amdgcn_raw_buffer_load_b128(rsC, lane * 16, tn * 8192 + x * 1024, AUX_SC1);
            const int nzx = -zero_x[0];
            int s[8];
#pragma unroll
            for (int d = 0; d < 4; ++d) {
                int lo = 0, hi = 0;
#pragma unroll
                for (int x = 0; x < 8; ++x) {
                    const unsigned w_ = cp[x][d];
                    lo += (int)(short)(w_ & 0xffffu);
                    hi += (int)w_ >> 16;
                }
                s[2 * d] = lo + __shfl_xor(lo, 32);
                s[2 * d + 1] = hi + __shfl_xor(hi, 32);
            }
            if (lane < 28) {
                *(LAS i32x4*)(uintptr_t)(smem_base + OFF_CORR + lane * 32) = (i32x4){nzx * s[0], nzx * s[1], nzx * s[2], nzx * s[3]};
                *(LAS i32x4*)(uintptr_t)(smem_base + OFF_CORR + lane * 32 + 16) = (i32x4){nzx * s[4], nzx * s[5], nzx * s[6], nzx * s[7]};
            }
        }
        TS(8);
        if (bid == 0 && wid == 5) {
            SPIN {
                bool ok = true;
#pragma unroll
                for (int i = 0; i < 4; ++i)
                    ok &= __hip_atomic_load(flags + (3 * 32 + i * 4 + (lane >> 4)) * FSTR + (lane & 15) * FSUB, __ATOMIC_RELAXED, __HIP_MEMORY_SCOPE_AGENT) == tag;
                if (__all(ok)) break;
                __builtin_amdgcn_s_sleep(8);
            }
            if (lane == 0) __hip_atomic_store((GAS u64*)(ws + WS_EPOCH), tag, __ATOMIC_RELAXED, __HIP_MEMORY_SCOPE_AGENT);
        }
        asm volatile("s_waitcnt lgkmcnt(0)" ::: "memory");
        __builtin_amdgcn_s_barrier();
    } else {
        const int wm = wid >> 1, wn = wid & 1;
        i32x4 acc[4][7];
        i32x4 af[2][4], bfr[2][7];
#pragma unroll
        for (int i = 0; i < 4; ++i)
#pragma unroll
            for (int j = 0; j < 7; ++j) {
                acc[i][j] = (i32x4){0, 0, 0, 0};
                asm volatile("" : "+v"(acc[i][j]));
            }
        const unsigned fA = smem_base + wm * 4096 + lane * 16;
        const unsigned fB = smem_base + A_STAGE + (lane >> 4) * UNIT + wn * 1792 + (lane & 15) * 16;
        const unsigned freew = ctl + CT_FREE + wid * 4;
        unsigned known = 0;
#define DSR(DST, ADDR, OFF) asm volatile("ds_read_b128 %0, %1 offset:%2" : "=v"(DST) : "v"(ADDR), "n"(OFF) : "memory")
#define MF(I, J, SET) asm volatile("v_mfma_i32_16x16x64_i8 %0, %1, %2, %0" : "+v"(acc[I][J]) : "v"(bfr[SET][J]), "v"(af[SET][I]))
#define NEED(X)                                                                                                \
    do {                                                                                                       \
        if (__builtin_expect(known <= (unsigned)(X), 0)) {                                                                          \
            SPIN {                                                                                         \
                const u32x4 v_ = lds_rd128(ctl + CT_FULL);                                                     \
                known = __builtin_amdgcn_readfirstlane(min(v_[0], v_[1]));                                     \
                if (known > (unsigned)(X)) break;                                                              \
                __builtin_amdgcn_s_sleep(1);                                                                   \
            }                                                                                                  \
        }                                                                                                      \
    } while (0)
#define STEP(SET, KB)                                                                        \
    do {                                                                                     \
        MF(0, 0, SET);                                                                       \
        if ((KB) + 1 < 16) {                                                                 \
            NEED((KB) + 1);                                                                  \
            __builtin_amdgcn_sched_barrier(0);                                               \
            const unsigned aA_ = fA + (((KB) + 1) % NSTAGE) * STAGE_BYTES;                   \
            const unsigned aB_ = fB + (((KB) + 1) % NSTAGE) * STAGE_BYTES;                   \
            MF(1, 0, SET);                                                                   \
            DSR(bfr[(SET) ^ 1][0], aB_, 0);       MF(2, 0, SET);                             \
            DSR(af[(SET) ^ 1][0], aA_, 0);        MF(3, 0, SET);                             \
            DSR(af[(SET) ^ 1][1], aA_, 1024);     MF(0, 1, SET);                             \
            DSR(af[(SET) ^ 1][2], aA_, 2048);     MF(1, 1, SET);                             \
            DSR(af[(SET) ^ 1][3], aA_, 3072);     MF(2, 1, SET);                             \
            DSR(bfr[(SET) ^ 1][1], aB_, 256);     MF(3, 1, SET);                             \
            DSR(bfr[(SET) ^ 1][2], aB_, 512);     MF(0, 2, SET);                             \
            DSR(bfr[(SET) ^ 1][3], aB_, 768);     MF(1, 2, SET);                             \
            DSR(bfr[(SET) ^ 1][4], aB_, 1024);    MF(2, 2, SET);                             \
            DSR(bfr[(SET) ^ 1][5], aB_, 1280);    MF(3, 2, SET);                             \
            DSR(bfr[(SET) ^ 1][6], aB_, 1536);                                               \
            MF(0, 3, SET); MF(1, 3, SET); MF(2, 3, SET); MF(3, 3, SET);                      \
            MF(0, 4, SET); MF(1, 4, SET);                                                    \
            if ((KB) + 2 < 16) asm volatile("ds_read_b64 %0, %1" : "=v"(fv) : "v"(fullv) : "memory"); \
            MF(2, 4, SET); MF(3, 4, SET);                                                    \
            MF(0, 5, SET); MF(1, 5, SET); MF(2, 5, SET); MF(3, 5, SET);                      \
            MF(0, 6, SET);                                                                   \
            asm volatile("s_waitcnt lgkmcnt(0)" : "+v"(fv)::"memory");                       \
            if ((KB) + 1 + NSTAGE < 16) lds_wr32(freew, (KB) + 2);                           \
            MF(1, 6, SET);                                                                   \
            if ((KB) + 2 < 16) known = max(known, (unsigned)__builtin_amdgcn_readfirstlane(min(fv[0], fv[1]))); \
            __builtin_amdgcn_sched_barrier(0);                                               \
            MF(2, 6, SET); MF(3, 6, SET);                                                    \
        } else {                                                                             \
            MF(1, 0, SET); MF(2, 0, SET); MF(3, 0, SET);                                     \
            _Pragma("unroll") for (int j = 1; j < 7; ++j)                                    \
            _Pragma("unroll") for (int i = 0; i < 4; ++i) MF(i, j, SET);                     \
        }                                                                                    \
        __builtin_amdgcn_sched_barrier(0);                                                   \
    } while (0)
        u32x2 fv = {0, 0};
        unsigned fullv = ctl + CT_FULL;
        asm volatile("" : "+v"(fullv));
        NEED(0);
        TS(4);
#pragma unroll
        for (int j = 0; j < 7; ++j) DSR(bfr[0][j], fB, j * 256);
#pragma unroll
        for (int i = 0; i < 4; ++i) DSR(af[0][i], fA, i * 1024);
        asm volatile("s_waitcnt lgkmcnt(0)" ::: "memory");
        lds_wr32(freew, 1);
        __builtin_amdgcn_sched_barrier(0);
#pragma unroll
        for (int kb = 0; kb < 16; kb += 2) {
            STEP(0, kb);
            STEP(1, kb + 1);
            if (kb == 2) TS(5);
            if (kb == 6) TS(6);
            if (kb == 10) TS(7);
        }
        TS(8);
        __builtin_amdgcn_sched_barrier(0);
        __builtin_amdgcn_s_barrier();
        asm volatile("s_nop 7\n\ts_nop 7" ::: "memory");
#pragma unroll
        for (int j = 0; j < 7; ++j) asm volatile("" : "+v"(acc[0][j]), "+v"(acc[1][j]), "+v"(acc[2][j]), "+v"(acc[3][j]));
        LAS uint8_t* dst = (LAS uint8_t*)(uintptr_t)(smem_base + (wm * 64 + (lane & 15)) * ACC_PITCH + (wn * 112 + (lane >> 4) * 4) * 4);
#pragma unroll
        for (int j = 0; j < 7; ++j)
#pragma unroll
            for (int i = 0; i < 4; ++i) *reinterpret_cast<LAS i32x4*>(dst + i * 16 * ACC_PITCH + j * 64) = acc[i][j];
    }
    asm volatile("s_waitcnt lgkmcnt(0)" ::: "memory");
    __builtin_amdgcn_s_barrier();
    TS(9);
    if (tid < 448) {
        const int cc = tid % 28, r0 = tid / 28;
        const LAS uint8_t* sm = (const LAS uint8_t*)(uintptr_t)smem_base;
        const f32x4 s0 = *reinterpret_cast<const LAS f32x4*>(sm + OFF_SCALE + cc * 32), s1 = *reinterpret_cast<const LAS f32x4*>(sm + OFF_SCALE + cc * 32 + 16);
        const f32x4 b0 = *reinterpret_cast<const LAS f32x4*>(sm + OFF_BIAS + cc * 32), b1 = *reinterpret_cast<const LAS f32x4*>(sm + OFF_BIAS + cc * 32 + 16);
        const i32x4 c0 = *reinterpret_cast<const LAS i32x4*>(sm + OFF_CORR + cc * 32), c1 = *reinterpret_cast<const LAS i32x4*>(sm + OFF_CORR + cc * 32 + 16);
        const __amdgpu_buffer_rsrc_t rsO = __builtin_amdgcn_make_buffer_rsrc((void*)out, 0, M_ * N_ * 2, RSRC_W3);
        const unsigned ovo = (unsigned)(((tm * BM + r0) * N_ + tn * BN + cc * 8) * 2);
#pragma unroll
        for (int i = 0; i < 8; ++i) {
            const LAS uint8_t* src = sm + (r0 + 16 * i) * ACC_PITCH + cc * 8;
            i32x2 a[4];
#pragma unroll
            for (int c = 0; c < 4; ++c) a[c] = *reinterpret_cast<const LAS i32x2*>(src + c * 224);
            f32x2 v0 = {(float)(a[0][0] + c0[0]) * s0[0] + b0[0], (float)(a[1][0] + c0[1]) * s0[1] + b0[1]};
            f32x2 v1 = {(float)(a[2][0] + c0[2]) * s0[2] + b0[2], (float)(a[3][0] + c0[3]) * s0[3] + b0[3]};
            f32x2 v2 = {(float)(a[0][1] + c1[0]) * s1[0] + b1[0], (float)(a[1][1] + c1[1]) * s1[1] + b1[1]};
            f32x2 v3 = {(float)(a[2][1] + c1[2]) * s1[2] + b1[2], (float)(a[3][1] + c1[3]) * s1[3] + b1[3]};
            u32x4 o;
            o[0] = __builtin_bit_cast(unsigned, __builtin_convertvector(v0, bf16x2_t));
            o[1] = __builtin_bit_cast(unsigned, __builtin_convertvector(v1, bf16x2_t));
            o[2] = __builtin_bit_cast(unsigned, __builtin_convertvector(v2, bf16x2_t));
            o[3] = __builtin_bit_cast(unsigned, __builtin_convertvector(v3, bf16x2_t));
            __builtin_amdgcn_raw_buffer_store_b128(o, rsO, ovo + i * 16 * (N_ * 2), 0, OUT_AUX);
        }
    }
    TS(10);
    asm volatile("s_waitcnt vmcnt(0)" ::: "memory");
    TS(11);
    ts[13] = __builtin_amdgcn_s_memtime();
    if (wid == (DIAG2 / 16) && (DIAG2 % 16) > 0 && (bid % 33) == 1) {
        const u64 d = (ts[DIAG2 % 16] - ts[0]) * 5;
        const u64 t = __builtin_amdgcn_s_memrealtime();
        while (__builtin_amdgcn_s_memrealtime() - t < d) __builtin_amdgcn_s_sleep(4);
    }
}

extern "C" void kernel_launch(void* const* d_in, const int* in_sizes, int n_in,
                              void* d_out, int out_size, void* d_ws, size_t ws_size,
                              hipStream_t stream) {
    const int* qx = (const int*)d_in[0];
    const int* qw = (const int*)d_in[1];
    const __hip_bfloat16* scale_x = (const __hip_bfloat16*)d_in[2];
    const int* zero_x = (const int*)d_in[3];
    const __hip_bfloat16* scale_w = (const __hip_bfloat16*)d_in[4];
    const __hip_bfloat16* bias = (const __hip_bfloat16*)d_in[5];
    (void)hipFuncSetAttribute((const void*)fused_kernel, hipFuncAttributeMaxDynamicSharedMemorySize, SMEM_BYTES);
    fused_kernel<<<256, 512, SMEM_BYTES, stream>>>(qx, qw, scale_x, zero_x, scale_w, bias, (__hip_bfloat16*)d_out,
                                                   (uint8_t*)d_ws);
}
```

```cpp
#include <hip/hip_runtime.h>
#include <hip/hip_bf16.h>
#include <stdint.h>

typedef int i32x4 __attribute__((ext_vector_type(4)));
typedef int i32x2 __attribute__((ext_vector_type(2)));
typedef unsigned int u32x4 __attribute__((ext_vector_type(4)));
typedef unsigned int u32x2 __attribute__((ext_vector_type(2)));
typedef float f32x2 __attribute__((ext_vector_type(2)));
typedef float f32x4 __attribute__((ext_vector_type(4)));
typedef short s16x2 __attribute__((ext_vector_type(2)));
typedef __bf16 bf16x2_t __attribute__((ext_vector_type(2)));
typedef unsigned long long u64;
#define GAS __attribute__((address_space(1)))
#define LAS __attribute__((address_space(3)))

constexpr int M_ = 2048, K_ = 1024, N_ = 3584;
constexpr int BM = 128, BN = 224;
constexpr int NSTAGE = 6;
constexpr int UNIT = 3584;
constexpr int A_STAGE = 8192, B_STAGE = 4 * UNIT;
constexpr int STAGE_BYTES = A_STAGE + B_STAGE;
constexpr int RING_BYTES = NSTAGE * STAGE_BYTES;
constexpr int ACC_PITCH = BN * 4 + 16;
constexpr int OFF_CTL = RING_BYTES;
constexpr int CT_FULL = 0, CT_FREE = 16, CT_TAG = 32, CT_ARR = 48, CT_GO = 56, CT_PH2 = 60;
constexpr int OFF_SCALE = OFF_CTL + 64;
constexpr int OFF_BIAS = OFF_SCALE + 896;
constexpr int OFF_CORR = OFF_BIAS + 896;
constexpr int OFF_TR = OFF_CORR + 896;
constexpr int SMEM_BYTES = OFF_TR + 2048;
static_assert(BM * ACC_PITCH <= RING_BYTES, "acc staging must fit in the ring");
constexpr int NPH = 4;
constexpr size_t WS_EPOCH = 0;
#define FSUB 16
#define FSTR 544
constexpr size_t WS_FLAGS = 8u << 20;
constexpr size_t WS_A8 = 65536;
constexpr size_t WS_B8 = WS_A8 + (size_t)M_ * K_;
constexpr size_t WS_CP = WS_B8 + (size_t)N_ * K_;
constexpr size_t WS_DBG = WS_CP + 16 * 64 * 512;
constexpr int AUX_SC1 = 16;
constexpr int RSRC_W3 = 0x00020000;
#define WB 4
#define WA 2
#define FLAGI 1
#define CSKB 13
#define CFLAG 1
#define CGS 64
#define CSTR (4 * CGS)
#define CF_OFF (128 * FSTR * 8)
#define SPOLL 1
#define SGATE 1
#define SSLEEP 2
#define LAG 1
#define PIPE 0
#define PGAP 6
#define PSLEEP 4
#define OUT_AUX 18
#define RAW_AUX 2
#define GO_KB 1

__device__ __forceinline__ unsigned pack4(int a, int b, int c, int d) {
    const unsigned lo = __builtin_amdgcn_perm((unsigned)b, (unsigned)a, 0x0c0c0400u);
    const unsigned hi = __builtin_amdgcn_perm((unsigned)d, (unsigned)c, 0x0c0c0400u);
    return __builtin_amdgcn_perm(hi, lo, 0x05040100u);
}
__device__ __forceinline__ u32x4 lds_rd128(unsigned addr) {
    u32x4 v;
    asm volatile("ds_read_b128 %0, %1\n\ts_waitcnt lgkmcnt(0)" : "=v"(v) : "v"(addr) : "memory");
    return v;
}
__device__ __forceinline__ void lds_wr32_max(unsigned addr, unsigned val) {
    asm volatile("ds_max_u32 %0, %1" ::"v"(addr), "v"(val) : "memory");
}
__device__ __forceinline__ void lds_wr32(unsigned addr, unsigned val) {
    asm volatile("ds_write_b32 %0, %1" ::"v"(addr), "v"(val) : "memory");
}

#define SPIN for (;;)
#define TS(i) do { } while (0)

__global__ __launch_bounds__(512, 2) void fused_kernel(const int* __restrict__ qx, const int* __restrict__ qw,
                                                       const __hip_bfloat16* __restrict__ scale_x,
                                                       const int* __restrict__ zero_x,
                                                       const __hip_bfloat16* __restrict__ scale_w,
                                                       const __hip_bfloat16* __restrict__ bias,
                                                       __hip_bfloat16* __restrict__ out, uint8_t* __restrict__ ws) {
    extern __shared__ __attribute__((aligned(16))) uint8_t smem[];
    const int tid = threadIdx.x, lane = tid & 63;
    const int wid = __builtin_amdgcn_readfirstlane(tid >> 6);
    const int bid = blockIdx.x;
    const int tm = (bid & 1) * 8 + ((bid >> 3) & 7);
    const int tn = ((bid >> 1) & 3) * 4 + (bid >> 6);
    const int tmA = bid >> 4, subA = bid & 15;
    const int tnB = bid & 15, subB = bid >> 4;
    const unsigned smem_base = (unsigned)(uintptr_t)smem;
    const unsigned ctl = smem_base + OFF_CTL;
    GAS u64* flags = (GAS u64*)(ws + WS_FLAGS);
    u64 tag;
#define TAGWAIT()                                                                                                        \
    do {                                                                                                                 \
        asm volatile("s_load_dwordx2 %0, %1, 0x0\n\ts_waitcnt lgkmcnt(0)" : "=s"(tag) : "s"(ws + WS_EPOCH) : "memory"); \
        tag += 0x9E3779B97F4A7C15ull;                                                                                    \
    } while (0)

    const __amdgpu_buffer_rsrc_t rsF = __builtin_amdgcn_make_buffer_rsrc((void*)(ws + WS_FLAGS), 0, CF_OFF + 256 * CSTR, RSRC_W3);
    if (wid == 4) {
        const __amdgpu_buffer_rsrc_t rsW = __builtin_amdgcn_make_buffer_rsrc((void*)qw, 0, K_ * N_ * 4, RSRC_W3);
        const __amdgpu_buffer_rsrc_t rsB8 = __builtin_amdgcn_make_buffer_rsrc((void*)(ws + WS_B8), 0, N_ * K_ + 16 * 16 * 512, RSRC_W3);
        const int q = lane < 56 ? lane : lane - 8;
        const unsigned voff = (unsigned)((tnB * 224 + q * 4) * 4);
        const int soff0 = ((subB >> 3) * 128 + (subB & 7) * 4) * (N_ * 4);
        const unsigned vo0 = lane < 56 ? (unsigned)((tnB * 64 + subB) * UNIT + lane * 16) : 0x80000000u;
        const unsigned vf0 = lane == 0 ? (unsigned)(((16 + tnB) * FSTR + subB * FSUB) * 8) : 0x80000000u;
        i32x4 st[WB][4];
        auto ld = [&](int t) {
#pragma unroll
            for (int x = 0; x < 4; ++x)
                st[t % WB][x] = __builtin_bit_cast(i32x4, __builtin_amdgcn_raw_buffer_load_b128(rsW, voff, soff0 + ((t >> 2) * 256 + (t & 3) * 32 + x) * (N_ * 4), RAW_AUX));
        };
#pragma unroll
        for (int t = 0; t < WB; ++t) ld(t);
        __builtin_amdgcn_sched_barrier(0);
        TAGWAIT();
        __builtin_amdgcn_s_barrier();
        __builtin_amdgcn_sched_barrier(0);
        const u32x2 tg = {(unsigned)tag, (unsigned)(tag >> 32)};
        int cs[4] = {0, 0, 0, 0};
        u32x4 pk[4];
#pragma unroll
        for (int t = 0; t < 16; ++t) {
            const int p = t >> 2, i = t & 3;
#pragma unroll
            for (int c = 0; c < 4; ++c) {
                pk[c][i] = pack4(st[t % WB][0][c], st[t % WB][1][c], st[t % WB][2][c], st[t % WB][3][c]);
                cs[c] = __builtin_amdgcn_sdot4((int)pk[c][i], 0x01010101, cs[c], false);
            }
            if (i == 3) {
                if (p == 0) TS(1);
#pragma unroll
                for (int c = 0; c < 4; ++c) __builtin_amdgcn_raw_buffer_store_b128(pk[c], rsB8, (c < 2 ? vo0 : vo0 ^ 16u) + p * 16 * UNIT + c * 896, 0, AUX_SC1);
                if (t == 15) {
                    const u32x2 cp = {__builtin_amdgcn_perm((unsigned)cs[1], (unsigned)cs[0], 0x05040100u),
                                      __builtin_amdgcn_perm((unsigned)cs[3], (unsigned)cs[2], 0x05040100u)};
                    __builtin_amdgcn_raw_buffer_store_b64(cp, rsB8, (unsigned)(N_ * K_ + (tnB * 16 + subB) * 512 + lane * 8), 0, AUX_SC1);
                }
            }
            __builtin_amdgcn_sched_barrier(0);
            if (t + WB < 16) ld(t + WB);
            __builtin_amdgcn_sched_barrier(0);
            if (i == FLAGI && t >= 4) {
                const int pp = p - 1;
                const int last = t + WB < 16 ? t : 15 - WB;
                const int after = last - (4 * pp + 3) + 1;
                asm volatile("s_waitcnt vmcnt(%0)" ::"n"((after > 0 ? after : 0) * 4) : "memory");
                if (CFLAG && pp == 0)
                    __builtin_amdgcn_raw_buffer_store_b64(tg, rsF, lane < 16 ? (unsigned)(CF_OFF + (lane * 16 + tnB) * CSTR + ((subB >> 3) * 2 + 1) * CGS + (subB & 7) * 8) : 0x80000000u, 0, AUX_SC1);
                else
                __builtin_amdgcn_raw_buffer_store_b64(tg, rsF, vf0 + pp * 32 * FSTR * 8, 0, AUX_SC1);
                lds_wr32(ctl + CT_GO, pp + 1);
                TS(2 + pp);
                __builtin_amdgcn_sched_barrier(0);
            }
            if (t == 15) {
                asm volatile("s_waitcnt vmcnt(0)" ::: "memory");
                __builtin_amdgcn_raw_buffer_store_b64(tg, rsF, vf0 + 3 * 32 * FSTR * 8, 0, AUX_SC1);
                lds_wr32(ctl + CT_GO, 4);
                TS(5);
            }
        }
    } else if (wid == 5) {
        const __amdgpu_buffer_rsrc_t rsX = __builtin_amdgcn_make_buffer_rsrc((void*)qx, 0, M_ * K_ * 4, RSRC_W3);
        const __amdgpu_buffer_rsrc_t rsA8 = __builtin_amdgcn_make_buffer_rsrc((void*)(ws + WS_A8), 0, M_ * K_, RSRC_W3);
        const int rb = tmA * 8 + (subA >> 1), kb0 = 2 * (subA & 1);
        const unsigned voff = (unsigned)(((rb * 16 + (lane >> 3)) * K_ + kb0 * 64 + (lane & 7) * 4) * 4);
        const unsigned vo0 = (unsigned)((rb * 16 + kb0) * 1024 + lane * 16);
        const unsigned vf0 = lane == 0 ? (unsigned)((tmA * FSTR + ((subA & 1) * 8 + (subA >> 1)) * FSUB) * 8) : 0x80000000u;
        const unsigned tw = smem_base + OFF_TR + (lane & 7) * 256 + (lane >> 3) * 16, tr = smem_base + OFF_TR + lane * 16;
        i32x4 v[WA][4];
        auto ld = [&](int t) {
#pragma unroll
            for (int x = 0; x < 4; ++x)
                v[t % WA][x] = __builtin_bit_cast(i32x4, __builtin_amdgcn_raw_buffer_load_b128(rsX, voff + (t >> 1) * 1024 + x * 128, (t & 1) * (8 * K_ * 4), RAW_AUX));
        };
#pragma unroll
        for (int t = 0; t < WA; ++t) ld(t);
        __builtin_amdgcn_sched_barrier(0);
        TAGWAIT();
        __builtin_amdgcn_s_barrier();
        __builtin_amdgcn_sched_barrier(0);
        const u32x2 tg = {(unsigned)tag, (unsigned)(tag >> 32)};
#pragma unroll
        for (int t = 0; t < 8; ++t) {
            const int p = t >> 1, h = t & 1;
            u32x4 o;
#pragma unroll
            for (int x = 0; x < 4; ++x) o[x] = pack4(v[t % WA][x][0], v[t % WA][x][1], v[t % WA][x][2], v[t % WA][x][3]);
            asm volatile("ds_write_b128 %0, %1 offset:%2" ::"v"(tw), "v"(o), "n"(h * 128) : "memory");
            if (h) {
                u32x4 o0, o1;
                asm volatile("ds_read_b128 %0, %2\n\tds_read_b128 %1, %2 offset:1024\n\ts_waitcnt lgkmcnt(0)" : "=&v"(o0), "=&v"(o1) : "v"(tr) : "memory");
                if (t == 1) TS(1);
                __builtin_amdgcn_raw_buffer_store_b128(o0, rsA8, vo0 + p * 4096, 0, AUX_SC1);
                __builtin_amdgcn_raw_buffer_store_b128(o1, rsA8, vo0 + p * 4096 + 1024, 0, AUX_SC1);
            }
            __builtin_amdgcn_sched_barrier(0);
            if (t + WA < 8) ld(t + WA);
            __builtin_amdgcn_sched_barrier(0);
            if (h == 0 && t >= 2) {
                const int last = t + WA < 8 ? t : 7 - WA;
                const int after = last - (t - 1) + 1;
                asm volatile("s_waitcnt vmcnt(%0)" ::"n"((after > 0 ? after : 0) * 4) : "memory");
                if (CFLAG && p == 1)
                    __builtin_amdgcn_raw_buffer_store_b64(tg, rsF, lane < 16 ? (unsigned)(CF_OFF + (tmA * 16 + lane) * CSTR + (subA & 1) * 2 * CGS + (subA >> 1) * 8) : 0x80000000u, 0, AUX_SC1);
                else
                __builtin_amdgcn_raw_buffer_store_b64(tg, rsF, vf0 + (p - 1) * 32 * FSTR * 8, 0, AUX_SC1);
                if (p == 1) lds_wr32(ctl + CT_ARR, 1u);
                TS(1 + p);
                __builtin_amdgcn_sched_barrier(0);
            }
            if (t == 7) {
                asm volatile("s_waitcnt vmcnt(0)" ::: "memory");
                __builtin_amdgcn_raw_buffer_store_b64(tg, rsF, vf0 + 3 * 32 * FSTR * 8, 0, AUX_SC1);
                TS(5);
            }
        }
    } else {
        if (wid == 6 && lane < 16) lds_wr32(ctl + lane * 4, 0u);
        TAGWAIT();
        __builtin_amdgcn_s_barrier();
    }

    if (wid >= 6) {
        const int lw = wid - 6;
        if (lw == 1) {
            const int l4 = lane < 56 ? lane : 0;
            const u32x2 sw4 = reinterpret_cast<const u32x2*>(scale_w)[tn * (BN / 4) + l4];
            const u32x2 bi4 = reinterpret_cast<const u32x2*>(bias)[tn * (BN / 4) + l4];
            const float sx = __uint_as_float((unsigned)reinterpret_cast<const unsigned short*>(scale_x)[0] << 16);
            if (lane < 56) {
                const f32x4 sc = {sx * __uint_as_float(sw4[0] << 16), sx * __uint_as_float(sw4[0] & 0xffff0000u),
                                  sx * __uint_as_float(sw4[1] << 16), sx * __uint_as_float(sw4[1] & 0xffff0000u)};
                const f32x4 bi = {__uint_as_float(bi4[0] << 16), __uint_as_float(bi4[0] & 0xffff0000u),
                                  __uint_as_float(bi4[1] << 16), __uint_as_float(bi4[1] & 0xffff0000u)};
                *(LAS f32x4*)(uintptr_t)(smem_base + OFF_SCALE + lane * 16) = sc;
                *(LAS f32x4*)(uintptr_t)(smem_base + OFF_BIAS + lane * 16) = bi;
            }
        }
        TS(1);
        GAS u64* fl = flags + ((lane & 16) ? 16 + tn : tm) * FSTR + (lane & 15) * FSUB;
        unsigned pub = 0;
        u64 pf = 0;
        const __amdgpu_buffer_rsrc_t rsL = __builtin_amdgcn_make_buffer_rsrc((void*)(ws + WS_A8), 0, (M_ + N_) * K_, RSRC_W3);
        const int soA = (tm * 8 + lw) * 16384, soB = M_ * K_ + tn * (64 * UNIT) + lw * 1024;
        const unsigned full = ctl + CT_FULL + lw * 4;
        unsigned kfree = 0, rdy23 = 0;
        u64 x0 = 0, x1 = 0, x2 = 0, x3 = 0;
#define PUBLISH(V) do { pub = max(pub, (unsigned)(V)); lds_wr32(full, pub); } while (0)
#pragma unroll
        for (int kb = 0; kb < 16; ++kb) {
            if ((kb & 3) == 0) {
                bool ok = false;
                if (kb) {
                    asm volatile("s_waitcnt vmcnt(11)" : "+v"(pf)::"memory");
                    PUBLISH(kb - 1);
                    ok = __all(pf == tag);
                    if (!ok) {
                        asm volatile("s_waitcnt vmcnt(0)" ::: "memory");
                        PUBLISH(kb);
                    }
                }
                if (SPOLL && kb == 0) {
                    SPIN {
                        const u32x4 v = lds_rd128(ctl + CT_TAG);
                        rdy23 = __builtin_amdgcn_readfirstlane(v[2] & v[3]);
                        if (__builtin_amdgcn_readfirstlane(v[0] & v[1])) break;
                        __builtin_amdgcn_s_sleep(1);
                    }
                    ok = true;
                }
                if (!ok) {
                    if (lw == 0) {
                        SPIN {
                            const u32x4 v = lds_rd128(ctl + CT_ARR);
                            if ((unsigned)__builtin_amdgcn_readfirstlane(v[2]) > (unsigned)(kb >> 2)) break;
                            __builtin_amdgcn_s_sleep(1);
                        }
                        if (PIPE && kb == 0) {
#define PISS(X) asm volatile("global_load_dwordx2 %0, %1, off sc1" : "=v"(X) : "v"(fl) : "memory")
#define PCHK(X) asm volatile("s_waitcnt vmcnt(3)" : "+v"(X)::"memory"); if (__all(X == tag)) break; PISS(X); __builtin_amdgcn_s_sleep(PGAP)
                            PISS(x0); __builtin_amdgcn_s_sleep(PGAP);
                            PISS(x1); __builtin_amdgcn_s_sleep(PGAP);
                            PISS(x2); __builtin_amdgcn_s_sleep(PGAP);
                            PISS(x3);
                            SPIN {
                                PCHK(x0);
                                PCHK(x1);
                                PCHK(x2);
                                PCHK(x3);
                            }
                        } else
                        SPIN {
                            u64 x;
                            asm volatile("global_load_dwordx2 %0, %1, off sc1\n\ts_waitcnt vmcnt(0)" : "=v"(x) : "v"(fl + (kb >> 2) * 32 * FSTR) : "memory");
                            if (__all(x == tag)) break;
                            __builtin_amdgcn_s_sleep(PSLEEP);
                        }
                    } else {
                        SPIN {
                            const u32x4 v = lds_rd128(ctl + CT_ARR);
                            if ((unsigned)__builtin_amdgcn_readfirstlane(v[3]) > (unsigned)(kb >> 2)) break;
                            __builtin_amdgcn_s_sleep(1);
                        }
                    }
                }
                if (lw == 0) lds_wr32(ctl + CT_PH2, (kb >> 2) + 1);
                TS(2 + (kb >> 2));
            }
            if (SPOLL && kb == 2 && !rdy23) {
                u32x4 v = lds_rd128(ctl + CT_TAG);
                if (!__builtin_amdgcn_readfirstlane(v[2] & v[3])) {
                    asm volatile("s_waitcnt vmcnt(0)" ::: "memory");
                    PUBLISH(2);
                    SPIN {
                        v = lds_rd128(ctl + CT_TAG);
                        if (__builtin_amdgcn_readfirstlane(v[2] & v[3])) break;
                        __builtin_amdgcn_s_sleep(1);
                    }
                }
                TS(6);
            }
            if (kb >= NSTAGE && kfree <= (unsigned)(kb - NSTAGE)) {
                u32x4 v = lds_rd128(ctl + CT_FREE);
                kfree = __builtin_amdgcn_readfirstlane(min(min(v[0], v[1]), min(v[2], v[3])));
                if (kfree <= (unsigned)(kb - NSTAGE)) {
                    if (LAG > 1) {
                        asm volatile("s_waitcnt vmcnt(11)" ::: "memory");
                        PUBLISH(kb - 1);
                    }
                    asm volatile("s_waitcnt vmcnt(0)" ::: "memory");
                    PUBLISH(kb);
                    SPIN {
                        v = lds_rd128(ctl + CT_FREE);
                        kfree = __builtin_amdgcn_readfirstlane(min(min(v[0], v[1]), min(v[2], v[3])));
                        if (kfree > (unsigned)(kb - NSTAGE)) break;
                        __builtin_amdgcn_s_sleep(1);
                    }
                }
            }
            LAS uint8_t* base = (LAS uint8_t*)(uintptr_t)(smem_base + (kb % NSTAGE) * STAGE_BYTES + lw * 1024);
#pragma unroll
            for (int i = 0; i < 11; ++i) {
                const int so = i < 4 ? soA + i * 32768 + kb * 1024 : soB + kb * B_STAGE + (i - 4) * 2048;
                __builtin_amdgcn_raw_ptr_buffer_load_lds(rsL, base + i * 2048, 16, lane * 16, so, 0, AUX_SC1);
            }
            if (kb == CSKB && lw == 0) lds_wr32(ctl + CT_ARR + 4, 1u);
            if (kb >= LAG) {
                if ((kb & 3) == 3 && kb < 12) asm volatile("s_waitcnt vmcnt(%0)" ::"n"(11 * LAG + 1) : "memory");
                else asm volatile("s_waitcnt vmcnt(%0)" ::"n"(11 * LAG) : "memory");
                PUBLISH(kb - LAG + 1);
                if (kb == LAG) asm volatile("" : "+v"(x0), "+v"(x1), "+v"(x2), "+v"(x3));
            }
            if ((kb & 3) == 2 && kb < 12)
                asm volatile("global_load_dwordx2 %0, %1, off sc1" : "=v"(pf) : "v"(fl + ((kb >> 2) + 1) * 32 * FSTR) : "memory");
        }
        asm volatile("s_waitcnt vmcnt(11)" ::: "memory");
        PUBLISH(15);
        asm volatile("s_waitcnt vmcnt(0)" ::: "memory");
        PUBLISH(16);
        TS(7);
    } else if (wid >= 4) {
        if (wid == 4) {
            SPIN {
                const u32x4 v = lds_rd128(ctl + CT_ARR);
                if (__builtin_amdgcn_readfirstlane(v[1]) != 0) break;
                __builtin_amdgcn_s_sleep(2);
            }
            const __amdgpu_buffer_rsrc_t rsC = __builtin_amdgcn_make_buffer_rsrc((void*)(ws + WS_CP), 0, 16 * 16 * 512, RSRC_W3);
            u32x4 cp[8];
#pragma unroll
            for (int x = 0; x < 8; ++x)
                cp[x] = __builtin_amdgcn_raw_buffer_load_b128(rsC, lane * 16, tn * 8192 + x * 1024, AUX_SC1);
            const int nzx = -zero_x[0];
            int s[8];
#pragma unroll
            for (int d = 0; d < 4; ++d) {
                int lo = 0, hi = 0;
#pragma unroll
                for (int x = 0; x < 8; ++x) {
                    const unsigned w_ = cp[x][d];
                    lo += (int)(short)(w_ & 0xffffu);
                    hi += (int)w_ >> 16;
                }
                s[2 * d] = lo + __shfl_xor(lo, 32);
                s[2 * d + 1] = hi + __shfl_xor(hi, 32);
            }
            if (lane < 28) {
                *(LAS i32x4*)(uintptr_t)(smem_base + OFF_CORR + lane * 32) = (i32x4){nzx * s[0], nzx * s[1], nzx * s[2], nzx * s[3]};
                *(LAS i32x4*)(uintptr_t)(smem_base + OFF_CORR + lane * 32 + 16) = (i32x4){nzx * s[4], nzx * s[5], nzx * s[6], nzx * s[7]};
            }
            asm volatile("s_waitcnt lgkmcnt(0)" ::: "memory");
            lds_wr32(ctl + CT_FULL + 8, 1u);
        }
        TS(8);
        if (bid == 0 && wid == 5) {
            SPIN {
                bool ok = true;
#pragma unroll
                for (int i = 0; i < 4; ++i)
                    ok &= __hip_atomic_load(flags + (3 * 32 + i * 4 + (lane >> 4)) * FSTR + (lane & 15) * FSUB, __ATOMIC_RELAXED, __HIP_MEMORY_SCOPE_AGENT) == tag;
                if (__all(ok)) break;
                __builtin_amdgcn_s_sleep(8);
            }
            if (lane == 0) __hip_atomic_store((GAS u64*)(ws + WS_EPOCH), tag, __ATOMIC_RELAXED, __HIP_MEMORY_SCOPE_AGENT);
        }
    } else {
        const int wm = wid >> 1, wn = wid & 1;
        i32x4 acc[4][6], acm[2][2];
        i32x4 af[2][4], bfr[2][8];
#pragma unroll
        for (int i = 0; i < 4; ++i)
#pragma unroll
            for (int j = 0; j < 6; ++j) {
                acc[i][j] = (i32x4){0, 0, 0, 0};
                asm volatile("" : "+v"(acc[i][j]));
            }
#pragma unroll
        for (int i = 0; i < 2; ++i)
#pragma unroll
            for (int j = 0; j < 2; ++j) {
                acm[i][j] = (i32x4){0, 0, 0, 0};
                asm volatile("" : "+v"(acm[i][j]));
            }
        unsigned aA0[2], aA2[2], aO0[2], aO1[2], aM0[2], aM1[2];
        {
            const unsigned fA = smem_base + wm * 4096 + lane * 16;
            const unsigned fB = smem_base + A_STAGE + (lane >> 4) * UNIT + (lane & 3) * 896 + ((lane >> 2) & 3) * 32;
            const unsigned x = (lane & 2) * 8;
#pragma unroll
            for (int u = 0; u < 2; ++u) {
                aA0[u] = fA + wn * 2048 + u * 3 * STAGE_BYTES;
                aA2[u] = fA + (wn ^ 1) * 2048 + u * 3 * STAGE_BYTES;
                aO0[u] = fB + wn * 512 + x + u * 3 * STAGE_BYTES;
                aO1[u] = fB + wn * 512 + (x ^ 16) + u * 3 * STAGE_BYTES;
                aM0[u] = fB + 384 + x + u * 3 * STAGE_BYTES;
                aM1[u] = fB + 384 + (x ^ 16) + u * 3 * STAGE_BYTES;
                asm volatile("" : "+v"(aA0[u]), "+v"(aA2[u]), "+v"(aO0[u]), "+v"(aO1[u]), "+v"(aM0[u]), "+v"(aM1[u]));
            }
        }
        const unsigned freew = ctl + CT_FREE + wid * 4;
        unsigned known = 0;
#define DSR(DST, ADDR, OFF) asm volatile("ds_read_b128 %0, %1 offset:%2" : "=v"(DST) : "v"(ADDR), "n"(OFF) : "memory")
#define MF(I, J, SET) asm volatile("v_mfma_i32_16x16x64_i8 %0, %1, %2, %0" : "+v"(acc[I][J]) : "v"(bfr[SET][J]), "v"(af[SET][I]))
#define MM(I, J, SET) asm volatile("v_mfma_i32_16x16x64_i8 %0, %1, %2, %0" : "+v"(acm[I][J]) : "v"(bfr[SET][6 + (J)]), "v"(af[SET][I]))
#define NEED(X)                                                                                                \
    do {                                                                                                       \
        if (__builtin_expect(known <= (unsigned)(X), 0)) {                                                     \
            SPIN {                                                                                             \
                const u32x4 v_ = lds_rd128(ctl + CT_FULL);                                                     \
                known = __builtin_amdgcn_readfirstlane(min(v_[0], v_[1]));                                     \
                if (known > (unsigned)(X)) break;                                                              \
                __builtin_amdgcn_s_sleep(1);                                                                   \
            }                                                                                                  \
        }                                                                                                      \
    } while (0)
#define SU(KB) ((((KB) % NSTAGE) >= 3) ? 1 : 0)
#define SO(KB) ((((KB) % NSTAGE) % 3) * STAGE_BYTES)
#define RA(SET, KB, I) DSR(af[SET][I], (I) < 2 ? aA0[SU(KB)] : aA2[SU(KB)], SO(KB) + ((I) & 1) * 1024)
#define RB(SET, KB, J) DSR(bfr[SET][J], (J) >= 6 ? ((J) & 1 ? aM1[SU(KB)] : aM0[SU(KB)]) : ((J) & 1 ? aO1[SU(KB)] : aO0[SU(KB)]), SO(KB) + ((J) < 6 ? ((J) >> 1) * 128 : 0))
#define STEP(SET, KB)                                                                        \
    do {                                                                                     \
        MF(0, 0, SET);                                                                       \
        if ((KB) + 1 < 16) {                                                                 \
            NEED((KB) + 1);                                                                  \
            __builtin_amdgcn_sched_barrier(0);                                               \
            MF(1, 0, SET);                                                                   \
            RB((SET) ^ 1, (KB) + 1, 0);   MF(2, 0, SET);                                     \
            RA((SET) ^ 1, (KB) + 1, 0);   MF(3, 0, SET);                                     \
            RA((SET) ^ 1, (KB) + 1, 1);   MF(0, 1, SET);                                     \
            RA((SET) ^ 1, (KB) + 1, 2);   MF(1, 1, SET);                                     \
            RA((SET) ^ 1, (KB) + 1, 3);   MF(2, 1, SET);                                     \
            RB((SET) ^ 1, (KB) + 1, 1);   MF(3, 1, SET);                                     \
            RB((SET) ^ 1, (KB) + 1, 2);   MF(0, 2, SET);                                     \
            RB((SET) ^ 1, (KB) + 1, 3);   MF(1, 2, SET);                                     \
            RB((SET) ^ 1, (KB) + 1, 4);   MF(2, 2, SET);                                     \
            RB((SET) ^ 1, (KB) + 1, 5);   MF(3, 2, SET);                                     \
            RB((SET) ^ 1, (KB) + 1, 6);   MF(0, 3, SET);                                     \
            RB((SET) ^ 1, (KB) + 1, 7);                                                      \
            MF(1, 3, SET); MF(2, 3, SET); MF(3, 3, SET);                                     \
            MF(0, 4, SET); MF(1, 4, SET);                                                    \
            if ((KB) + 2 < 16) asm volatile("ds_read_b64 %0, %1" : "=v"(fv) : "v"(fullv) : "memory"); \
            MF(2, 4, SET); MF(3, 4, SET);                                                    \
            MF(0, 5, SET); MF(1, 5, SET); MF(2, 5, SET); MF(3, 5, SET);                      \
            MM(0, 0, SET);                                                                   \
            asm volatile("s_waitcnt lgkmcnt(0)" : "+v"(fv)::"memory");                       \
            if ((KB) + 1 + NSTAGE < 16) lds_wr32(freew, (KB) + 2);                           \
            MM(1, 0, SET);                                                                   \
            if ((KB) + 2 < 16) known = max(known, (unsigned)__builtin_amdgcn_readfirstlane(min(fv[0], fv[1]))); \
            __builtin_amdgcn_sched_barrier(0);                                               \
            MM(0, 1, SET); MM(1, 1, SET);                                                    \
        } else {                                                                             \
            MF(1, 0, SET); MF(2, 0, SET); MF(3, 0, SET);                                     \
            _Pragma("unroll") for (int j = 1; j < 6; ++j)                                    \
            _Pragma("unroll") for (int i = 0; i < 4; ++i) MF(i, j, SET);                     \
            MM(0, 0, SET); MM(1, 0, SET); MM(0, 1, SET); MM(1, 1, SET);                      \
        }                                                                                    \
        __builtin_amdgcn_sched_barrier(0);                                                   \
    } while (0)
        u32x2 fv = {0, 0};
        unsigned fullv = ctl + CT_FULL;
        asm volatile("" : "+v"(fullv));
        if (SPOLL) {
            if (SGATE) SPIN {
                const u32x4 v = lds_rd128(ctl + CT_ARR);
                if (__builtin_amdgcn_readfirstlane((SGATE == 2 && !(wid & 1)) ? v[0] : v[2])) break;
                __builtin_amdgcn_s_sleep(1);
            }
            TS(1);
            GAS u64* fp = flags + ((wid & 1) ? 16 + tn : tm) * FSTR + (wid >> 1) * 8 * FSUB;
            bool okp = false;
            int it = 0;
            if (CFLAG) {
                fp = flags + (CF_OFF + (tm * 16 + tn) * CSTR + wid * CGS) / 8;
                typedef u64 u64x8 __attribute__((ext_vector_type(8)));
                for (; it < 256; ++it) {
                    u64x8 f;
                    asm volatile("s_load_dwordx16 %0, %1, 0x0 glc\n\ts_waitcnt lgkmcnt(0)" : "=s"(f) : "s"(fp) : "memory");
                    const u64 f0 = f[0], f1 = f[1], f2 = f[2], f3 = f[3], f4 = f[4], f5 = f[5], f6 = f[6], f7 = f[7];
                    const u64 d = (((f0 ^ tag) | (f1 ^ tag)) | ((f2 ^ tag) | (f3 ^ tag))) | (((f4 ^ tag) | (f5 ^ tag)) | ((f6 ^ tag) | (f7 ^ tag)));
                    if (d == 0) { okp = true; break; }
                    __builtin_amdgcn_s_sleep(SSLEEP);
                }
            } else
            for (; it < 256; ++it) {
                u64 f0, f1, f2, f3, f4, f5, f6, f7;
                asm volatile("s_load_dwordx2 %0, %8, %9 glc\n\ts_load_dwordx2 %1, %8, %10 glc\n\ts_load_dwordx2 %2, %8, %11 glc\n\t"
                             "s_load_dwordx2 %3, %8, %12 glc\n\ts_load_dwordx2 %4, %8, %13 glc\n\ts_load_dwordx2 %5, %8, %14 glc\n\t"
                             "s_load_dwordx2 %6, %8, %15 glc\n\ts_load_dwordx2 %7, %8, %16 glc\n\ts_waitcnt lgkmcnt(0)"
                             : "=&s"(f0), "=&s"(f1), "=&s"(f2), "=&s"(f3), "=&s"(f4), "=&s"(f5), "=&s"(f6), "=&s"(f7)
                             : "s"(fp), "n"(0), "n"(FSUB * 8), "n"(FSUB * 16), "n"(FSUB * 24), "n"(FSUB * 32), "n"(FSUB * 40), "n"(FSUB * 48), "n"(FSUB * 56)
                             : "memory");
                const u64 d = (((f0 ^ tag) | (f1 ^ tag)) | ((f2 ^ tag) | (f3 ^ tag))) | (((f4 ^ tag) | (f5 ^ tag)) | ((f6 ^ tag) | (f7 ^ tag)));
                if (d == 0) { okp = true; break; }
                __builtin_amdgcn_s_sleep(SSLEEP);
            }
            if (!okp) SPIN {
                u64 x;
                asm volatile("global_load_dwordx2 %0, %1, off sc1\n\ts_waitcnt vmcnt(0)" : "=v"(x) : "v"(fp + (lane & 7) * (CFLAG ? 1 : FSUB)) : "memory");
                if (__all(x == tag)) break;
                __builtin_amdgcn_s_sleep(PSLEEP);
            }
            lds_wr32(ctl + CT_TAG + wid * 4, 1u);
            TS(2);
        }
        NEED(0);
        TS(4);
        RB(0, 0, 0); RA(0, 0, 0); RA(0, 0, 1); RA(0, 0, 2); RA(0, 0, 3);
        RB(0, 0, 1); RB(0, 0, 2); RB(0, 0, 3); RB(0, 0, 4); RB(0, 0, 5); RB(0, 0, 6); RB(0, 0, 7);
        asm volatile("s_waitcnt lgkmcnt(0)" ::: "memory");
        lds_wr32(freew, 1);
        __builtin_amdgcn_sched_barrier(0);
#pragma unroll
        for (int kb = 0; kb < 16; kb += 2) {
            STEP(0, kb);
            STEP(1, kb + 1);
            if (kb == 2) TS(5);
            if (kb == 6) TS(6);
            if (kb == 10) TS(7);
        }
        TS(8);
        __builtin_amdgcn_sched_barrier(0);
        SPIN {
            const u32x4 v_ = lds_rd128(ctl + CT_FULL);
            if (__builtin_amdgcn_readfirstlane(v_[2])) break;
            __builtin_amdgcn_s_sleep(1);
        }
        TS(9);
        asm volatile("s_nop 7" ::: "memory");
#pragma unroll
        for (int j = 0; j < 6; ++j) asm volatile("" : "+v"(acc[0][j]), "+v"(acc[1][j]), "+v"(acc[2][j]), "+v"(acc[3][j]));
        asm volatile("" : "+v"(acm[0][0]), "+v"(acm[0][1]), "+v"(acm[1][0]), "+v"(acm[1][1]));
        const __amdgpu_buffer_rsrc_t rsO = __builtin_amdgcn_make_buffer_rsrc((void*)out, 0, M_ * N_ * 2, RSRC_W3);
        const unsigned ovo = (unsigned)(((tm * BM + wm * 64 + (lane & 15)) * N_ + tn * BN + (lane >> 4) * 8) * 2);
        const unsigned ro0 = ovo + wn * (32 * N_ * 2), ro2 = ovo + (wn ^ 1) * (32 * N_ * 2);
#pragma unroll
        for (int ch = 0; ch < 4; ++ch) {
            const int cb = ch < 3 ? wn * 128 + ch * 32 : 96;
            const LAS uint8_t* sm = (const LAS uint8_t*)(uintptr_t)(smem_base + (cb + (lane >> 4) * 8) * 4);
            const f32x4 s0 = *reinterpret_cast<const LAS f32x4*>(sm + OFF_SCALE), s1 = *reinterpret_cast<const LAS f32x4*>(sm + OFF_SCALE + 16);
            const f32x4 b0 = *reinterpret_cast<const LAS f32x4*>(sm + OFF_BIAS), b1 = *reinterpret_cast<const LAS f32x4*>(sm + OFF_BIAS + 16);
            const i32x4 c0 = *reinterpret_cast<const LAS i32x4*>(sm + OFF_CORR), c1 = *reinterpret_cast<const LAS i32x4*>(sm + OFF_CORR + 16);
#pragma unroll
            for (int i = 0; i < (ch < 3 ? 4 : 2); ++i) {
                const i32x4 a0 = ch < 3 ? acc[i][2 * ch] : acm[i][0], a1 = ch < 3 ? acc[i][2 * ch + 1] : acm[i][1];
                f32x2 v0 = {(float)(a0[0] + c0[0]) * s0[0] + b0[0], (float)(a0[1] + c0[1]) * s0[1] + b0[1]};
                f32x2 v1 = {(float)(a0[2] + c0[2]) * s0[2] + b0[2], (float)(a0[3] + c0[3]) * s0[3] + b0[3]};
                f32x2 v2 = {(float)(a1[0] + c1[0]) * s1[0] + b1[0], (float)(a1[1] + c1[1]) * s1[1] + b1[1]};
                f32x2 v3 = {(float)(a1[2] + c1[2]) * s1[2] + b1[2], (float)(a1[3] + c1[3]) * s1[3] + b1[3]};
                u32x4 o;
                o[0] = __builtin_bit_cast(unsigned, __builtin_convertvector(v0, bf16x2_t));
                o[1] = __builtin_bit_cast(unsigned, __builtin_convertvector(v1, bf16x2_t));
                o[2] = __builtin_bit_cast(unsigned, __builtin_convertvector(v2, bf16x2_t));
                o[3] = __builtin_bit_cast(unsigned, __builtin_convertvector(v3, bf16x2_t));
                __builtin_amdgcn_raw_buffer_store_b128(o, rsO, (i < 2 ? ro0 : ro2) + (i & 1) * (16 * N_ * 2) + cb * 2, 0, OUT_AUX);
            }
        }
    }
    TS(10);
}

extern "C" void kernel_launch(void* const* d_in, const int* in_sizes, int n_in,
                              void* d_out, int out_size, void* d_ws, size_t ws_size,
                              hipStream_t stream) {
    const int* qx = (const int*)d_in[0];
    const int* qw = (const int*)d_in[1];
    const __hip_bfloat16* scale_x = (const __hip_bfloat16*)d_in[2];
    const int* zero_x = (const int*)d_in[3];
    const __hip_bfloat16* scale_w = (const __hip_bfloat16*)d_in[4];
    const __hip_bfloat16* bias = (const __hip_bfloat16*)d_in[5];
    (void)hipFuncSetAttribute((const void*)fused_kernel, hipFuncAttributeMaxDynamicSharedMemorySize, SMEM_BYTES);
    fused_kernel<<<256, 512, SMEM_BYTES, stream>>>(qx, qw, scale_x, zero_x, scale_w, bias, (__hip_bfloat16*)d_out,
                                                   (uint8_t*)d_ws);
}
```

```cpp
#include <hip/hip_runtime.h>
#include <hip/hip_bf16.h>
#include <stdint.h>

typedef int i32x4 __attribute__((ext_vector_type(4)));
typedef int i32x2 __attribute__((ext_vector_type(2)));
typedef unsigned int u32x4 __attribute__((ext_vector_type(4)));
typedef unsigned int u32x2 __attribute__((ext_vector_type(2)));
typedef float f32x2 __attribute__((ext_vector_type(2)));
typedef float f32x4 __attribute__((ext_vector_type(4)));
typedef short s16x2 __attribute__((ext_vector_type(2)));
typedef __bf16 bf16x2_t __attribute__((ext_vector_type(2)));
typedef unsigned long long u64;
#define GAS __attribute__((address_space(1)))
#define LAS __attribute__((address_space(3)))

constexpr int M_ = 2048, K_ = 1024, N_ = 3584;
constexpr int BM = 128, BN = 224;
constexpr int NSTAGE = 6;
constexpr int UNIT = 3584;
constexpr int A_STAGE = 8192, B_STAGE = 4 * UNIT;
constexpr int STAGE_BYTES = A_STAGE + B_STAGE;
constexpr int RING_BYTES = NSTAGE * STAGE_BYTES;
constexpr int ACC_PITCH = BN * 4 + 16;
constexpr int OFF_CTL = RING_BYTES;
constexpr int CT_FULL = 0, CT_FREE = 16, CT_TAG = 32, CT_ARR = 48, CT_GO = 56, CT_PH2 = 60, CT_SIG = 64;
constexpr int OFF_SCALE = OFF_CTL + 128;
constexpr int OFF_BIAS = OFF_SCALE + 896;
constexpr int OFF_TR = OFF_BIAS + 896;
constexpr int SMEM_BYTES = OFF_TR + 2048;
static_assert(SMEM_BYTES <= 163840, "LDS");
static_assert(BM * ACC_PITCH <= RING_BYTES, "acc staging must fit in the ring");
constexpr int NPH = 4;
constexpr size_t WS_EPOCH = 0;
#define FSUB 16
#define FSTR 544
constexpr size_t WS_FLAGS = 8u << 20;
constexpr size_t WS_A8 = 65536;
constexpr size_t WS_B8 = WS_A8 + (size_t)M_ * K_;
constexpr size_t WS_CP = WS_B8 + (size_t)N_ * K_;
constexpr size_t WS_DBG = WS_CP + 16 * 64 * 512;
constexpr int AUX_SC1 = 16;
constexpr int RSRC_W3 = 0x00020000;
#define WB 4
#define WA 2
#define FLAGI 1
#define CSKB 13
#define CFLAG 1
#define CGS 64
#define CSTR (4 * CGS)
#define CF_OFF (128 * FSTR * 8)
#define CF2_OFF (CF_OFF + 256 * CSTR)
#define SPOLL 1
#define SGATE 1
#define CSLEEP 4
#define HLIGHT 1
#define REALV 1
#define HSLEEP 1
#define CPF 10
#define SSLEEP 2
#define DMA_AUX 16
#define LAGK 16
#define LAG (kb >= LAGK ? 2 : 1)
#define PIPE 0
#define PGAP 6
#define PSLEEP 4
#define OUT_AUX 18
#define RAW_AUX 2
#define GO_KB 1

__device__ __forceinline__ unsigned pack4(int a, int b, int c, int d) {
    const unsigned lo = __builtin_amdgcn_perm((unsigned)b, (unsigned)a, 0x0c0c0400u);
    const unsigned hi = __builtin_amdgcn_perm((unsigned)d, (unsigned)c, 0x0c0c0400u);
    return __builtin_amdgcn_perm(hi, lo, 0x05040100u);
}
__device__ __forceinline__ u32x4 lds_rd128(unsigned addr) {
    u32x4 v;
    asm volatile("ds_read_b128 %0, %1\n\ts_waitcnt lgkmcnt(0)" : "=v"(v) : "v"(addr) : "memory");
    return v;
}
__device__ __forceinline__ void lds_wr32_max(unsigned addr, unsigned val) {
    asm volatile("ds_max_u32 %0, %1" ::"v"(addr), "v"(val) : "memory");
}
__device__ __forceinline__ void lds_wr32(unsigned addr, unsigned val) {
    asm volatile("ds_write_b32 %0, %1" ::"v"(addr), "v"(val) : "memory");
}

__device__ __forceinline__ float sfma(int a, float s, float b) {
    float r;
    asm("v_fma_f32 %0, %1, %2, %3" : "=v"(r) : "v"((float)a), "v"(s), "v"(b));
    return r;
}
#define SPIN for (;;)
#define TS(i) do { } while (0)

__global__ __launch_bounds__(512, 2) void fused_kernel(const int* __restrict__ qx, const int* __restrict__ qw,
                                                       const __hip_bfloat16* __restrict__ scale_x,
                                                       const int* __restrict__ zero_x,
                                                       const __hip_bfloat16* __restrict__ scale_w,
                                                       const __hip_bfloat16* __restrict__ bias,
                                                       __hip_bfloat16* __restrict__ out, uint8_t* __restrict__ ws) {
    extern __shared__ __attribute__((aligned(16))) uint8_t smem[];
    const int tid = threadIdx.x, lane = tid & 63;
    const int wid = __builtin_amdgcn_readfirstlane(tid >> 6);
    const int bid = blockIdx.x;
    const int tm = (bid & 1) * 8 + ((bid >> 3) & 7);
    const int tn = ((bid >> 1) & 3) * 4 + (bid >> 6);
    const int tmA = bid >> 4, subA = bid & 15;
    const int tnB = bid & 15, subB = bid >> 4;
    const unsigned smem_base = (unsigned)(uintptr_t)smem;
    const unsigned ctl = smem_base + OFF_CTL;
    GAS u64* flags = (GAS u64*)(ws + WS_FLAGS);
    u64 tag;
    unsigned pfd = 0;
#define TAGWAIT()                                                                                                        \
    do {                                                                                                                 \
        asm volatile("s_load_dwordx2 %0, %1, 0x0\n\ts_waitcnt lgkmcnt(0)" : "=s"(tag) : "s"(ws + WS_EPOCH) : "memory"); \
        tag += 0x9E3779B97F4A7C15ull;                                                                                    \
    } while (0)

    const __amdgpu_buffer_rsrc_t rsF = __builtin_amdgcn_make_buffer_rsrc((void*)(ws + WS_FLAGS), 0, CF2_OFF + 256 * 128, RSRC_W3);
    if (wid == 4) {
        const __amdgpu_buffer_rsrc_t rsW = __builtin_amdgcn_make_buffer_rsrc((void*)qw, 0, K_ * N_ * 4, RSRC_W3);
        const __amdgpu_buffer_rsrc_t rsB8 = __builtin_amdgcn_make_buffer_rsrc((void*)(ws + WS_B8), 0, N_ * K_ + 16 * 16 * 512, RSRC_W3);
        const int q = lane < 56 ? lane : lane - 8;
        const unsigned voff = (unsigned)((tnB * 224 + q * 4) * 4);
        const int soff0 = ((subB >> 3) * 128 + (subB & 7) * 4) * (N_ * 4);
        const unsigned vo0 = lane < 56 ? (unsigned)((tnB * 64 + subB) * UNIT + lane * 16) : 0x80000000u;
        const unsigned vf0 = lane == 0 ? (unsigned)(((16 + tnB) * FSTR + subB * FSUB) * 8) : 0x80000000u;
        i32x4 st[WB][4];
        auto ld = [&](int t) {
#pragma unroll
            for (int x = 0; x < 4; ++x)
                st[t % WB][x] = __builtin_bit_cast(i32x4, __builtin_amdgcn_raw_buffer_load_b128(rsW, voff, soff0 + ((t >> 2) * 256 + (t & 3) * 32 + x) * (N_ * 4), RAW_AUX));
        };
#pragma unroll
        for (int t = 0; t < WB; ++t) ld(t);
        __builtin_amdgcn_sched_barrier(0);
        TAGWAIT();
        __builtin_amdgcn_s_barrier();
        __builtin_amdgcn_sched_barrier(0);
        const u32x2 tg = {(unsigned)tag, (unsigned)(tag >> 32)};
        int cs[4] = {0, 0, 0, 0};
        u32x4 pk[4];
#pragma unroll
        for (int t = 0; t < 16; ++t) {
            const int p = t >> 2, i = t & 3;
#pragma unroll
            for (int c = 0; c < 4; ++c) {
                pk[c][i] = pack4(st[t % WB][0][c], st[t % WB][1][c], st[t % WB][2][c], st[t % WB][3][c]);
                cs[c] = __builtin_amdgcn_sdot4((int)pk[c][i], 0x01010101, cs[c], false);
            }
            if (i == 3) {
                if (p == 0) TS(1);
#pragma unroll
                for (int c = 0; c < 4; ++c) __builtin_amdgcn_raw_buffer_store_b128(pk[c], rsB8, (c < 2 ? vo0 : vo0 ^ 16u) + p * 16 * UNIT + c * 896, 0, AUX_SC1);
                if (t == 15) {
                    const u32x2 cp = {__builtin_amdgcn_perm((unsigned)cs[1], (unsigned)cs[0], 0x05040100u),
                                      __builtin_amdgcn_perm((unsigned)cs[3], (unsigned)cs[2], 0x05040100u)};
                    __builtin_amdgcn_raw_buffer_store_b64(cp, rsB8, (unsigned)(N_ * K_ + (tnB * 16 + subB) * 512 + lane * 8), 0, AUX_SC1);
                }
            }
            __builtin_amdgcn_sched_barrier(0);
            if (t + WB < 16) ld(t + WB);
            __builtin_amdgcn_sched_barrier(0);
            if (i == FLAGI && t >= 4) {
                const int pp = p - 1;
                const int last = t + WB < 16 ? t : 15 - WB;
                const int after = last - (4 * pp + 3) + 1;
                asm volatile("s_waitcnt vmcnt(%0)" ::"n"((after > 0 ? after : 0) * 4) : "memory");
                if (CFLAG && pp == 0)
                    __builtin_amdgcn_raw_buffer_store_b64(tg, rsF, lane < 16 ? (unsigned)(CF_OFF + (lane * 16 + tnB) * CSTR + ((subB >> 3) * 2 + 1) * CGS + (subB & 7) * 8) : 0x80000000u, 0, AUX_SC1);
                else
                __builtin_amdgcn_raw_buffer_store_b64(tg, rsF, vf0 + pp * 32 * FSTR * 8, 0, AUX_SC1);
                lds_wr32(ctl + CT_GO, pp + 1);
                TS(2 + pp);
                __builtin_amdgcn_sched_barrier(0);
            }
            if (t == 15) {
                asm volatile("s_waitcnt vmcnt(0)" ::: "memory");
                __builtin_amdgcn_raw_buffer_store_b64(tg, rsF, (lane & 48) == 16 ? (unsigned)(CF2_OFF + ((lane & 15) * 16 + tnB) * 128 + subB * 8) : vf0 + 3 * 32 * FSTR * 8, 0, AUX_SC1);
                lds_wr32(ctl + CT_GO, 4);
                TS(5);
            }
        }
    } else if (wid == 5) {
        const __amdgpu_buffer_rsrc_t rsX = __builtin_amdgcn_make_buffer_rsrc((void*)qx, 0, M_ * K_ * 4, RSRC_W3);
        const __amdgpu_buffer_rsrc_t rsA8 = __builtin_amdgcn_make_buffer_rsrc((void*)(ws + WS_A8), 0, M_ * K_, RSRC_W3);
        const int rb = tmA * 8 + (subA >> 1), kb0 = 2 * (subA & 1);
        const unsigned voff = (unsigned)(((rb * 16 + (lane >> 3)) * K_ + kb0 * 64 + (lane & 7) * 4) * 4);
        const unsigned vo0 = (unsigned)((rb * 16 + kb0) * 1024 + lane * 16);
        const unsigned vf0 = lane == 0 ? (unsigned)((tmA * FSTR + ((subA & 1) * 8 + (subA >> 1)) * FSUB) * 8) : 0x80000000u;
        const unsigned tw = smem_base + OFF_TR + (lane & 7) * 256 + (lane >> 3) * 16, tr = smem_base + OFF_TR + lane * 16;
        i32x4 v[WA][4];
        auto ld = [&](int t) {
#pragma unroll
            for (int x = 0; x < 4; ++x)
                v[t % WA][x] = __builtin_bit_cast(i32x4, __builtin_amdgcn_raw_buffer_load_b128(rsX, voff + (t >> 1) * 1024 + x * 128, (t & 1) * (8 * K_ * 4), RAW_AUX));
        };
#pragma unroll
        for (int t = 0; t < WA; ++t) ld(t);
        __builtin_amdgcn_sched_barrier(0);
        TAGWAIT();
        __builtin_amdgcn_s_barrier();
        __builtin_amdgcn_sched_barrier(0);
        const u32x2 tg = {(unsigned)tag, (unsigned)(tag >> 32)};
#pragma unroll
        for (int t = 0; t < 8; ++t) {
            const int p = t >> 1, h = t & 1;
            u32x4 o;
#pragma unroll
            for (int x = 0; x < 4; ++x) o[x] = pack4(v[t % WA][x][0], v[t % WA][x][1], v[t % WA][x][2], v[t % WA][x][3]);
            asm volatile("ds_write_b128 %0, %1 offset:%2" ::"v"(tw), "v"(o), "n"(h * 128) : "memory");
            if (h) {
                u32x4 o0, o1;
                asm volatile("ds_read_b128 %0, %2\n\tds_read_b128 %1, %2 offset:1024\n\ts_waitcnt lgkmcnt(0)" : "=&v"(o0), "=&v"(o1) : "v"(tr) : "memory");
                if (t == 1) TS(1);
                __builtin_amdgcn_raw_buffer_store_b128(o0, rsA8, vo0 + p * 4096, 0, AUX_SC1);
                __builtin_amdgcn_raw_buffer_store_b128(o1, rsA8, vo0 + p * 4096 + 1024, 0, AUX_SC1);
            }
            __builtin_amdgcn_sched_barrier(0);
            if (t + WA < 8) ld(t + WA);
            __builtin_amdgcn_sched_barrier(0);
            if (h == 0 && t >= 2) {
                const int last = t + WA < 8 ? t : 7 - WA;
                const int after = last - (t - 1) + 1;
                asm volatile("s_waitcnt vmcnt(%0)" ::"n"((after > 0 ? after : 0) * 4) : "memory");
                if (CFLAG && p == 1)
                    __builtin_amdgcn_raw_buffer_store_b64(tg, rsF, lane < 16 ? (unsigned)(CF_OFF + (tmA * 16 + lane) * CSTR + (subA & 1) * 2 * CGS + (subA >> 1) * 8) : 0x80000000u, 0, AUX_SC1);
                else
                __builtin_amdgcn_raw_buffer_store_b64(tg, rsF, vf0 + (p - 1) * 32 * FSTR * 8, 0, AUX_SC1);
                if (p == 1) lds_wr32(ctl + CT_ARR, 1u);
                TS(1 + p);
                __builtin_amdgcn_sched_barrier(0);
            }
            if (t == 7) {
                asm volatile("s_waitcnt vmcnt(0)" ::: "memory");
                __builtin_amdgcn_raw_buffer_store_b64(tg, rsF, vf0 + 3 * 32 * FSTR * 8, 0, AUX_SC1);
                TS(5);
            }
        }
    } else {
        if (CPF && wid < 4) {
            u64 pc;
            int clen;
            asm volatile(".Lpf_start:\n\ts_getpc_b64 %0\n\ts_mov_b32 %1, .Lpf_end-.Lpf_start" : "=s"(pc), "=s"(clen));
            const __amdgpu_buffer_rsrc_t rsP = __builtin_amdgcn_make_buffer_rsrc((void*)pc, 0, max(clen, 4) - 4, RSRC_W3);
            const int idx = (bid >> 3) * 4 + wid;
            pfd = __builtin_amdgcn_raw_buffer_load_b32(rsP, idx < 3 * CPF ? (unsigned)((idx % CPF) * 4096 + lane * 64) : 0x80000000u, 0, 0);
        }
        if (wid == 6 && lane < 32) lds_wr32(ctl + lane * 4, 0u);
        TAGWAIT();
        __builtin_amdgcn_s_barrier();
    }

    if (wid >= 6) {
        const int lw = wid - 6;
        if (lw == 1) {
            const int l4 = lane < 56 ? lane : 0;
            const u32x2 sw4 = reinterpret_cast<const u32x2*>(scale_w)[tn * (BN / 4) + l4];
            const u32x2 bi4 = reinterpret_cast<const u32x2*>(bias)[tn * (BN / 4) + l4];
            const float sx = __uint_as_float((unsigned)reinterpret_cast<const unsigned short*>(scale_x)[0] << 16);
            if (lane < 56) {
                const f32x4 sc = {sx * __uint_as_float(sw4[0] << 16), sx * __uint_as_float(sw4[0] & 0xffff0000u),
                                  sx * __uint_as_float(sw4[1] << 16), sx * __uint_as_float(sw4[1] & 0xffff0000u)};
                const f32x4 bi = {__uint_as_float(bi4[0] << 16), __uint_as_float(bi4[0] & 0xffff0000u),
                                  __uint_as_float(bi4[1] << 16), __uint_as_float(bi4[1] & 0xffff0000u)};
                *(LAS f32x4*)(uintptr_t)(smem_base + OFF_SCALE + lane * 16) = sc;
                *(LAS f32x4*)(uintptr_t)(smem_base + OFF_BIAS + lane * 16) = bi;
            }
        }
        TS(1);
        GAS u64* fl = flags + ((lane & 16) ? 16 + tn : tm) * FSTR + (lane & 15) * FSUB;
        unsigned pub = 0;
        u64 pf = 0;
        const __amdgpu_buffer_rsrc_t rsL = __builtin_amdgcn_make_buffer_rsrc((void*)(ws + WS_A8), 0, (M_ + N_) * K_, RSRC_W3);
        const int soA = (tm * 8 + lw) * 16384, soB = M_ * K_ + tn * (64 * UNIT) + lw * 1024;
        const unsigned full = ctl + CT_FULL + lw * 4;
        unsigned kfree = 0, rdy23 = 0;
        u64 x0 = 0, x1 = 0, x2 = 0, x3 = 0;
#define PUBLISH(V) do { pub = max(pub, (unsigned)(V)); lds_wr32(full, pub); } while (0)
#pragma unroll
        for (int kb = 0; kb < 16; ++kb) {
            if ((kb & 3) == 0) {
                bool ok = false;
                if (kb) {
                    asm volatile("s_waitcnt vmcnt(11)" : "+v"(pf)::"memory");
                    PUBLISH(kb - 1);
                    ok = __all(pf == tag);
                    if (!ok) {
                        asm volatile("s_waitcnt vmcnt(0)" ::: "memory");
                        PUBLISH(kb);
                    }
                }
                if (SPOLL && kb == 0) {
                    SPIN {
                        const u32x4 v = lds_rd128(ctl + CT_TAG);
                        rdy23 = __builtin_amdgcn_readfirstlane(v[2] & v[3]);
                        if (__builtin_amdgcn_readfirstlane(v[0] & v[1])) break;
                        __builtin_amdgcn_s_sleep(1);
                    }
                    ok = true;
                }
                if (!ok) {
                    if (lw == 0) {
                        SPIN {
                            const u32x4 v = lds_rd128(ctl + CT_ARR);
                            if ((unsigned)__builtin_amdgcn_readfirstlane(v[2]) > (unsigned)(kb >> 2)) break;
                            __builtin_amdgcn_s_sleep(1);
                        }
                        if (PIPE && kb == 0) {
#define PISS(X) asm volatile("global_load_dwordx2 %0, %1, off sc1" : "=v"(X) : "v"(fl) : "memory")
#define PCHK(X) asm volatile("s_waitcnt vmcnt(3)" : "+v"(X)::"memory"); if (__all(X == tag)) break; PISS(X); __builtin_amdgcn_s_sleep(PGAP)
                            PISS(x0); __builtin_amdgcn_s_sleep(PGAP);
                            PISS(x1); __builtin_amdgcn_s_sleep(PGAP);
                            PISS(x2); __builtin_amdgcn_s_sleep(PGAP);
                            PISS(x3);
                            SPIN {
                                PCHK(x0);
                                PCHK(x1);
                                PCHK(x2);
                                PCHK(x3);
                            }
                        } else
                        SPIN {
                            u64 x;
                            asm volatile("global_load_dwordx2 %0, %1, off sc1\n\ts_waitcnt vmcnt(0)" : "=v"(x) : "v"(fl + (kb >> 2) * 32 * FSTR) : "memory");
                            if (__all(x == tag)) break;
                            __builtin_amdgcn_s_sleep(PSLEEP);
                        }
                    } else {
                        SPIN {
                            const u32x4 v = lds_rd128(ctl + CT_ARR);
                            if ((unsigned)__builtin_amdgcn_readfirstlane(v[3]) > (unsigned)(kb >> 2)) break;
                            __builtin_amdgcn_s_sleep(1);
                        }
                    }
                }
                if (lw == 0) lds_wr32(ctl + CT_PH2, (kb >> 2) + 1);
                TS(2 + (kb >> 2));
            }
            if (SPOLL && kb == 2 && !rdy23) {
                u32x4 v = lds_rd128(ctl + CT_TAG);
                if (!__builtin_amdgcn_readfirstlane(v[2] & v[3])) {
                    asm volatile("s_waitcnt vmcnt(0)" ::: "memory");
                    PUBLISH(2);
                    SPIN {
                        v = lds_rd128(ctl + CT_TAG);
                        if (__builtin_amdgcn_readfirstlane(v[2] & v[3])) break;
                        __builtin_amdgcn_s_sleep(1);
                    }
                }
                TS(6);
            }
            if (kb >= NSTAGE && kfree <= (unsigned)(kb - NSTAGE)) {
                u32x4 v = lds_rd128(ctl + CT_FREE);
                kfree = __builtin_amdgcn_readfirstlane(min(min(v[0], v[1]), min(v[2], v[3])));
                if (kfree <= (unsigned)(kb - NSTAGE)) {
                    if (LAG > 1) {
                        asm volatile("s_waitcnt vmcnt(11)" ::: "memory");
                        PUBLISH(kb - 1);
                    }
                    asm volatile("s_waitcnt vmcnt(0)" ::: "memory");
                    PUBLISH(kb);
                    SPIN {
                        v = lds_rd128(ctl + CT_FREE);
                        kfree = __builtin_amdgcn_readfirstlane(min(min(v[0], v[1]), min(v[2], v[3])));
                        if (kfree > (unsigned)(kb - NSTAGE)) break;
                        __builtin_amdgcn_s_sleep(1);
                    }
                }
            }
            LAS uint8_t* base = (LAS uint8_t*)(uintptr_t)(smem_base + (kb % NSTAGE) * STAGE_BYTES + lw * 1024);
#pragma unroll
            for (int i = 0; i < 11; ++i) {
                const int so = i < 4 ? soA + i * 32768 + kb * 1024 : soB + kb * B_STAGE + (i - 4) * 2048;
                __builtin_amdgcn_raw_ptr_buffer_load_lds(rsL, base + i * 2048, 16, lane * 16, so, 0, DMA_AUX);
            }
            if (kb >= LAG) {
                if ((kb & 3) == 3 && kb < 12) asm volatile("s_waitcnt vmcnt(%0)" ::"n"(11 * LAG + 1) : "memory");
                else asm volatile("s_waitcnt vmcnt(%0)" ::"n"(11 * LAG) : "memory");
                PUBLISH(kb - LAG + 1);
                if (kb == 1) asm volatile("" : "+v"(x0), "+v"(x1), "+v"(x2), "+v"(x3));
            }
            if ((kb & 3) == 2 && kb < 12)
                asm volatile("global_load_dwordx2 %0, %1, off sc1" : "=v"(pf) : "v"(fl + ((kb >> 2) + 1) * 32 * FSTR) : "memory");
        }
        asm volatile("s_waitcnt vmcnt(11)" ::: "memory");
        PUBLISH(15);
        asm volatile("s_waitcnt vmcnt(0)" ::: "memory");
        PUBLISH(16);
        TS(7);
    } else if (wid >= 4) {
        if (wid == 4) {
            const float nzx = -(float)zero_x[0];
            {
                GAS u64* fp = flags + (CF2_OFF + (tm * 16 + tn) * 128) / 8;
                typedef u64 u64x8 __attribute__((ext_vector_type(8)));
                int it = 0;
                SPIN {
                    if ((++it & 63) == 0) {
                        u64 x;
                        asm volatile("global_load_dwordx2 %0, %1, off sc1\n\ts_waitcnt vmcnt(0)" : "=v"(x) : "v"(fp + (lane & 15)) : "memory");
                        if (__all(x == tag)) break;
                    } else {
                        u64x8 f, g;
                        asm volatile("s_load_dwordx16 %0, %2, 0x0 glc\n\ts_load_dwordx16 %1, %2, 0x40 glc\n\ts_waitcnt lgkmcnt(0)" : "=&s"(f), "=&s"(g) : "s"(fp) : "memory");
                        u64 d = 0;
#pragma unroll
                        for (int i = 0; i < 8; ++i) {
                            const u64 a = f[i], b = g[i];
                            d |= (a ^ tag) | (b ^ tag);
                        }
                        if (d == 0) break;
                    }
                    __builtin_amdgcn_s_sleep(CSLEEP);
                }
            }
            TS(6);
            const __amdgpu_buffer_rsrc_t rsC = __builtin_amdgcn_make_buffer_rsrc((void*)(ws + WS_CP), 0, 16 * 16 * 512, RSRC_W3);
            typedef short i16x8 __attribute__((ext_vector_type(8)));
            i16x8 cp[8];
#pragma unroll
            for (int x = 0; x < 8; ++x)
                cp[x] = __builtin_bit_cast(i16x8, __builtin_amdgcn_raw_buffer_load_b128(rsC, lane * 16, tn * 8192 + x * 1024, AUX_SC1));
            SPIN {
                const u32x4 v = lds_rd128(ctl + CT_FULL);
                if (__builtin_amdgcn_readfirstlane(v[1]) != 0) break;
                __builtin_amdgcn_s_sleep(2);
            }
            const int l8 = lane < 28 ? lane : 0;
            LAS f32x4* sp = (LAS f32x4*)(uintptr_t)(smem_base + OFF_SCALE + l8 * 32);
            LAS f32x4* bp = (LAS f32x4*)(uintptr_t)(smem_base + OFF_BIAS + l8 * 32);
            const f32x4 s0 = sp[0], s1 = sp[1];
            f32x4 b0 = bp[0], b1 = bp[1];
            const i16x8 u0 = (cp[0] + cp[1]) + (cp[2] + cp[3]), u1 = (cp[4] + cp[5]) + (cp[6] + cp[7]);
#pragma unroll
            for (int e = 0; e < 8; ++e) {
                const short x0 = u0[e], x1 = u1[e];
                int t = (int)x0 + (int)x1;
                t += __shfl_xor(t, 32);
                const float cf = nzx * (float)t;
                if (e < 4) b0[e] = __builtin_fmaf(cf, s0[e], b0[e]);
                else b1[e - 4] = __builtin_fmaf(cf, s1[e - 4], b1[e - 4]);
            }
            if (lane < 28) {
                bp[0] = b0;
                bp[1] = b1;
            }
            asm volatile("s_waitcnt lgkmcnt(0)" ::: "memory");
            lds_wr32(ctl + CT_FULL + 8, 1u);
        }
        TS(8);
        if (bid == 0 && wid == 5) {
            SPIN {
                bool ok = true;
#pragma unroll
                for (int i = 0; i < 4; ++i)
                    ok &= __hip_atomic_load(flags + (3 * 32 + i * 4 + (lane >> 4)) * FSTR + (lane & 15) * FSUB, __ATOMIC_RELAXED, __HIP_MEMORY_SCOPE_AGENT) == tag;
                if (__all(ok)) break;
                __builtin_amdgcn_s_sleep(8);
            }
            if (lane == 0) __hip_atomic_store((GAS u64*)(ws + WS_EPOCH), tag, __ATOMIC_RELAXED, __HIP_MEMORY_SCOPE_AGENT);
        }
    } else {
        const int wm = wid >> 1, wn = wid & 1;
        i32x4 acc[4][6], acm[2][2];
        i32x4 af[2][4], bfr[2][8];
#pragma unroll
        for (int i = 0; i < 4; ++i)
#pragma unroll
            for (int j = 0; j < 6; ++j) {
                acc[i][j] = (i32x4){0, 0, 0, 0};
                asm volatile("" : "+v"(acc[i][j]));
            }
#pragma unroll
        for (int i = 0; i < 2; ++i)
#pragma unroll
            for (int j = 0; j < 2; ++j) {
                acm[i][j] = (i32x4){0, 0, 0, 0};
                asm volatile("" : "+v"(acm[i][j]));
            }
        constexpr int NU = (NSTAGE + 2) / 3;
        unsigned aA0[NU], aA2[NU], aO0[NU], aO1[NU], aM0[NU], aM1[NU];
        {
            const unsigned fA = smem_base + wm * 4096 + lane * 16;
            const unsigned fB = smem_base + A_STAGE + (lane >> 4) * UNIT + (lane & 3) * 896 + ((lane >> 2) & 3) * 32;
            const unsigned x = (lane & 2) * 8;
#pragma unroll
            for (int u = 0; u < NU; ++u) {
                aA0[u] = fA + wn * 2048 + u * 3 * STAGE_BYTES;
                aA2[u] = fA + (wn ^ 1) * 2048 + u * 3 * STAGE_BYTES;
                aO0[u] = fB + wn * 512 + x + u * 3 * STAGE_BYTES;
                aO1[u] = fB + wn * 512 + (x ^ 16) + u * 3 * STAGE_BYTES;
                aM0[u] = fB + 384 + x + u * 3 * STAGE_BYTES;
                aM1[u] = fB + 384 + (x ^ 16) + u * 3 * STAGE_BYTES;
                asm volatile("" : "+v"(aA0[u]), "+v"(aA2[u]), "+v"(aO0[u]), "+v"(aO1[u]), "+v"(aM0[u]), "+v"(aM1[u]));
            }
        }
        const unsigned freew = ctl + CT_FREE + wid * 4;
        unsigned known = 0;
#define DSR(DST, ADDR, OFF) asm volatile("ds_read_b128 %0, %1 offset:%2" : "=v"(DST) : "v"(ADDR), "n"(OFF) : "memory")
#define MF(I, J, SET) asm volatile("v_mfma_i32_16x16x64_i8 %0, %1, %2, %0" : "+v"(acc[I][J]) : "v"(bfr[SET][J]), "v"(af[SET][I]))
#define MM(I, J, SET) asm volatile("v_mfma_i32_16x16x64_i8 %0, %1, %2, %0" : "+v"(acm[I][J]) : "v"(bfr[SET][6 + (J)]), "v"(af[SET][I]))
#define NEED(X)                                                                                                \
    do {                                                                                                       \
        if (__builtin_expect(known <= (unsigned)(X), 0)) {                                                     \
            SPIN {                                                                                             \
                const u32x4 v_ = lds_rd128(ctl + CT_FULL);                                                     \
                known = __builtin_amdgcn_readfirstlane(min(v_[0], v_[1]));                                     \
                if (known > (unsigned)(X)) break;                                                              \
                __builtin_amdgcn_s_sleep(1);                                                                   \
            }                                                                                                  \
        }                                                                                                      \
    } while (0)
#define SU(KB) (((KB) % NSTAGE) / 3)
#define SO(KB) ((((KB) % NSTAGE) % 3) * STAGE_BYTES)
#define RA(SET, KB, I) DSR(af[SET][I], (I) < 2 ? aA0[SU(KB)] : aA2[SU(KB)], SO(KB) + ((I) & 1) * 1024)
#define RB(SET, KB, J) DSR(bfr[SET][J], (J) >= 6 ? ((J) & 1 ? aM1[SU(KB)] : aM0[SU(KB)]) : ((J) & 1 ? aO1[SU(KB)] : aO0[SU(KB)]), SO(KB) + ((J) < 6 ? ((J) >> 1) * 128 : 0))
#define STEP(SET, KB)                                                                        \
    do {                                                                                     \
        MF(0, 0, SET);                                                                       \
        if ((KB) + 1 < 16) {                                                                 \
            NEED((KB) + 1);                                                                  \
            __builtin_amdgcn_sched_barrier(0);                                               \
            MF(1, 0, SET);                                                                   \
            RB((SET) ^ 1, (KB) + 1, 0);   MF(2, 0, SET);                                     \
            RA((SET) ^ 1, (KB) + 1, 0);   MF(3, 0, SET);                                     \
            RA((SET) ^ 1, (KB) + 1, 1);   MF(0, 1, SET);                                     \
            RA((SET) ^ 1, (KB) + 1, 2);   MF(1, 1, SET);                                     \
            RA((SET) ^ 1, (KB) + 1, 3);   MF(2, 1, SET);                                     \
            RB((SET) ^ 1, (KB) + 1, 1);   MF(3, 1, SET);                                     \
            RB((SET) ^ 1, (KB) + 1, 2);   MF(0, 2, SET);                                     \
            RB((SET) ^ 1, (KB) + 1, 3);   MF(1, 2, SET);                                     \
            RB((SET) ^ 1, (KB) + 1, 4);   MF(2, 2, SET);                                     \
            RB((SET) ^ 1, (KB) + 1, 5);   MF(3, 2, SET);                                     \
            RB((SET) ^ 1, (KB) + 1, 6);   MF(0, 3, SET);                                     \
            RB((SET) ^ 1, (KB) + 1, 7);                                                      \
            MF(1, 3, SET); MF(2, 3, SET); MF(3, 3, SET);                                     \
            MF(0, 4, SET); MF(1, 4, SET);                                                    \
            if ((KB) + 2 < 16) asm volatile("ds_read_b64 %0, %1" : "=v"(fv) : "v"(fullv) : "memory"); \
            MF(2, 4, SET); MF(3, 4, SET);                                                    \
            MF(0, 5, SET); MF(1, 5, SET); MF(2, 5, SET); MF(3, 5, SET);                      \
            MM(0, 0, SET);                                                                   \
            asm volatile("s_waitcnt lgkmcnt(0)" : "+v"(fv)::"memory");                       \
            if ((KB) + 1 + NSTAGE < 16) lds_wr32(freew, (KB) + 2);                           \
            MM(1, 0, SET);                                                                   \
            if ((KB) + 2 < 16) known = max(known, (unsigned)__builtin_amdgcn_readfirstlane(min(fv[0], fv[1]))); \
            __builtin_amdgcn_sched_barrier(0);                                               \
            MM(0, 1, SET); MM(1, 1, SET);                                                    \
        } else {                                                                             \
            MF(1, 0, SET); MF(2, 0, SET); MF(3, 0, SET);                                     \
            _Pragma("unroll") for (int j = 1; j < 6; ++j)                                    \
            _Pragma("unroll") for (int i = 0; i < 4; ++i) MF(i, j, SET);                     \
            MM(0, 0, SET); MM(1, 0, SET); MM(0, 1, SET); MM(1, 1, SET);                      \
        }                                                                                    \
        __builtin_amdgcn_sched_barrier(0);                                                   \
    } while (0)
        u32x2 fv = {0, 0};
        unsigned fullv = ctl + CT_FULL;
        asm volatile("" : "+v"(fullv));
        if (SPOLL) {
            if (SGATE) SPIN {
                const u32x4 v = lds_rd128(ctl + CT_ARR);
                if (__builtin_amdgcn_readfirstlane((SGATE == 2 && !(wid & 1)) ? v[0] : v[2])) break;
                __builtin_amdgcn_s_sleep(1);
            }
            TS(1);
            GAS u64* fp = flags + ((wid & 1) ? 16 + tn : tm) * FSTR + (wid >> 1) * 8 * FSUB;
            bool okp = false;
            int it = 0;
            if (CFLAG) {
                fp = flags + (CF_OFF + (tm * 16 + tn) * CSTR + wid * CGS) / 8;
                typedef u64 u64x8 __attribute__((ext_vector_type(8)));
                for (; it < 256; ++it) {
                    u64x8 f;
                    asm volatile("s_load_dwordx16 %0, %1, 0x0 glc\n\ts_waitcnt lgkmcnt(0)" : "=s"(f) : "s"(fp) : "memory");
                    const u64 f0 = f[0], f1 = f[1], f2 = f[2], f3 = f[3], f4 = f[4], f5 = f[5], f6 = f[6], f7 = f[7];
                    const u64 d = (((f0 ^ tag) | (f1 ^ tag)) | ((f2 ^ tag) | (f3 ^ tag))) | (((f4 ^ tag) | (f5 ^ tag)) | ((f6 ^ tag) | (f7 ^ tag)));
                    if (d == 0) { okp = true; break; }
                    __builtin_amdgcn_s_sleep(SSLEEP);
                }
            } else
            for (; it < 256; ++it) {
                u64 f0, f1, f2, f3, f4, f5, f6, f7;
                asm volatile("s_load_dwordx2 %0, %8, %9 glc\n\ts_load_dwordx2 %1, %8, %10 glc\n\ts_load_dwordx2 %2, %8, %11 glc\n\t"
                             "s_load_dwordx2 %3, %8, %12 glc\n\ts_load_dwordx2 %4, %8, %13 glc\n\ts_load_dwordx2 %5, %8, %14 glc\n\t"
                             "s_load_dwordx2 %6, %8, %15 glc\n\ts_load_dwordx2 %7, %8, %16 glc\n\ts_waitcnt lgkmcnt(0)"
                             : "=&s"(f0), "=&s"(f1), "=&s"(f2), "=&s"(f3), "=&s"(f4), "=&s"(f5), "=&s"(f6), "=&s"(f7)
                             : "s"(fp), "n"(0), "n"(FSUB * 8), "n"(FSUB * 16), "n"(FSUB * 24), "n"(FSUB * 32), "n"(FSUB * 40), "n"(FSUB * 48), "n"(FSUB * 56)
                             : "memory");
                const u64 d = (((f0 ^ tag) | (f1 ^ tag)) | ((f2 ^ tag) | (f3 ^ tag))) | (((f4 ^ tag) | (f5 ^ tag)) | ((f6 ^ tag) | (f7 ^ tag)));
                if (d == 0) { okp = true; break; }
                __builtin_amdgcn_s_sleep(SSLEEP);
            }
            if (!okp) SPIN {
                u64 x;
                asm volatile("global_load_dwordx2 %0, %1, off sc1\n\ts_waitcnt vmcnt(0)" : "=v"(x) : "v"(fp + (lane & 7) * (CFLAG ? 1 : FSUB)) : "memory");
                if (__all(x == tag)) break;
                __builtin_amdgcn_s_sleep(PSLEEP);
            }
            lds_wr32(ctl + CT_TAG + wid * 4, 1u);
            TS(2);
        }
        u64 outp = (u64)out;
        asm volatile("" : "+s"(outp));
        const __amdgpu_buffer_rsrc_t rsO = __builtin_amdgcn_make_buffer_rsrc((void*)outp, 0, M_ * N_ * 2, RSRC_W3);
        unsigned ro0, ro1, ro2, ro3, rm0, rm1, cado, cadm, xpr, crdy = 0;
        const unsigned sigw = ctl + CT_SIG + wid * 4;
        u32x4 fr;
        typedef unsigned u32x3 __attribute__((ext_vector_type(3)));
        u32x3 fq;
        {
            const unsigned ovo = (unsigned)(((tm * BM + wm * 64 + (lane >> 2)) * N_ + tn * BN + (lane & 3) * 8) * 2);
            xpr = ((lane & 3) * 16 + (lane >> 2)) * 4;
            rm0 = ovo + wn * (32 * N_ * 2) + 192;
            rm1 = rm0 + 16 * N_ * 2;
            ro0 = ovo + wn * (32 * N_ * 2) + wn * 256;
            ro1 = ro0 + 16 * N_ * 2;
            ro2 = ovo + (wn ^ 1) * (32 * N_ * 2) + wn * 256;
            ro3 = ro2 + 16 * N_ * 2;
            cado = smem_base + OFF_SCALE + (lane >> 4) * 32 + wn * 512;
            cadm = smem_base + OFF_SCALE + (lane >> 4) * 32 + 384;
            asm volatile("" : "+v"(ro0), "+v"(ro1), "+v"(ro2), "+v"(ro3), "+v"(rm0), "+v"(rm1), "+v"(cado), "+v"(cadm), "+v"(xpr));
        }
#define SB() __builtin_amdgcn_sched_barrier(0)
#define MFX(ACC, B, A) asm volatile("v_mfma_i32_16x16x64_i8 %0, %1, %2, %0" : "+v"(ACC) : "v"(B), "v"(A))
#define CVF(F, X) do { SB(); F = (float)(X); SB(); } while (0)
#define FMA(R, F, S, B) do { const float s_ = S, b_ = B; asm volatile("v_fma_f32 %0, %1, %2, %3" : "=v"(R) : "v"(F), "v"(s_), "v"(b_)); } while (0)
#define CPK(O, R0, R1) do { SB(); const f32x2 v_ = {R0, R1}; O = __builtin_bit_cast(unsigned, __builtin_convertvector(v_, bf16x2_t)); SB(); } while (0)
#define BPM(X, O) asm volatile("ds_bpermute_b32 %0, %1, %2" : "=v"(X) : "v"(xpr), "v"(O) : "memory")
#define XPW(O0, O1, O2, O3) do { const u32x4 o_ = {O0, O1, O2, O3}; asm volatile("ds_write_b128 %0, %1" ::"v"(xpw), "v"(o_) : "memory"); } while (0)
#define STO(X, ADDR) __builtin_amdgcn_raw_buffer_store_b128(X, rsO, ADDR, 0, OUT_AUX)
#define FREEW()                                                                                                \
    do {                                                                                                       \
        if (__builtin_expect(__builtin_amdgcn_readfirstlane(min(min(fr[0], fr[1]), min(fr[2], fr[3]))) < 12, 0)) { \
            SPIN {                                                                                             \
                const u32x4 v_ = lds_rd128(ctl + CT_FREE);                                                     \
                if (__builtin_amdgcn_readfirstlane(min(min(v_[0], v_[1]), min(v_[2], v_[3]))) >= 12) break;    \
                __builtin_amdgcn_s_sleep(1);                                                                   \
            }                                                                                                  \
        }                                                                                                      \
    } while (0)
#define CORRW()                                                                                                \
    do {                                                                                                       \
        if (__builtin_expect(!crdy, 0)) {                                                                      \
            SPIN {                                                                                             \
                const u32x4 v_ = lds_rd128(ctl + CT_FULL);                                                     \
                if (__builtin_amdgcn_readfirstlane(v_[2])) break;                                              \
                __builtin_amdgcn_s_sleep(1);                                                                   \
            }                                                                                                  \
        }                                                                                                      \
    } while (0)
        asm volatile("" ::"v"(pfd));
        NEED(0);
        TS(4);
        RB(0, 0, 0); RA(0, 0, 0); RA(0, 0, 1); RA(0, 0, 2); RA(0, 0, 3);
        RB(0, 0, 1); RB(0, 0, 2); RB(0, 0, 3); RB(0, 0, 4); RB(0, 0, 5); RB(0, 0, 6); RB(0, 0, 7);
        asm volatile("s_waitcnt lgkmcnt(0)" ::: "memory");
        lds_wr32(freew, 1);
        __builtin_amdgcn_sched_barrier(0);
        STEP(0, 0);
        STEP(1, 1);
        STEP(0, 2);
        STEP(1, 3);
        TS(5);
        STEP(0, 4);
        STEP(1, 5);
        STEP(0, 6);
        STEP(1, 7);
        TS(6);
        STEP(0, 8);
        STEP(1, 9);
        STEP(0, 10);
        i32x4 ta0_12, ta0_13, ta0_14, ta0_15, ta1_12, ta1_13, ta1_14, ta1_15, ta2_12, ta2_13, ta2_14, ta2_15, ta3_12, ta3_13, ta3_14, ta3_15, tb00_12, tb00_13, tb00_14, tb00_15, tb01_12, tb01_13, tb01_14, tb01_15, tb10_12, tb10_13, tb10_14, tb10_15, tb11_12, tb11_13, tb11_14, tb11_15, tb20_12, tb20_13, tb20_14, tb20_15, tb21_12, tb21_13, tb21_14, tb21_15, tb30_12, tb30_13, tb30_14, tb30_15, tb31_12, tb31_13, tb31_14, tb31_15;
        f32x4 kb0_0, kb0_1, kb1_0, kb1_1, kb2_0, kb2_1, kb3_0, kb3_1, ks0_0, ks0_1, ks1_0, ks1_1, ks2_0, ks2_1, ks3_0, ks3_1;
        float f30_0, f30_1, f30_2, f30_3, f30_4, f30_5, f30_6, f30_7, f31_0, f31_1, f31_2, f31_3, f31_4, f31_5, f31_6, f31_7, f00_0, f00_1, f00_2, f00_3, f00_4, f00_5, f00_6, f00_7, f01_0, f01_1, f01_2, f01_3, f01_4, f01_5, f01_6, f01_7, f02_0, f02_1, f02_2, f02_3, f02_4, f02_5, f02_6, f02_7, f03_0, f03_1, f03_2, f03_3, f03_4, f03_5, f03_6, f03_7, f10_0, f10_1, f10_2, f10_3, f10_4, f10_5, f10_6, f10_7, f11_0, f11_1, f11_2, f11_3, f11_4, f11_5, f11_6, f11_7, f12_0, f12_1, f12_2, f12_3, f12_4, f12_5, f12_6, f12_7, f13_0, f13_1, f13_2, f13_3, f13_4, f13_5, f13_6, f13_7, f20_0, f20_1, f20_2, f20_3, f20_4, f20_5, f20_6, f20_7, f21_0, f21_1, f21_2, f21_3, f21_4, f21_5, f21_6, f21_7, f22_0, f22_1, f22_2, f22_3, f22_4, f22_5, f22_6, f22_7, f23_0, f23_1, f23_2, f23_3, f23_4, f23_5, f23_6, f23_7;
        float r30_0, r30_1, r30_2, r30_3, r30_4, r30_5, r30_6, r30_7, r31_0, r31_1, r31_2, r31_3, r31_4, r31_5, r31_6, r31_7, r00_0, r00_1, r00_2, r00_3, r00_4, r00_5, r00_6, r00_7, r01_0, r01_1, r01_2, r01_3, r01_4, r01_5, r01_6, r01_7, r02_0, r02_1, r02_2, r02_3, r02_4, r02_5, r02_6, r02_7, r03_0, r03_1, r03_2, r03_3, r03_4, r03_5, r03_6, r03_7, r10_0, r10_1, r10_2, r10_3, r10_4, r10_5, r10_6, r10_7, r11_0, r11_1, r11_2, r11_3, r11_4, r11_5, r11_6, r11_7, r12_0, r12_1, r12_2, r12_3, r12_4, r12_5, r12_6, r12_7, r13_0, r13_1, r13_2, r13_3, r13_4, r13_5, r13_6, r13_7, r20_0, r20_1, r20_2, r20_3, r20_4, r20_5, r20_6, r20_7, r21_0, r21_1, r21_2, r21_3, r21_4, r21_5, r21_6, r21_7, r22_0, r22_1, r22_2, r22_3, r22_4, r22_5, r22_6, r22_7, r23_0, r23_1, r23_2, r23_3, r23_4, r23_5, r23_6, r23_7;
        unsigned o30_0, o30_1, o30_2, o30_3, o31_0, o31_1, o31_2, o31_3, o00_0, o00_1, o00_2, o00_3, o01_0, o01_1, o01_2, o01_3, o02_0, o02_1, o02_2, o02_3, o03_0, o03_1, o03_2, o03_3, o10_0, o10_1, o10_2, o10_3, o11_0, o11_1, o11_2, o11_3, o12_0, o12_1, o12_2, o12_3, o13_0, o13_1, o13_2, o13_3, o20_0, o20_1, o20_2, o20_3, o21_0, o21_1, o21_2, o21_3, o22_0, o22_1, o22_2, o22_3, o23_0, o23_1, o23_2, o23_3;
        unsigned x30_0, x30_1, x30_2, x30_3, x31_0, x31_1, x31_2, x31_3, x00_0, x00_1, x00_2, x00_3, x01_0, x01_1, x01_2, x01_3, x02_0, x02_1, x02_2, x02_3, x03_0, x03_1, x03_2, x03_3, x10_0, x10_1, x10_2, x10_3, x11_0, x11_1, x11_2, x11_3, x12_0, x12_1, x12_2, x12_3, x13_0, x13_1, x13_2, x13_3, x20_0, x20_1, x20_2, x20_3, x21_0, x21_1, x21_2, x21_3, x22_0, x22_1, x22_2, x22_3, x23_0, x23_1, x23_2, x23_3;
        MF(0, 0, 1);
        MF(1, 0, 1);
        MF(2, 0, 1);
        MF(3, 0, 1);
        MF(0, 1, 1);
        MF(1, 1, 1);
        MF(2, 1, 1);
        MF(3, 1, 1);
        MF(0, 2, 1);
        MF(1, 2, 1);
        MF(2, 2, 1);
        MF(3, 2, 1);
        MF(0, 3, 1);
        MF(1, 3, 1);
        MF(2, 3, 1);
        asm volatile("ds_read_b96 %0, %1" : "=v"(fq) : "v"(fullv) : "memory");
        MF(3, 3, 1);
        MF(0, 4, 1);
        MF(1, 4, 1);
        MF(2, 4, 1);
        MF(3, 4, 1);
        asm volatile("s_waitcnt lgkmcnt(0)" : "+v"(fq)::"memory");
        known = max(known, (unsigned)__builtin_amdgcn_readfirstlane(min(fq[0], fq[1])));
        NEED(12);
        SB();
        asm volatile("ds_read_b96 %0, %1" : "=v"(fq) : "v"(fullv) : "memory");
        DSR(ta0_12, aA0[SU(12)], SO(12) + 0);
        DSR(tb30_12, aM0[SU(12)], SO(12));
        MF(0, 5, 1);
        DSR(ta1_12, aA0[SU(12)], SO(12) + 1024);
        MF(1, 5, 1);
        DSR(tb31_12, aM1[SU(12)], SO(12));
        MF(2, 5, 1);
        MF(3, 5, 1);
        asm volatile("s_waitcnt lgkmcnt(4)" : "+v"(fq)::"memory");
        known = max(known, (unsigned)__builtin_amdgcn_readfirstlane(min(fq[0], fq[1])));
        NEED(13);
        SB();
        asm volatile("ds_read_b96 %0, %1" : "=v"(fq) : "v"(fullv) : "memory");
        DSR(ta0_13, aA0[SU(13)], SO(13) + 0);
        DSR(tb30_13, aM0[SU(13)], SO(13));
        MM(0, 0, 1);
        DSR(ta1_13, aA0[SU(13)], SO(13) + 1024);
        MM(1, 0, 1);
        DSR(tb31_13, aM1[SU(13)], SO(13));
        MM(0, 1, 1);
        MM(1, 1, 1);
        TS(7);
        asm volatile("s_waitcnt lgkmcnt(4)" : "+v"(fq)::"memory");
        known = max(known, (unsigned)__builtin_amdgcn_readfirstlane(min(fq[0], fq[1])));
        NEED(14);
        SB();
        asm volatile("ds_read_b96 %0, %1" : "=v"(fq) : "v"(fullv) : "memory");
        DSR(ta0_14, aA0[SU(14)], SO(14) + 0);
        DSR(tb30_14, aM0[SU(14)], SO(14));
        MFX(acm[0][0], tb30_12, ta0_12);
        DSR(ta1_14, aA0[SU(14)], SO(14) + 1024);
        MFX(acm[1][0], tb30_12, ta1_12);
        DSR(tb31_14, aM1[SU(14)], SO(14));
        MFX(acm[0][1], tb31_12, ta0_12);
        MFX(acm[1][1], tb31_12, ta1_12);
        asm volatile("s_waitcnt lgkmcnt(4)" : "+v"(fq)::"memory");
        known = max(known, (unsigned)__builtin_amdgcn_readfirstlane(min(fq[0], fq[1])));
        NEED(15);
        SB();
        asm volatile("ds_read_b96 %0, %1" : "=v"(fq) : "v"(fullv) : "memory");
        DSR(ta0_15, aA0[SU(15)], SO(15) + 0);
        DSR(tb30_15, aM0[SU(15)], SO(15));
        MFX(acm[0][0], tb30_13, ta0_13);
        DSR(ta1_15, aA0[SU(15)], SO(15) + 1024);
        MFX(acm[1][0], tb30_13, ta1_13);
        DSR(tb31_15, aM1[SU(15)], SO(15));
        MFX(acm[0][1], tb31_13, ta0_13);
        MFX(acm[1][1], tb31_13, ta1_13);
        DSR(tb00_12, aO0[SU(12)], SO(12) + 0);
        asm volatile("s_waitcnt lgkmcnt(7)" ::: "memory");
        MFX(acm[0][0], tb30_14, ta0_14);
        asm volatile("s_waitcnt lgkmcnt(5)" : "+v"(fq)::"memory");
        known = max(known, (unsigned)__builtin_amdgcn_readfirstlane(min(fq[0], fq[1]))); crdy = __builtin_amdgcn_readfirstlane(fq[2]);
        CORRW();
        SB();
        DSR(ks3_0, cadm, 0);
        DSR(tb01_12, aO1[SU(12)], SO(12) + 0);
        MFX(acm[1][0], tb30_14, ta1_14);
        DSR(kb3_0, cadm, 896);
        DSR(tb00_13, aO0[SU(13)], SO(13) + 0);
        MFX(acm[0][1], tb31_14, ta0_14);
        DSR(ks3_1, cadm, 16);
        DSR(tb01_13, aO1[SU(13)], SO(13) + 0);
        MFX(acm[1][1], tb31_14, ta1_14);
        DSR(kb3_1, cadm, 912);
        DSR(tb00_14, aO0[SU(14)], SO(14) + 0);
        asm volatile("s_waitcnt lgkmcnt(10)" ::: "memory");
        MFX(acm[0][0], tb30_15, ta0_15);
        DSR(tb01_14, aO1[SU(14)], SO(14) + 0);
        MFX(acm[1][0], tb30_15, ta1_15);
        DSR(tb00_15, aO0[SU(15)], SO(15) + 0);
        asm volatile("s_waitcnt lgkmcnt(11)" ::: "memory");
        MFX(acm[0][1], tb31_15, ta0_15);
        DSR(tb01_15, aO1[SU(15)], SO(15) + 0);
        MFX(acm[1][1], tb31_15, ta1_15);
        CVF(f30_0, acm[0][0][0]);
        CVF(f30_1, acm[0][0][1]);
        CVF(f30_2, acm[0][0][2]);
        asm volatile("s_waitcnt lgkmcnt(11)" ::: "memory");
        MFX(acc[0][0], tb00_12, ta0_12);
        CVF(f30_3, acm[0][0][3]);
        asm volatile("s_waitcnt lgkmcnt(4)" ::: "memory");
        FMA(r30_0, f30_0, ks3_0[0], kb3_0[0]);
        MFX(acc[0][1], tb01_12, ta0_12);
        CVF(f30_4, acm[0][1][0]);
        FMA(r30_1, f30_1, ks3_0[1], kb3_0[1]);
        CVF(f30_5, acm[0][1][1]);
        FMA(r30_2, f30_2, ks3_0[2], kb3_0[2]);
        MFX(acc[0][0], tb00_13, ta0_13);
        CVF(f30_6, acm[0][1][2]);
        FMA(r30_3, f30_3, ks3_0[3], kb3_0[3]);
        CVF(f30_7, acm[0][1][3]);
        FMA(r30_4, f30_4, ks3_1[0], kb3_1[0]);
        MFX(acc[0][1], tb01_13, ta0_13);
        FMA(r30_5, f30_5, ks3_1[1], kb3_1[1]);
        FMA(r30_6, f30_6, ks3_1[2], kb3_1[2]);
        FMA(r30_7, f30_7, ks3_1[3], kb3_1[3]);
        CPK(o30_0, r30_0, r30_1);
        asm volatile("s_waitcnt lgkmcnt(2)" ::: "memory");
        MFX(acc[0][0], tb00_14, ta0_14);
        CPK(o30_1, r30_2, r30_3);
        BPM(x30_0, o30_0);
        CPK(o30_2, r30_4, r30_5);
        BPM(x30_1, o30_1);
        MFX(acc[0][1], tb01_14, ta0_14);
        CPK(o30_3, r30_6, r30_7);
        BPM(x30_2, o30_2);
        BPM(x30_3, o30_3);
        CVF(f31_0, acm[1][0][0]);
        asm volatile("s_waitcnt lgkmcnt(4)" ::: "memory");
        MFX(acc[0][0], tb00_15, ta0_15);
        DSR(ks0_0, cado, 0);
        CVF(f31_1, acm[1][0][1]);
        CVF(f31_2, acm[1][0][2]);
        CVF(f31_3, acm[1][0][3]);
        FMA(r31_0, f31_0, ks3_0[0], kb3_0[0]);
        MFX(acc[0][1], tb01_15, ta0_15);
        DSR(kb0_0, cado, 896);
        DSR(ta2_12, aA2[SU(12)], SO(12) + 0);
        CVF(f31_4, acm[1][1][0]);
        FMA(r31_1, f31_1, ks3_0[1], kb3_0[1]);
        MFX(acc[1][0], tb00_12, ta1_12);
        DSR(ks0_1, cado, 16);
        CVF(f31_5, acm[1][1][1]);
        FMA(r31_2, f31_2, ks3_0[2], kb3_0[2]);
        CVF(f31_6, acm[1][1][2]);
        MFX(acc[1][1], tb01_12, ta1_12);
        DSR(kb0_1, cado, 912);
        DSR(ta2_13, aA2[SU(13)], SO(13) + 0);
        FMA(r31_3, f31_3, ks3_0[3], kb3_0[3]);
        CVF(f31_7, acm[1][1][3]);
        MFX(acc[1][0], tb00_13, ta1_13);
        FMA(r31_4, f31_4, ks3_1[0], kb3_1[0]);
        FMA(r31_5, f31_5, ks3_1[1], kb3_1[1]);
        FMA(r31_6, f31_6, ks3_1[2], kb3_1[2]);
        FMA(r31_7, f31_7, ks3_1[3], kb3_1[3]);
        MFX(acc[1][1], tb01_13, ta1_13);
        DSR(ta2_14, aA2[SU(14)], SO(14) + 0);
        asm volatile("s_waitcnt lgkmcnt(7)" : "+v"(x30_0), "+v"(x30_1), "+v"(x30_2), "+v"(x30_3)::"memory");
        TS(9);
        STO(((u32x4){x30_0, x30_1, x30_2, x30_3}), rm0 + 0);
        CPK(o31_0, r31_0, r31_1);
        CPK(o31_1, r31_2, r31_3);
        MFX(acc[1][0], tb00_14, ta1_14);
        BPM(x31_0, o31_0);
        CPK(o31_2, r31_4, r31_5);
        BPM(x31_1, o31_1);
        CPK(o31_3, r31_6, r31_7);
        MFX(acc[1][1], tb01_14, ta1_14);
        DSR(ta2_15, aA2[SU(15)], SO(15) + 0);
        BPM(x31_2, o31_2);
        BPM(x31_3, o31_3);
        CVF(f00_0, acc[0][0][0]);
        MFX(acc[1][0], tb00_15, ta1_15);
        CVF(f00_1, acc[0][0][1]);
        CVF(f00_2, acc[0][0][2]);
        CVF(f00_3, acc[0][0][3]);
        asm volatile("s_waitcnt lgkmcnt(7)" ::: "memory");
        FMA(r00_0, f00_0, ks0_0[0], kb0_0[0]);
        MFX(acc[1][1], tb01_15, ta1_15);
        DSR(ta3_12, aA2[SU(12)], SO(12) + 1024);
        CVF(f00_4, acc[0][1][0]);
        FMA(r00_1, f00_1, ks0_0[1], kb0_0[1]);
        MFX(acc[2][0], tb00_12, ta2_12);
        CVF(f00_5, acc[0][1][1]);
        FMA(r00_2, f00_2, ks0_0[2], kb0_0[2]);
        CVF(f00_6, acc[0][1][2]);
        MFX(acc[2][1], tb01_12, ta2_12);
        DSR(ta3_13, aA2[SU(13)], SO(13) + 1024);
        FMA(r00_3, f00_3, ks0_0[3], kb0_0[3]);
        CVF(f00_7, acc[0][1][3]);
        asm volatile("s_waitcnt lgkmcnt(8)" ::: "memory");
        MFX(acc[2][0], tb00_13, ta2_13);
        FMA(r00_4, f00_4, ks0_1[0], kb0_1[0]);
        FMA(r00_5, f00_5, ks0_1[1], kb0_1[1]);
        FMA(r00_6, f00_6, ks0_1[2], kb0_1[2]);
        FMA(r00_7, f00_7, ks0_1[3], kb0_1[3]);
        MFX(acc[2][1], tb01_13, ta2_13);
        DSR(ta3_14, aA2[SU(14)], SO(14) + 1024);
        asm volatile("s_waitcnt lgkmcnt(3)" : "+v"(x31_0), "+v"(x31_1), "+v"(x31_2), "+v"(x31_3)::"memory");
        STO(((u32x4){x31_0, x31_1, x31_2, x31_3}), rm1 + 0);
        CPK(o00_0, r00_0, r00_1);
        CPK(o00_1, r00_2, r00_3);
        MFX(acc[2][0], tb00_14, ta2_14);
        BPM(x00_0, o00_0);
        CPK(o00_2, r00_4, r00_5);
        BPM(x00_1, o00_1);
        CPK(o00_3, r00_6, r00_7);
        MFX(acc[2][1], tb01_14, ta2_14);
        DSR(ta3_15, aA2[SU(15)], SO(15) + 1024);
        BPM(x00_2, o00_2);
        BPM(x00_3, o00_3);
        CVF(f01_0, acc[1][0][0]);
        MFX(acc[2][0], tb00_15, ta2_15);
        CVF(f01_1, acc[1][0][1]);
        CVF(f01_2, acc[1][0][2]);
        CVF(f01_3, acc[1][0][3]);
        FMA(r01_0, f01_0, ks0_0[0], kb0_0[0]);
        MFX(acc[2][1], tb01_15, ta2_15);
        DSR(tb10_12, aO0[SU(12)], SO(12) + 128);
        CVF(f01_4, acc[1][1][0]);
        FMA(r01_1, f01_1, ks0_0[1], kb0_0[1]);
        asm volatile("s_waitcnt lgkmcnt(8)" ::: "memory");
        MFX(acc[3][0], tb00_12, ta3_12);
        DSR(tb11_12, aO1[SU(12)], SO(12) + 128);
        CVF(f01_5, acc[1][1][1]);
        FMA(r01_2, f01_2, ks0_0[2], kb0_0[2]);
        MFX(acc[3][1], tb01_12, ta3_12);
        DSR(tb10_13, aO0[SU(13)], SO(13) + 128);
        CVF(f01_6, acc[1][1][2]);
        FMA(r01_3, f01_3, ks0_0[3], kb0_0[3]);
        asm volatile("s_waitcnt lgkmcnt(9)" ::: "memory");
        MFX(acc[3][0], tb00_13, ta3_13);
        DSR(tb11_13, aO1[SU(13)], SO(13) + 128);
        CVF(f01_7, acc[1][1][3]);
        FMA(r01_4, f01_4, ks0_1[0], kb0_1[0]);
        FMA(r01_5, f01_5, ks0_1[1], kb0_1[1]);
        MFX(acc[3][1], tb01_13, ta3_13);
        DSR(tb10_14, aO0[SU(14)], SO(14) + 128);
        asm volatile("s_waitcnt lgkmcnt(5)" : "+v"(x00_0), "+v"(x00_1), "+v"(x00_2), "+v"(x00_3)::"memory");
        STO(((u32x4){x00_0, x00_1, x00_2, x00_3}), ro0 + 0);
        FMA(r01_6, f01_6, ks0_1[2], kb0_1[2]);
        FMA(r01_7, f01_7, ks0_1[3], kb0_1[3]);
        MFX(acc[3][0], tb00_14, ta3_14);
        DSR(tb11_14, aO1[SU(14)], SO(14) + 128);
        CPK(o01_0, r01_0, r01_1);
        CPK(o01_1, r01_2, r01_3);
        BPM(x01_0, o01_0);
        MFX(acc[3][1], tb01_14, ta3_14);
        DSR(tb10_15, aO0[SU(15)], SO(15) + 128);
        CPK(o01_2, r01_4, r01_5);
        BPM(x01_1, o01_1);
        CPK(o01_3, r01_6, r01_7);
        MFX(acc[3][0], tb00_15, ta3_15);
        DSR(tb11_15, aO1[SU(15)], SO(15) + 128);
        BPM(x01_2, o01_2);
        BPM(x01_3, o01_3);
        CVF(f02_0, acc[2][0][0]);
        MFX(acc[3][1], tb01_15, ta3_15);
        CVF(f02_1, acc[2][0][1]);
        CVF(f02_2, acc[2][0][2]);
        CVF(f02_3, acc[2][0][3]);
        FMA(r02_0, f02_0, ks0_0[0], kb0_0[0]);
        asm volatile("s_waitcnt lgkmcnt(10)" ::: "memory");
        MFX(acc[0][2], tb10_12, ta0_12);
        CVF(f02_4, acc[2][1][0]);
        FMA(r02_1, f02_1, ks0_0[1], kb0_0[1]);
        CVF(f02_5, acc[2][1][1]);
        MFX(acc[0][3], tb11_12, ta0_12);
        FMA(r02_2, f02_2, ks0_0[2], kb0_0[2]);
        CVF(f02_6, acc[2][1][2]);
        FMA(r02_3, f02_3, ks0_0[3], kb0_0[3]);
        asm volatile("s_waitcnt lgkmcnt(8)" ::: "memory");
        MFX(acc[0][2], tb10_13, ta0_13);
        CVF(f02_7, acc[2][1][3]);
        FMA(r02_4, f02_4, ks0_1[0], kb0_1[0]);
        FMA(r02_5, f02_5, ks0_1[1], kb0_1[1]);
        FMA(r02_6, f02_6, ks0_1[2], kb0_1[2]);
        MFX(acc[0][3], tb11_13, ta0_13);
        FMA(r02_7, f02_7, ks0_1[3], kb0_1[3]);
        CPK(o02_0, r02_0, r02_1);
        CPK(o02_1, r02_2, r02_3);
        BPM(x02_0, o02_0);
        asm volatile("s_waitcnt lgkmcnt(7)" ::: "memory");
        MFX(acc[0][2], tb10_14, ta0_14);
        asm volatile("s_waitcnt lgkmcnt(1)" : "+v"(x01_0), "+v"(x01_1), "+v"(x01_2), "+v"(x01_3)::"memory");
        STO(((u32x4){x01_0, x01_1, x01_2, x01_3}), ro1 + 0);
        CPK(o02_2, r02_4, r02_5);
        BPM(x02_1, o02_1);
        CPK(o02_3, r02_6, r02_7);
        MFX(acc[0][3], tb11_14, ta0_14);
        DSR(ks1_0, cado, 128);
        BPM(x02_2, o02_2);
        BPM(x02_3, o02_3);
        CVF(f03_0, acc[3][0][0]);
        CVF(f03_1, acc[3][0][1]);
        MFX(acc[0][2], tb10_15, ta0_15);
        DSR(kb1_0, cado, 1024);
        CVF(f03_2, acc[3][0][2]);
        CVF(f03_3, acc[3][0][3]);
        FMA(r03_0, f03_0, ks0_0[0], kb0_0[0]);
        CVF(f03_4, acc[3][1][0]);
        MFX(acc[0][3], tb11_15, ta0_15);
        DSR(ks1_1, cado, 144);
        FMA(r03_1, f03_1, ks0_0[1], kb0_0[1]);
        CVF(f03_5, acc[3][1][1]);
        FMA(r03_2, f03_2, ks0_0[2], kb0_0[2]);
        MFX(acc[1][2], tb10_12, ta1_12);
        DSR(kb1_1, cado, 1040);
        CVF(f03_6, acc[3][1][2]);
        FMA(r03_3, f03_3, ks0_0[3], kb0_0[3]);
        CVF(f03_7, acc[3][1][3]);
        MFX(acc[1][3], tb11_12, ta1_12);
        FMA(r03_4, f03_4, ks0_1[0], kb0_1[0]);
        FMA(r03_5, f03_5, ks0_1[1], kb0_1[1]);
        FMA(r03_6, f03_6, ks0_1[2], kb0_1[2]);
        MFX(acc[1][2], tb10_13, ta1_13);
        FMA(r03_7, f03_7, ks0_1[3], kb0_1[3]);
        CPK(o03_0, r03_0, r03_1);
        CPK(o03_1, r03_2, r03_3);
        BPM(x03_0, o03_0);
        MFX(acc[1][3], tb11_13, ta1_13);
        asm volatile("s_waitcnt lgkmcnt(4)" : "+v"(x02_0), "+v"(x02_1), "+v"(x02_2), "+v"(x02_3)::"memory");
        STO(((u32x4){x02_0, x02_1, x02_2, x02_3}), ro2 + 0);
        CPK(o03_2, r03_4, r03_5);
        BPM(x03_1, o03_1);
        CPK(o03_3, r03_6, r03_7);
        MFX(acc[1][2], tb10_14, ta1_14);
        BPM(x03_2, o03_2);
        BPM(x03_3, o03_3);
        CVF(f10_0, acc[0][2][0]);
        CVF(f10_1, acc[0][2][1]);
        MFX(acc[1][3], tb11_14, ta1_14);
        CVF(f10_2, acc[0][2][2]);
        CVF(f10_3, acc[0][2][3]);
        asm volatile("s_waitcnt lgkmcnt(4)" ::: "memory");
        FMA(r10_0, f10_0, ks1_0[0], kb1_0[0]);
        CVF(f10_4, acc[0][3][0]);
        MFX(acc[1][2], tb10_15, ta1_15);
        FMA(r10_1, f10_1, ks1_0[1], kb1_0[1]);
        CVF(f10_5, acc[0][3][1]);
        FMA(r10_2, f10_2, ks1_0[2], kb1_0[2]);
        MFX(acc[1][3], tb11_15, ta1_15);
        CVF(f10_6, acc[0][3][2]);
        FMA(r10_3, f10_3, ks1_0[3], kb1_0[3]);
        CVF(f10_7, acc[0][3][3]);
        MFX(acc[2][2], tb10_12, ta2_12);
        FMA(r10_4, f10_4, ks1_1[0], kb1_1[0]);
        FMA(r10_5, f10_5, ks1_1[1], kb1_1[1]);
        FMA(r10_6, f10_6, ks1_1[2], kb1_1[2]);
        MFX(acc[2][3], tb11_12, ta2_12);
        FMA(r10_7, f10_7, ks1_1[3], kb1_1[3]);
        CPK(o10_0, r10_0, r10_1);
        CPK(o10_1, r10_2, r10_3);
        MFX(acc[2][2], tb10_13, ta2_13);
        asm volatile("s_waitcnt lgkmcnt(0)" : "+v"(x03_0), "+v"(x03_1), "+v"(x03_2), "+v"(x03_3)::"memory");
        STO(((u32x4){x03_0, x03_1, x03_2, x03_3}), ro3 + 0);
        BPM(x10_0, o10_0);
        CPK(o10_2, r10_4, r10_5);
        BPM(x10_1, o10_1);
        MFX(acc[2][3], tb11_13, ta2_13);
        CPK(o10_3, r10_6, r10_7);
        BPM(x10_2, o10_2);
        BPM(x10_3, o10_3);
        CVF(f11_0, acc[1][2][0]);
        MFX(acc[2][2], tb10_14, ta2_14);
        CVF(f11_1, acc[1][2][1]);
        CVF(f11_2, acc[1][2][2]);
        CVF(f11_3, acc[1][2][3]);
        FMA(r11_0, f11_0, ks1_0[0], kb1_0[0]);
        MFX(acc[2][3], tb11_14, ta2_14);
        CVF(f11_4, acc[1][3][0]);
        FMA(r11_1, f11_1, ks1_0[1], kb1_0[1]);
        CVF(f11_5, acc[1][3][1]);
        MFX(acc[2][2], tb10_15, ta2_15);
        FMA(r11_2, f11_2, ks1_0[2], kb1_0[2]);
        CVF(f11_6, acc[1][3][2]);
        FMA(r11_3, f11_3, ks1_0[3], kb1_0[3]);
        MFX(acc[2][3], tb11_15, ta2_15);
        DSR(tb20_12, aO0[SU(12)], SO(12) + 256);
        CVF(f11_7, acc[1][3][3]);
        FMA(r11_4, f11_4, ks1_1[0], kb1_1[0]);
        MFX(acc[3][2], tb10_12, ta3_12);
        DSR(tb21_12, aO1[SU(12)], SO(12) + 256);
        FMA(r11_5, f11_5, ks1_1[1], kb1_1[1]);
        FMA(r11_6, f11_6, ks1_1[2], kb1_1[2]);
        MFX(acc[3][3], tb11_12, ta3_12);
        DSR(tb20_13, aO0[SU(13)], SO(13) + 256);
        asm volatile("s_waitcnt lgkmcnt(3)" : "+v"(x10_0), "+v"(x10_1), "+v"(x10_2), "+v"(x10_3)::"memory");
        STO(((u32x4){x10_0, x10_1, x10_2, x10_3}), ro0 + 64);
        FMA(r11_7, f11_7, ks1_1[3], kb1_1[3]);
        MFX(acc[3][2], tb10_13, ta3_13);
        DSR(tb21_13, aO1[SU(13)], SO(13) + 256);
        CPK(o11_0, r11_0, r11_1);
        CPK(o11_1, r11_2, r11_3);
        BPM(x11_0, o11_0);
        MFX(acc[3][3], tb11_13, ta3_13);
        DSR(tb20_14, aO0[SU(14)], SO(14) + 256);
        CPK(o11_2, r11_4, r11_5);
        BPM(x11_1, o11_1);
        CPK(o11_3, r11_6, r11_7);
        MFX(acc[3][2], tb10_14, ta3_14);
        DSR(tb21_14, aO1[SU(14)], SO(14) + 256);
        BPM(x11_2, o11_2);
        BPM(x11_3, o11_3);
        CVF(f12_0, acc[2][2][0]);
        MFX(acc[3][3], tb11_14, ta3_14);
        DSR(tb20_15, aO0[SU(15)], SO(15) + 256);
        CVF(f12_1, acc[2][2][1]);
        CVF(f12_2, acc[2][2][2]);
        CVF(f12_3, acc[2][2][3]);
        MFX(acc[3][2], tb10_15, ta3_15);
        DSR(tb21_15, aO1[SU(15)], SO(15) + 256);
        FMA(r12_0, f12_0, ks1_0[0], kb1_0[0]);
        CVF(f12_4, acc[2][3][0]);
        MFX(acc[3][3], tb11_15, ta3_15);
        FMA(r12_1, f12_1, ks1_0[1], kb1_0[1]);
        CVF(f12_5, acc[2][3][1]);
        FMA(r12_2, f12_2, ks1_0[2], kb1_0[2]);
        asm volatile("s_waitcnt lgkmcnt(10)" ::: "memory");
        MFX(acc[0][4], tb20_12, ta0_12);
        CVF(f12_6, acc[2][3][2]);
        FMA(r12_3, f12_3, ks1_0[3], kb1_0[3]);
        CVF(f12_7, acc[2][3][3]);
        MFX(acc[0][5], tb21_12, ta0_12);
        FMA(r12_4, f12_4, ks1_1[0], kb1_1[0]);
        FMA(r12_5, f12_5, ks1_1[1], kb1_1[1]);
        FMA(r12_6, f12_6, ks1_1[2], kb1_1[2]);
        asm volatile("s_waitcnt lgkmcnt(8)" ::: "memory");
        MFX(acc[0][4], tb20_13, ta0_13);
        asm volatile("s_waitcnt lgkmcnt(2)" : "+v"(x11_0), "+v"(x11_1), "+v"(x11_2), "+v"(x11_3)::"memory");
        STO(((u32x4){x11_0, x11_1, x11_2, x11_3}), ro1 + 64);
        FMA(r12_7, f12_7, ks1_1[3], kb1_1[3]);
        CPK(o12_0, r12_0, r12_1);
        CPK(o12_1, r12_2, r12_3);
        MFX(acc[0][5], tb21_13, ta0_13);
        BPM(x12_0, o12_0);
        CPK(o12_2, r12_4, r12_5);
        BPM(x12_1, o12_1);
        CPK(o12_3, r12_6, r12_7);
        MFX(acc[0][4], tb20_14, ta0_14);
        DSR(ks2_0, cado, 256);
        BPM(x12_2, o12_2);
        BPM(x12_3, o12_3);
        CVF(f13_0, acc[3][2][0]);
        CVF(f13_1, acc[3][2][1]);
        MFX(acc[0][5], tb21_14, ta0_14);
        DSR(kb2_0, cado, 1152);
        CVF(f13_2, acc[3][2][2]);
        CVF(f13_3, acc[3][2][3]);
        FMA(r13_0, f13_0, ks1_0[0], kb1_0[0]);
        CVF(f13_4, acc[3][3][0]);
        asm volatile("s_waitcnt lgkmcnt(6)" ::: "memory");
        MFX(acc[0][4], tb20_15, ta0_15);
        DSR(ks2_1, cado, 272);
        FMA(r13_1, f13_1, ks1_0[1], kb1_0[1]);
        CVF(f13_5, acc[3][3][1]);
        FMA(r13_2, f13_2, ks1_0[2], kb1_0[2]);
        MFX(acc[0][5], tb21_15, ta0_15);
        DSR(kb2_1, cado, 1168);
        CVF(f13_6, acc[3][3][2]);
        FMA(r13_3, f13_3, ks1_0[3], kb1_0[3]);
        CVF(f13_7, acc[3][3][3]);
        MFX(acc[1][4], tb20_12, ta1_12);
        FMA(r13_4, f13_4, ks1_1[0], kb1_1[0]);
        FMA(r13_5, f13_5, ks1_1[1], kb1_1[1]);
        FMA(r13_6, f13_6, ks1_1[2], kb1_1[2]);
        MFX(acc[1][5], tb21_12, ta1_12);
        FMA(r13_7, f13_7, ks1_1[3], kb1_1[3]);
        CPK(o13_0, r13_0, r13_1);
        CPK(o13_1, r13_2, r13_3);
        MFX(acc[1][4], tb20_13, ta1_13);
        asm volatile("s_waitcnt lgkmcnt(3)" : "+v"(x12_0), "+v"(x12_1), "+v"(x12_2), "+v"(x12_3)::"memory");
        STO(((u32x4){x12_0, x12_1, x12_2, x12_3}), ro2 + 64);
        BPM(x13_0, o13_0);
        CPK(o13_2, r13_4, r13_5);
        BPM(x13_1, o13_1);
        MFX(acc[1][5], tb21_13, ta1_13);
        CPK(o13_3, r13_6, r13_7);
        BPM(x13_2, o13_2);
        BPM(x13_3, o13_3);
        CVF(f20_0, acc[0][4][0]);
        MFX(acc[1][4], tb20_14, ta1_14);
        CVF(f20_1, acc[0][4][1]);
        CVF(f20_2, acc[0][4][2]);
        CVF(f20_3, acc[0][4][3]);
        asm volatile("s_waitcnt lgkmcnt(4)" ::: "memory");
        FMA(r20_0, f20_0, ks2_0[0], kb2_0[0]);
        MFX(acc[1][5], tb21_14, ta1_14);
        CVF(f20_4, acc[0][5][0]);
        FMA(r20_1, f20_1, ks2_0[1], kb2_0[1]);
        CVF(f20_5, acc[0][5][1]);
        MFX(acc[1][4], tb20_15, ta1_15);
        FMA(r20_2, f20_2, ks2_0[2], kb2_0[2]);
        CVF(f20_6, acc[0][5][2]);
        FMA(r20_3, f20_3, ks2_0[3], kb2_0[3]);
        MFX(acc[1][5], tb21_15, ta1_15);
        CVF(f20_7, acc[0][5][3]);
        FMA(r20_4, f20_4, ks2_1[0], kb2_1[0]);
        FMA(r20_5, f20_5, ks2_1[1], kb2_1[1]);
        MFX(acc[2][4], tb20_12, ta2_12);
        FMA(r20_6, f20_6, ks2_1[2], kb2_1[2]);
        FMA(r20_7, f20_7, ks2_1[3], kb2_1[3]);
        CPK(o20_0, r20_0, r20_1);
        MFX(acc[2][5], tb21_12, ta2_12);
        asm volatile("s_waitcnt lgkmcnt(0)" : "+v"(x13_0), "+v"(x13_1), "+v"(x13_2), "+v"(x13_3)::"memory");
        STO(((u32x4){x13_0, x13_1, x13_2, x13_3}), ro3 + 64);
        CPK(o20_1, r20_2, r20_3);
        BPM(x20_0, o20_0);
        MFX(acc[2][4], tb20_13, ta2_13);
        CPK(o20_2, r20_4, r20_5);
        BPM(x20_1, o20_1);
        CPK(o20_3, r20_6, r20_7);
        BPM(x20_2, o20_2);
        MFX(acc[2][5], tb21_13, ta2_13);
        BPM(x20_3, o20_3);
        CVF(f21_0, acc[1][4][0]);
        CVF(f21_1, acc[1][4][1]);
        CVF(f21_2, acc[1][4][2]);
        MFX(acc[2][4], tb20_14, ta2_14);
        CVF(f21_3, acc[1][4][3]);
        FMA(r21_0, f21_0, ks2_0[0], kb2_0[0]);
        CVF(f21_4, acc[1][5][0]);
        MFX(acc[2][5], tb21_14, ta2_14);
        FMA(r21_1, f21_1, ks2_0[1], kb2_0[1]);
        CVF(f21_5, acc[1][5][1]);
        FMA(r21_2, f21_2, ks2_0[2], kb2_0[2]);
        MFX(acc[2][4], tb20_15, ta2_15);
        CVF(f21_6, acc[1][5][2]);
        FMA(r21_3, f21_3, ks2_0[3], kb2_0[3]);
        CVF(f21_7, acc[1][5][3]);
        MFX(acc[2][5], tb21_15, ta2_15);
        FMA(r21_4, f21_4, ks2_1[0], kb2_1[0]);
        FMA(r21_5, f21_5, ks2_1[1], kb2_1[1]);
        FMA(r21_6, f21_6, ks2_1[2], kb2_1[2]);
        MFX(acc[3][4], tb20_12, ta3_12);
        FMA(r21_7, f21_7, ks2_1[3], kb2_1[3]);
        CPK(o21_0, r21_0, r21_1);
        CPK(o21_1, r21_2, r21_3);
        MFX(acc[3][5], tb21_12, ta3_12);
        asm volatile("s_waitcnt lgkmcnt(0)" : "+v"(x20_0), "+v"(x20_1), "+v"(x20_2), "+v"(x20_3)::"memory");
        STO(((u32x4){x20_0, x20_1, x20_2, x20_3}), ro0 + 128);
        BPM(x21_0, o21_0);
        CPK(o21_2, r21_4, r21_5);
        MFX(acc[3][4], tb20_13, ta3_13);
        BPM(x21_1, o21_1);
        CPK(o21_3, r21_6, r21_7);
        BPM(x21_2, o21_2);
        BPM(x21_3, o21_3);
        MFX(acc[3][5], tb21_13, ta3_13);
        CVF(f22_0, acc[2][4][0]);
        CVF(f22_1, acc[2][4][1]);
        CVF(f22_2, acc[2][4][2]);
        CVF(f22_3, acc[2][4][3]);
        MFX(acc[3][4], tb20_14, ta3_14);
        FMA(r22_0, f22_0, ks2_0[0], kb2_0[0]);
        CVF(f22_4, acc[2][5][0]);
        FMA(r22_1, f22_1, ks2_0[1], kb2_0[1]);
        MFX(acc[3][5], tb21_14, ta3_14);
        CVF(f22_5, acc[2][5][1]);
        FMA(r22_2, f22_2, ks2_0[2], kb2_0[2]);
        CVF(f22_6, acc[2][5][2]);
        MFX(acc[3][4], tb20_15, ta3_15);
        FMA(r22_3, f22_3, ks2_0[3], kb2_0[3]);
        CVF(f22_7, acc[2][5][3]);
        FMA(r22_4, f22_4, ks2_1[0], kb2_1[0]);
        MFX(acc[3][5], tb21_15, ta3_15);
        TS(8);
        asm volatile("s_nop 7" ::: "memory");
        FMA(r22_5, f22_5, ks2_1[1], kb2_1[1]);
        FMA(r22_6, f22_6, ks2_1[2], kb2_1[2]);
        FMA(r22_7, f22_7, ks2_1[3], kb2_1[3]);
        CPK(o22_0, r22_0, r22_1);
        CPK(o22_1, r22_2, r22_3);
        BPM(x22_0, o22_0);
        asm volatile("s_waitcnt lgkmcnt(1)" : "+v"(x21_0), "+v"(x21_1), "+v"(x21_2), "+v"(x21_3)::"memory");
        STO(((u32x4){x21_0, x21_1, x21_2, x21_3}), ro1 + 128);
        CPK(o22_2, r22_4, r22_5);
        BPM(x22_1, o22_1);
        CPK(o22_3, r22_6, r22_7);
        BPM(x22_2, o22_2);
        BPM(x22_3, o22_3);
        CVF(f23_0, acc[3][4][0]);
        CVF(f23_1, acc[3][4][1]);
        CVF(f23_2, acc[3][4][2]);
        CVF(f23_3, acc[3][4][3]);
        FMA(r23_0, f23_0, ks2_0[0], kb2_0[0]);
        CVF(f23_4, acc[3][5][0]);
        FMA(r23_1, f23_1, ks2_0[1], kb2_0[1]);
        CVF(f23_5, acc[3][5][1]);
        asm volatile("s_waitcnt lgkmcnt(0)" : "+v"(x22_0), "+v"(x22_1), "+v"(x22_2), "+v"(x22_3)::"memory");
        STO(((u32x4){x22_0, x22_1, x22_2, x22_3}), ro2 + 128);
        FMA(r23_2, f23_2, ks2_0[2], kb2_0[2]);
        CVF(f23_6, acc[3][5][2]);
        FMA(r23_3, f23_3, ks2_0[3], kb2_0[3]);
        CVF(f23_7, acc[3][5][3]);
        FMA(r23_4, f23_4, ks2_1[0], kb2_1[0]);
        FMA(r23_5, f23_5, ks2_1[1], kb2_1[1]);
        FMA(r23_6, f23_6, ks2_1[2], kb2_1[2]);
        FMA(r23_7, f23_7, ks2_1[3], kb2_1[3]);
        CPK(o23_0, r23_0, r23_1);
        CPK(o23_1, r23_2, r23_3);
        BPM(x23_0, o23_0);
        CPK(o23_2, r23_4, r23_5);
        BPM(x23_1, o23_1);
        CPK(o23_3, r23_6, r23_7);
        BPM(x23_2, o23_2);
        BPM(x23_3, o23_3);
        asm volatile("s_waitcnt lgkmcnt(0)" : "+v"(x23_0), "+v"(x23_1), "+v"(x23_2), "+v"(x23_3)::"memory");
        STO(((u32x4){x23_0, x23_1, x23_2, x23_3}), ro3 + 128);
    }
    asm volatile(".Lpf_end:");
    TS(10);
}

extern "C" void kernel_launch(void* const* d_in, const int* in_sizes, int n_in,
                              void* d_out, int out_size, void* d_ws, size_t ws_size,
                              hipStream_t stream) {
    const int* qx = (const int*)d_in[0];
    const int* qw = (const int*)d_in[1];
    const __hip_bfloat16* scale_x = (const __hip_bfloat16*)d_in[2];
    const int* zero_x = (const int*)d_in[3];
    const __hip_bfloat16* scale_w = (const __hip_bfloat16*)d_in[4];
    const __hip_bfloat16* bias = (const __hip_bfloat16*)d_in[5];
    (void)hipFuncSetAttribute((const void*)fused_kernel, hipFuncAttributeMaxDynamicSharedMemorySize, SMEM_BYTES);
    fused_kernel<<<256, 512, SMEM_BYTES, stream>>>(qx, qw, scale_x, zero_x, scale_w, bias, (__hip_bfloat16*)d_out,
                                                   (uint8_t*)d_ws);
}
```

```cpp
#include <hip/hip_runtime.h>
#include <hip/hip_bf16.h>
#include <stdint.h>

typedef int i32x4 __attribute__((ext_vector_type(4)));
typedef int i32x2 __attribute__((ext_vector_type(2)));
typedef unsigned int u32x4 __attribute__((ext_vector_type(4)));
typedef unsigned int u32x2 __attribute__((ext_vector_type(2)));
typedef float f32x2 __attribute__((ext_vector_type(2)));
typedef float f32x4 __attribute__((ext_vector_type(4)));
typedef short s16x2 __attribute__((ext_vector_type(2)));
typedef __bf16 bf16x2_t __attribute__((ext_vector_type(2)));
typedef unsigned long long u64;
#define GAS __attribute__((address_space(1)))
#define LAS __attribute__((address_space(3)))

constexpr int M_ = 2048, K_ = 1024, N_ = 3584;
constexpr int BM = 128, BN = 224;
#define NST 7
constexpr int NSTAGE = NST;
constexpr int UNIT = 3584;
constexpr int A_STAGE = 8192, B_STAGE = 4 * UNIT;
constexpr int STAGE_BYTES = A_STAGE + B_STAGE;
constexpr int RING_BYTES = NSTAGE * STAGE_BYTES;
constexpr int ACC_PITCH = BN * 4 + 16;
constexpr int OFF_CTL = RING_BYTES;
constexpr int CT_FULL = 0, CT_FREE = 16, CT_TAG = 32, CT_ARR = 48, CT_GO = 56, CT_PH2 = 60, CT_SIG = 64;
constexpr int OFF_SCALE = OFF_CTL + 128;
constexpr int OFF_BIAS = OFF_SCALE + 896;
constexpr int OFF_TR = OFF_BIAS + 896;
constexpr int OFF_FREE = OFF_TR + 4;
constexpr int SMEM_BYTES = OFF_FREE + 1024 + 4;
static_assert(SMEM_BYTES <= 163840, "LDS");
static_assert(BM * ACC_PITCH <= RING_BYTES, "acc staging must fit in the ring");
constexpr int NPH = 4;
constexpr size_t WS_EPOCH = 0;
#define FSUB 16
#define FSTR 544
constexpr size_t WS_FLAGS = 8u << 20;
constexpr size_t WS_A8 = 65536;
constexpr size_t WS_B8 = WS_A8 + (size_t)M_ * K_;
constexpr size_t WS_CP = WS_B8 + (size_t)N_ * K_;
constexpr size_t WS_DBG = 200u << 20;
constexpr size_t WS_REG = 16u << 20;
constexpr int AUX_SC1 = 16;
constexpr int RSRC_W3 = 0x00020000;
#define WB 4
#define WA 2
#define FLAGI 1
#define CSKB 13
#define CFLAG 1
#define CGS 64
#define CSTR (4 * CGS)
#define NCA 1
#define NCB 1
#define FL 128
#define NREG 1
#define CPH (16 * (NCA + NCB) * FL)
#define SPOLL 1
#define SGATE 1
#define CSLEEP 4
#define HLIGHT 1
#define REALV 1
#define HSLEEP 1
#define CPF 10
#define SLAG 2
#define PFK 2
#define PGATE 1
#define S1 0
#define S2 6
#define S3 9
#define TB 11
#define TA 5
#define ALAY 1
#define ATR 0
#define FMASK 0xffff
#define CPRIO 0
#define PW 6
#define FI 1
#define ORD 0
#define TG (23 - PW)
#define RSIG 0
#define RDP 8
#define SSLEEP 2
#define DMA_AUX 16
#define LAGK 16
#define LAG (kb >= LAGK ? 2 : 1)
#define PIPE 0
#define PGAP 6
#define PSLEEP 4
#define OUT_AUX 18
#define RAW_AUX 2
#define GO_KB 1

constexpr int styp(int t) {
    constexpr int o[3][6] = {{4, 5, 0, 1, 2, 3}, {0, 1, 2, 3, 4, 5}, {0, 4, 1, 2, 5, 3}};
    return o[ORD][t % 6];
}
constexpr int ops_after(int p, int t) {
    int n = 0;
    for (int s = 6 * p + 5; s <= t; ++s) {
        if (s > 6 * p + 5) n += styp(s) >= 4 ? 1 : styp(s) == 3 ? 4 : 0;
        if (s + PW < 24) n += 4;
    }
    return n;
}
__device__ __forceinline__ unsigned pack4(int a, int b, int c, int d) {
    const unsigned lo = __builtin_amdgcn_perm((unsigned)b, (unsigned)a, 0x0c0c0400u);
    const unsigned hi = __builtin_amdgcn_perm((unsigned)d, (unsigned)c, 0x0c0c0400u);
    return __builtin_amdgcn_perm(hi, lo, 0x05040100u);
}
__device__ __forceinline__ u32x4 lds_rd128(unsigned addr) {
    u32x4 v;
    asm volatile("ds_read_b128 %0, %1\n\ts_waitcnt lgkmcnt(0)" : "=v"(v) : "v"(addr) : "memory");
    return v;
}
__device__ __forceinline__ void lds_wr32_max(unsigned addr, unsigned val) {
    asm volatile("ds_max_u32 %0, %1" ::"v"(addr), "v"(val) : "memory");
}
__device__ __forceinline__ void lds_wr32(unsigned addr, unsigned val) {
    asm volatile("ds_write_b32 %0, %1" ::"v"(addr), "v"(val) : "memory");
}

__device__ __forceinline__ float sfma(int a, float s, float b) {
    float r;
    asm("v_fma_f32 %0, %1, %2, %3" : "=v"(r) : "v"((float)a), "v"(s), "v"(b));
    return r;
}
#define SPIN for (;;)
#define TS(i) do { } while (0)

__global__ __launch_bounds__(512, 2) void fused_kernel(const int* __restrict__ qx, const int* __restrict__ qw,
                                                       const __hip_bfloat16* __restrict__ scale_x,
                                                       const int* __restrict__ zero_x,
                                                       const __hip_bfloat16* __restrict__ scale_w,
                                                       const __hip_bfloat16* __restrict__ bias,
                                                       __hip_bfloat16* __restrict__ out, uint8_t* __restrict__ ws) {
    extern __shared__ __attribute__((aligned(16))) uint8_t smem[];
    const int tid = threadIdx.x, lane = tid & 63;
    const int wid = __builtin_amdgcn_readfirstlane(tid >> 6);
    const int bid = blockIdx.x;
    const int tm = (bid & 1) * 8 + ((bid >> 3) & 7);
    const int tn = ((bid >> 1) & 3) * 4 + (bid >> 6);
    const int tmA = bid >> 4, subA = bid & 15;
    const int tnB = bid & 15, subB = bid >> 4;
    const unsigned smem_base = (unsigned)(uintptr_t)smem;
    const unsigned ctl = smem_base + OFF_CTL;
    uint8_t* wr;
    u64 tag;
    unsigned pfd = 0;
    i32x4 rsT = {0, 0, 0, 0};
#define SIG(ADDR, V) asm volatile("s_mov_b64 exec, 1\n\ts_mov_b32 m0, %0\n\ts_nop 0\n\tbuffer_load_dword off, %1, %2 lds\n\ts_mov_b64 exec, -1" \
                                  ::"s"(ADDR), "s"(rsT), "n"((V) * 4) : "memory", "m0")
#define TAGWAIT()                                                                                                        \
    do {                                                                                                                 \
        asm volatile("s_load_dwordx2 %0, %1, 0x0\n\ts_waitcnt lgkmcnt(0)" : "=s"(tag) : "s"(ws + WS_EPOCH) : "memory"); \
        tag += 0x9E3779B97F4A7C15ull;                                                                                    \
        wr = ws + ((unsigned)tag & (NREG - 1)) * WS_REG;                                                                 \
    } while (0)

    if (wid == 4) {
        const __amdgpu_buffer_rsrc_t rsW = __builtin_amdgcn_make_buffer_rsrc((void*)qw, 0, K_ * N_ * 4, RSRC_W3);
        const __amdgpu_buffer_rsrc_t rsX = __builtin_amdgcn_make_buffer_rsrc((void*)qx, 0, M_ * K_ * 4, RSRC_W3);
        const int q = lane < 56 ? lane : lane - 8;
        const unsigned voB = (unsigned)((tnB * 224 + q * 4) * 4);
        const int soB = ((subB >> 2) * 64 + (subB & 3) * 4) * (N_ * 4);
        const int kb0 = 2 * (subA & 1);
        const unsigned voA = (unsigned)((((tmA * 8 + (subA >> 1)) * 16 + (lane >> 2)) * K_ + kb0 * 64 + ((lane & 3) ^ ((lane >> 5) * 2)) * 4) * 4);
        i32x4 st[PW][4];
        auto ld = [&](int t) {
            const int p = t / 6, j = styp(t);
#pragma unroll
            for (int x = 0; x < 4; ++x)
                st[t % PW][x] = __builtin_bit_cast(i32x4, j < 4 ? __builtin_amdgcn_raw_buffer_load_b128(rsW, voB, soB + (p * 256 + j * 16 + x) * (N_ * 4), RAW_AUX)
                                                                : __builtin_amdgcn_raw_buffer_load_b128(rsX, voA + p * 1024 + (j - 4) * 256 + x * 64, 0, RAW_AUX));
        };
#pragma unroll
        for (int t = 0; t < PW; ++t) ld(t);
        __builtin_amdgcn_sched_barrier(0);
        const unsigned wB = lane < 56 ? (unsigned)(M_ * K_ + (tnB * 64 + subB) * UNIT + lane * 16) : 0x80000000u;
        const unsigned wA = (unsigned)(((tmA * 16 + kb0) * 8 + (subA >> 1)) * 1024 + lane * 16);
        const unsigned vf0 = lane < NCB ? (unsigned)((16 * NCA + tnB * NCB + lane) * FL + subB * 8)
                           : (lane & 48) == 16 && (lane & 15) < NCA ? (unsigned)((tmA * NCA + (lane & 15)) * FL + (subA & 1) * 64 + (subA >> 1) * 8) : 0x80000000u;
        TAGWAIT();
        __builtin_amdgcn_s_barrier();
        __builtin_amdgcn_sched_barrier(0);
        const u32x2 tg = {(unsigned)tag, (unsigned)(tag >> 32)};
        const __amdgpu_buffer_rsrc_t rs8 = __builtin_amdgcn_make_buffer_rsrc((void*)(wr + WS_A8), 0, (M_ + N_) * K_ + 16 * 16 * 512, RSRC_W3);
        const __amdgpu_buffer_rsrc_t rsF = __builtin_amdgcn_make_buffer_rsrc((void*)(wr + WS_FLAGS), 0, 4 * CPH, RSRC_W3);
        int cs[4] = {0, 0, 0, 0};
        u32x4 pk[4];
#pragma unroll
        for (int t = 0; t < 24; ++t) {
            const int p = t / 6, j = styp(t);
            if (j < 4) {
#pragma unroll
                for (int c = 0; c < 4; ++c) {
                    pk[c][j] = pack4(st[t % PW][0][c], st[t % PW][1][c], st[t % PW][2][c], st[t % PW][3][c]);
                    cs[c] = __builtin_amdgcn_sdot4((int)pk[c][j], 0x01010101, cs[c], false);
                }
                if (j == 3) {
#pragma unroll
                    for (int c = 0; c < 4; ++c) __builtin_amdgcn_raw_buffer_store_b128(pk[c], rs8, wB + p * 16 * UNIT + ((c & 1) * 2 + (c >> 1)) * 896, 0, AUX_SC1);
                    if (p == 3) {
                        const u32x2 cp = {__builtin_amdgcn_perm((unsigned)cs[1], (unsigned)cs[0], 0x05040100u),
                                          __builtin_amdgcn_perm((unsigned)cs[3], (unsigned)cs[2], 0x05040100u)};
                        __builtin_amdgcn_raw_buffer_store_b64(cp, rs8, (unsigned)((M_ + N_) * K_ + (tnB * 16 + subB) * 512 + lane * 8), 0, AUX_SC1);
                    }
                }
            } else {
                u32x4 o;
#pragma unroll
                for (int x = 0; x < 4; ++x) o[x] = pack4(st[t % PW][x][0], st[t % PW][x][1], st[t % PW][x][2], st[t % PW][x][3]);
                __builtin_amdgcn_raw_buffer_store_b128(o, rs8, wA + (p * 4 + j - 4) * 8192, 0, AUX_SC1);
            }
            if (t == 5) TS(1);
            __builtin_amdgcn_sched_barrier(0);
            if (t + PW < 24) ld(t + PW);
            if (t == TG) lds_wr32(ctl + CT_SIG + 4, 1u);
            __builtin_amdgcn_sched_barrier(0);
            if (t % 6 == FI && t >= 6) {
                asm volatile("s_waitcnt vmcnt(%0)" ::"n"(ops_after(p - 1, t)) : "memory");
                __builtin_amdgcn_raw_buffer_store_b64(tg, rsF, vf0 + (p - 1) * CPH, 0, AUX_SC1);
                if (p == 1) lds_wr32(ctl + CT_GO, 1u);
                TS(1 + p);
                __builtin_amdgcn_sched_barrier(0);
            }
            if (t == 23) {
                asm volatile("s_waitcnt vmcnt(0)" ::: "memory");
                __builtin_amdgcn_raw_buffer_store_b64(tg, rsF, vf0 + 3 * CPH, 0, AUX_SC1);
                TS(5);
            }
        }
    } else {
        u64 pc;
        int clen, toff;
        asm volatile(".Lpf_start:\n\ts_getpc_b64 %0\n\ts_mov_b32 %1, .Lpf_end-.Lpf_start\n\ts_mov_b32 %2, .Ltab-.Lpf_start-4\n\ts_branch .Ltab_end\n\t"
                     ".p2align 7\n.Ltab:\n\t.long 0, 1, 2, 3, 4, 5, 6, 7, 8, 9, 10, 11, 12, 13, 14, 15, 16, 0\n.Ltab_end:"
                     : "=s"(pc), "=s"(clen), "=s"(toff));
        {
            const u64 tp = pc + (unsigned)toff;
            rsT = (i32x4){(int)(unsigned)tp, (int)(unsigned)(tp >> 32), 68, RSRC_W3};
            asm volatile("" : "+s"(rsT));
        }
        if (CPF && wid < 4) {
            const __amdgpu_buffer_rsrc_t rsP = __builtin_amdgcn_make_buffer_rsrc((void*)pc, 0, max(clen, 4) - 4, RSRC_W3);
            const int idx = (bid >> 3) * 4 + wid;
            pfd = __builtin_amdgcn_raw_buffer_load_b32(rsP, idx < 3 * CPF ? (unsigned)((idx % CPF) * 4096 + lane * 64) : 0x80000000u, 0, 0);
        }
        if (wid == 6) lds_wr32(lane < 32 ? ctl + lane * 4 : smem_base + OFF_FREE + 252 + (lane & 1) * 4 + (lane & 2) * 256, 0u);
        TAGWAIT();
        __builtin_amdgcn_s_barrier();
    }

    if (wid >= 6) {
        const int lw = wid - 6;
        TS(1);
        const __amdgpu_buffer_rsrc_t rsL = __builtin_amdgcn_make_buffer_rsrc((void*)(wr + WS_A8), 0, (M_ + N_) * K_, RSRC_W3);
        const int soA = tm * 131072 + lw * 1024, soB = M_ * K_ + tn * (64 * UNIT) + lw * 1024;
        const unsigned full = ctl + CT_FULL + lw * 4;
        unsigned rdy = 0, kfree = 0, aR = ctl + CT_SIG, aF = smem_base + OFF_FREE + 252;
        asm volatile("" : "+v"(aR), "+v"(aF));
        u32x2 vr;
        u32x4 vf;
#define LOOK() asm volatile("ds_read_b64 %0, %2\n\tds_read2st64_b64 %1, %3 offset1:1" : "=&v"(vr), "=&v"(vf) : "v"(aR), "v"(aF) : "memory")
#define SEEN()                                                                                     \
    do {                                                                                           \
        asm volatile("s_waitcnt lgkmcnt(0)" : "+v"(vr), "+v"(vf)::"memory");                       \
        rdy = __builtin_amdgcn_readfirstlane(vr[1] ? vr[0] : 0u);                                  \
        kfree = __builtin_amdgcn_readfirstlane(min(min(vf[0], vf[1]), min(vf[2], vf[3])));         \
    } while (0)
#pragma unroll
        for (int kb = 0; kb < 16; ++kb) {
            if (__builtin_expect(rdy <= (unsigned)(kb >> 1) || (kb >= NSTAGE && kfree <= (unsigned)(kb - NSTAGE)), 0)) {
                SPIN {
                    LOOK();
                    SEEN();
                    if (rdy > (unsigned)(kb >> 1) && (kb < NSTAGE || kfree > (unsigned)(kb - NSTAGE))) break;
                    __builtin_amdgcn_s_sleep(1);
                }
            }
            if ((kb & 3) == 0) TS(2 + (kb >> 2));
            if (kb == 2) TS(6);
            LAS uint8_t* base = (LAS uint8_t*)(uintptr_t)(smem_base + lw * 1024 + (kb % NSTAGE) * STAGE_BYTES);
#pragma unroll
            for (int i = 0; i < 11; ++i) {
                if (i == RDP && kb < 15) LOOK();
                const int so = i < 4 ? soA + kb * 8192 + i * 2048 : soB + kb * B_STAGE + (i - 4) * 2048;
                __builtin_amdgcn_raw_ptr_buffer_load_lds(rsL, base + i * 2048, 16, lane * 16, so, 0, DMA_AUX);
            }
            SIG(full, kb + 1);
            if (kb < 15) SEEN();
            asm volatile("s_waitcnt vmcnt(%0)" ::"n"(12 * SLAG) : "memory");
        }
        asm volatile("s_waitcnt vmcnt(0)" ::: "memory");
        TS(7);
    } else if (wid == 5) {
        const int l8 = lane < 28 ? lane : 0;
        const u32x4 sw8 = reinterpret_cast<const u32x4*>(scale_w)[tn * (BN / 8) + l8];
        const u32x4 bi8 = reinterpret_cast<const u32x4*>(bias)[tn * (BN / 8) + l8];
        const float sx = __uint_as_float((unsigned)reinterpret_cast<const unsigned short*>(scale_x)[0] << 16);
        const float nzx = -(float)zero_x[0];
        f32x4 sc[2], bi[2];
#pragma unroll
        for (int e = 0; e < 4; ++e) {
            const unsigned w = sw8[e], c = bi8[e];
            sc[e >> 1][(e & 1) * 2] = sx * __uint_as_float(w << 16);
            sc[e >> 1][(e & 1) * 2 + 1] = sx * __uint_as_float(w & 0xffff0000u);
            bi[e >> 1][(e & 1) * 2] = __uint_as_float(c << 16);
            bi[e >> 1][(e & 1) * 2 + 1] = __uint_as_float(c & 0xffff0000u);
        }
        if (lane < 28) asm volatile("ds_write_b128 %0, %1\n\tds_write_b128 %0, %2 offset:16" ::"v"(smem_base + OFF_SCALE + lane * 32), "v"(sc[0]), "v"(sc[1]) : "memory");
        if (SGATE) SPIN {
            const u32x4 v = lds_rd128(ctl + CT_ARR);
            if (__builtin_amdgcn_readfirstlane(v[2])) break;
            __builtin_amdgcn_s_sleep(1);
        }
        TS(1);
        GAS u64* fa = (GAS u64*)(wr + WS_FLAGS + (tm * NCA + tn / (16 / NCA)) * FL);
        GAS u64* fb = (GAS u64*)(wr + WS_FLAGS + (16 * NCA + tn * NCB + tm / (16 / NCB)) * FL);
        typedef u64 u64x8 __attribute__((ext_vector_type(8)));
#define FD8(D, F)                                                                                                                       \
    do {                                                                                                                                \
        u64 t_;                                                                                                                         \
        const u64 a0_ = F[0], a1_ = F[1], a2_ = F[2], a3_ = F[3], a4_ = F[4], a5_ = F[5], a6_ = F[6], a7_ = F[7];                       \
        asm("s_xor_b64 %1, %3, %2\n\ts_or_b64 %0, %0, %1\n\ts_xor_b64 %1, %4, %2\n\ts_or_b64 %0, %0, %1\n\t"                            \
            "s_xor_b64 %1, %5, %2\n\ts_or_b64 %0, %0, %1\n\ts_xor_b64 %1, %6, %2\n\ts_or_b64 %0, %0, %1\n\t"                            \
            "s_xor_b64 %1, %7, %2\n\ts_or_b64 %0, %0, %1\n\ts_xor_b64 %1, %8, %2\n\ts_or_b64 %0, %0, %1\n\t"                            \
            "s_xor_b64 %1, %9, %2\n\ts_or_b64 %0, %0, %1\n\ts_xor_b64 %1, %10, %2\n\ts_or_b64 %0, %0, %1"                               \
            : "+s"(D), "=&s"(t_) : "s"(tag), "s"(a0_), "s"(a1_), "s"(a2_), "s"(a3_), "s"(a4_), "s"(a5_), "s"(a6_), "s"(a7_) : "scc");     \
    } while (0)
#pragma unroll
        for (int q = 0; q < 4; ++q) {
            int it = 0;
            bool half = false;
            SPIN {
                if ((++it & 63) == 0) {
                    u64 x;
                    asm volatile("global_load_dwordx2 %0, %1, off sc1\n\ts_waitcnt vmcnt(0)" : "=v"(x) : "v"(((lane & 16) ? fb : fa) + q * (CPH / 8) + (lane & 15)) : "memory");
                    if (__all(x == tag)) break;
                } else {
                    u64x8 f0, f1, f2, f3;
                    asm volatile("s_load_dwordx16 %0, %4, %6 glc\n\ts_load_dwordx16 %1, %5, %6 glc\n\ts_load_dwordx16 %2, %4, %7 glc\n\ts_load_dwordx16 %3, %5, %7 glc\n\ts_waitcnt lgkmcnt(0)"
                                 : "=&s"(f0), "=&s"(f1), "=&s"(f2), "=&s"(f3)
                                 : "s"(fa), "s"(fb), "n"(q * CPH), "n"(q * CPH + 64) : "memory");
                    u64 dl = 0, dh = 0;
                    FD8(dl, f0);
                    FD8(dl, f1);
                    if (dl == 0) {
                        FD8(dh, f2);
                        FD8(dh, f3);
                        if (dh == 0) break;
                        if (!half) {
                            if (RSIG && q) SIG(ctl + CT_SIG, 2 * q + 1);
                            else lds_wr32(ctl + CT_SIG, 2 * q + 1);
                            half = true;
                        }
                    }
                }
            }
            if (RSIG && q) SIG(ctl + CT_SIG, 2 * q + 2);
            else lds_wr32(ctl + CT_SIG, 2 * q + 2);
            TS(2 + q);
        }
        const __amdgpu_buffer_rsrc_t rsC = __builtin_amdgcn_make_buffer_rsrc((void*)(wr + WS_CP), 0, 16 * 16 * 512, RSRC_W3);
        typedef short i16x8 __attribute__((ext_vector_type(8)));
        i16x8 cp[8];
#pragma unroll
        for (int x = 0; x < 8; ++x)
            cp[x] = __builtin_bit_cast(i16x8, __builtin_amdgcn_raw_buffer_load_b128(rsC, lane * 16, tn * 8192 + x * 1024, AUX_SC1));
        const i16x8 u0 = (cp[0] + cp[1]) + (cp[2] + cp[3]), u1 = (cp[4] + cp[5]) + (cp[6] + cp[7]);
#pragma unroll
        for (int e = 0; e < 8; ++e) {
            const short x0 = u0[e], x1 = u1[e];
            const unsigned t = (unsigned)((int)x0 + (int)x1);
            const u32x2 sw = __builtin_amdgcn_permlane32_swap(t, t, false, false);
            const unsigned lo = sw[0], hi = sw[1];
            const float cf = nzx * (float)(int)(lo + hi);
            bi[e >> 2][e & 3] = __builtin_fmaf(cf, sc[e >> 2][e & 3], bi[e >> 2][e & 3]);
        }
        if (lane < 28)
            asm volatile("ds_write_b128 %0, %1\n\tds_write_b128 %0, %2 offset:16\n\tds_write_b32 %3, %4" ::"v"(smem_base + OFF_BIAS + lane * 32),
                         "v"(bi[0]), "v"(bi[1]), "v"(ctl + CT_FULL + 8), "v"(1u) : "memory");
        TS(8);
    } else if (wid == 4) {
        if (bid == 0) {
            SPIN {
                const u32x4 v = lds_rd128(ctl + CT_FULL);
                if (__builtin_amdgcn_readfirstlane(min(v[0], v[1])) >= 16) break;
                __builtin_amdgcn_s_sleep(8);
            }
            SPIN {
                bool ok = true;
#pragma unroll
                for (int i = 0; i < 4; ++i)
                    ok &= __hip_atomic_load((GAS u64*)(wr + WS_FLAGS + 3 * CPH + (i * 4 + (lane >> 4)) * NCA * FL) + (lane & 15), __ATOMIC_RELAXED, __HIP_MEMORY_SCOPE_AGENT) == tag;
                if (__all(ok)) break;
                __builtin_amdgcn_s_sleep(8);
            }
            if (lane == 0) __hip_atomic_store((GAS u64*)(ws + WS_EPOCH), tag, __ATOMIC_RELAXED, __HIP_MEMORY_SCOPE_AGENT);
        }
    } else {
        const int wm = wid >> 1, wn = wid & 1;
        const int qG4 = wn * 32 + (tn & 1) * 8, qG2 = wn * 32 + 16 - (tn & 1) * 16;
        i32x4 acc[4][6], acm[2][2];
        i32x4 af[2][4], bfr[2][8];
#pragma unroll
        for (int i = 0; i < 4; ++i)
#pragma unroll
            for (int j = 0; j < 6; ++j) {
                acc[i][j] = (i32x4){0, 0, 0, 0};
                asm volatile("" : "+v"(acc[i][j]));
            }
#pragma unroll
        for (int i = 0; i < 2; ++i)
#pragma unroll
            for (int j = 0; j < 2; ++j) {
                acm[i][j] = (i32x4){0, 0, 0, 0};
                asm volatile("" : "+v"(acm[i][j]));
            }
        constexpr int NU = (NSTAGE + 2) / 3;
        unsigned aA0[NU], aA2[NU], aG4[NU], aG2[NU], aM[NU];
        {
            const unsigned fA = smem_base + wm * 4096 + (ATR ? lane * 16 : (lane & 15) * 64 + ((lane >> 4) ^ ((lane >> 2) & 2)) * 16);
            const unsigned fB = smem_base + A_STAGE + (lane >> 4) * UNIT, f2 = fB + (lane & 1) * 896 + ((lane >> 1) & 7) * 16;
#pragma unroll
            for (int u = 0; u < NU; ++u) {
                aA0[u] = fA + wn * 2048 + u * 3 * STAGE_BYTES;
                aA2[u] = fA + (wn ^ 1) * 2048 + u * 3 * STAGE_BYTES;
                aG4[u] = fB + (qG4 + (lane & 15)) * 16 + u * 3 * STAGE_BYTES;
                aG2[u] = f2 + qG2 * 16 + u * 3 * STAGE_BYTES;
                aM[u] = f2 + 384 + u * 3 * STAGE_BYTES;
                asm volatile("" : "+v"(aA0[u]), "+v"(aA2[u]), "+v"(aG4[u]), "+v"(aG2[u]), "+v"(aM[u]));
            }
        }
#define FSIG(V) do { if ((FMASK >> (V)) & 1) asm volatile("buffer_load_dword off, %0, %1 lds" ::"s"(rsT), "n"((V) * 4) : "memory"); } while (0)
        unsigned fmin;
        unsigned known = 0;
#define DSR(DST, ADDR, OFF) asm volatile("ds_read_b128 %0, %1 offset:%2" : "=v"(DST) : "v"(ADDR), "n"(OFF) : "memory")
#define MF(I, J, SET) asm volatile("v_mfma_i32_16x16x64_i8 %0, %1, %2, %0" : "+v"(acc[I][J]) : "v"(af[SET][I]), "v"(bfr[SET][J]))
#define MM(I, J, SET) asm volatile("v_mfma_i32_16x16x64_i8 %0, %1, %2, %0" : "+v"(acm[I][J]) : "v"(af[SET][I]), "v"(bfr[SET][6 + (J)]))
#define NEED(X)                                                                                                \
    do {                                                                                                       \
        if (__builtin_expect(known <= (unsigned)(X), 0)) {                                                     \
            SPIN {                                                                                             \
                const u32x4 v_ = lds_rd128(ctl + CT_FULL);                                                     \
                known = __builtin_amdgcn_readfirstlane(min(v_[0], v_[1]));                                     \
                if (known > (unsigned)(X)) break;                                                              \
                __builtin_amdgcn_s_sleep(1);                                                                   \
            }                                                                                                  \
        }                                                                                                      \
    } while (0)
#define SU(KB) (((KB) % NSTAGE) / 3)
#define SO(KB) ((((KB) % NSTAGE) % 3) * STAGE_BYTES)
#define RA(SET, KB, I) DSR(af[SET][I], (I) < 2 ? aA0[SU(KB)] : aA2[SU(KB)], SO(KB) + ((I) & 1) * 1024)
#define RB(SET, KB, J) DSR(bfr[SET][J], (J) >= 6 ? aM[SU(KB)] : (J) < 2 ? aG2[SU(KB)] : aG4[SU(KB)], SO(KB) + ((J) & 1) * 1792 + ((J) >> 1 == 2 ? 896 : 0))
#define STEP(SET, KB)                                                                        \
    do {                                                                                     \
        MF(0, 0, SET);                                                                       \
        if ((KB) + 1 < 16) {                                                                 \
            NEED((KB) + 1);                                                                  \
            __builtin_amdgcn_sched_barrier(0);                                               \
            MF(1, 0, SET);                                                                   \
            RB((SET) ^ 1, (KB) + 1, 0);   MF(2, 0, SET);                                     \
            RA((SET) ^ 1, (KB) + 1, 0);   MF(3, 0, SET);                                     \
            RA((SET) ^ 1, (KB) + 1, 1);   MF(0, 1, SET);                                     \
            RA((SET) ^ 1, (KB) + 1, 2);   MF(1, 1, SET);                                     \
            RA((SET) ^ 1, (KB) + 1, 3);   MF(2, 1, SET);                                     \
            RB((SET) ^ 1, (KB) + 1, 1);   MF(3, 1, SET);                                     \
            RB((SET) ^ 1, (KB) + 1, 2);   MF(0, 2, SET);                                     \
            RB((SET) ^ 1, (KB) + 1, 3);   MF(1, 2, SET);                                     \
            RB((SET) ^ 1, (KB) + 1, 4);   MF(2, 2, SET);                                     \
            RB((SET) ^ 1, (KB) + 1, 5);   MF(3, 2, SET);                                     \
            RB((SET) ^ 1, (KB) + 1, 6);   MF(0, 3, SET);                                     \
            RB((SET) ^ 1, (KB) + 1, 7);                                                      \
            MF(1, 3, SET); MF(2, 3, SET); MF(3, 3, SET);                                     \
            MF(0, 4, SET); MF(1, 4, SET);                                                    \
            if ((KB) + 2 < 16) asm volatile("ds_read_b64 %0, %1" : "=v"(fv) : "v"(fullv) : "memory"); \
            MF(2, 4, SET); MF(3, 4, SET);                                                    \
            MF(0, 5, SET); MF(1, 5, SET); MF(2, 5, SET); MF(3, 5, SET);                      \
            MM(0, 0, SET);                                                                   \
            asm volatile("s_waitcnt lgkmcnt(0)" : "+v"(fv)::"memory");                       \
            if ((KB) + 1 + NSTAGE < 16) FSIG((KB) + 2);                                      \
            MM(1, 0, SET);                                                                   \
            { const unsigned f0_ = fv[0], f1_ = fv[1]; asm volatile("v_min_u32 %0, %1, %2" : "=v"(fmin) : "v"(f0_), "v"(f1_)); } \
            MM(0, 1, SET);                                                                   \
            asm volatile("v_readfirstlane_b32 %0, %1" : "=s"(known) : "v"(fmin));            \
            MM(1, 1, SET);                                                                   \
        } else {                                                                             \
            MF(1, 0, SET); MF(2, 0, SET); MF(3, 0, SET);                                     \
            _Pragma("unroll") for (int j = 1; j < 6; ++j)                                    \
            _Pragma("unroll") for (int i = 0; i < 4; ++i) MF(i, j, SET);                     \
            MM(0, 0, SET); MM(1, 0, SET); MM(0, 1, SET); MM(1, 1, SET);                      \
        }                                                                                    \
        __builtin_amdgcn_sched_barrier(0);                                                   \
    } while (0)
        u32x2 fv = {0, 0};
        unsigned fullv = ctl + CT_FULL;
        asm volatile("" : "+v"(fullv));
        u64 outp = (u64)out;
        asm volatile("" : "+s"(outp));
        const __amdgpu_buffer_rsrc_t rsO = __builtin_amdgcn_make_buffer_rsrc((void*)outp, 0, M_ * N_ * 2, RSRC_W3);
        unsigned vG4, vG2, vM, cad4, cad0, cad3, crdy = 0;
        int so[4][4];
        typedef unsigned u32x3 __attribute__((ext_vector_type(3)));
        u32x3 fq;
        {
            const unsigned ovo = (unsigned)(((tm * BM + wm * 64 + (lane >> 4) * 4) * N_ + tn * BN) * 2), l = lane & 15;
            vG4 = ovo + (qG4 + l) * 8;
            vG2 = ovo + qG2 * 8 + l * 4;
            vM = ovo + 192 + l * 4;
            cad4 = smem_base + OFF_SCALE + (qG4 + l) * 16;
            cad0 = smem_base + OFF_SCALE + qG2 * 16 + l * 8;
            cad3 = smem_base + OFF_SCALE + 384 + l * 8;
            asm volatile("" : "+v"(vG4), "+v"(vG2), "+v"(vM), "+v"(cad4), "+v"(cad0), "+v"(cad3));
#pragma unroll
            for (int i = 0; i < 4; ++i)
#pragma unroll
                for (int r = 0; r < 4; ++r) {
                    so[i][r] = (((i ^ (wn * 2)) * 16 + r) * N_) * 2;
                    asm volatile("" : "+s"(so[i][r]));
                }
        }
#define SB() __builtin_amdgcn_sched_barrier(0)
#define MFX(ACC, B, A) asm volatile("v_mfma_i32_16x16x64_i8 %0, %1, %2, %0" : "+v"(ACC) : "v"(A), "v"(B))
#define CVF(F, X) do { SB(); F = (float)(X); SB(); } while (0)
#define FMA(R, F, S, B) do { const float s_ = S, b_ = B; asm volatile("v_fma_f32 %0, %1, %2, %3" : "=v"(R) : "v"(F), "v"(s_), "v"(b_)); } while (0)
#define CPK(O, R0, R1) do { SB(); const f32x2 v_ = {R0, R1}; O = __builtin_bit_cast(unsigned, __builtin_convertvector(v_, bf16x2_t)); SB(); } while (0)
#define DSR2(DST, ADDR, OFF) asm volatile("ds_read_b64 %0, %1 offset:%2" : "=v"(DST) : "v"(ADDR), "n"(OFF) : "memory")
#define ST1(X, ADDR, I, R) do { SB(); __builtin_amdgcn_raw_buffer_store_b32(X, rsO, ADDR, so[I][R], OUT_AUX); SB(); } while (0)
#define ST2(X, Y, I, R) do { SB(); __builtin_amdgcn_raw_buffer_store_b64((u32x2){X, Y}, rsO, vG4, so[I][R], OUT_AUX); SB(); } while (0)
#define STO(X, ADDR) __builtin_amdgcn_raw_buffer_store_b128(X, rsO, ADDR, 0, OUT_AUX)
#define CORRW()                                                                                                \
    do {                                                                                                       \
        if (__builtin_expect(!crdy, 0)) {                                                                      \
            SPIN {                                                                                             \
                const u32x4 v_ = lds_rd128(ctl + CT_FULL);                                                     \
                if (__builtin_amdgcn_readfirstlane(v_[2])) break;                                              \
                __builtin_amdgcn_s_sleep(1);                                                                   \
            }                                                                                                  \
        }                                                                                                      \
    } while (0)
        asm volatile("s_mov_b32 m0, %1" ::"v"(pfd), "s"(smem_base + OFF_FREE + wid * 256) : "memory");
        if (CPRIO) __builtin_amdgcn_s_setprio(CPRIO);
        NEED(0);
        TS(4);
        RB(0, 0, 0); RA(0, 0, 0); RA(0, 0, 1); RA(0, 0, 2); RA(0, 0, 3);
        RB(0, 0, 1); RB(0, 0, 2); RB(0, 0, 3); RB(0, 0, 4); RB(0, 0, 5); RB(0, 0, 6); RB(0, 0, 7);
        asm volatile("s_waitcnt lgkmcnt(0)" ::: "memory");
        FSIG(1);
        __builtin_amdgcn_sched_barrier(0);
        STEP(0, 0);
        STEP(1, 1);
        STEP(0, 2);
        STEP(1, 3);
        TS(5);
        STEP(0, 4);
        STEP(1, 5);
        STEP(0, 6);
        STEP(1, 7);
        TS(6);
        STEP(0, 8);
        STEP(1, 9);
        STEP(0, 10);
        i32x4 ta0_12, ta0_13, ta0_14, ta0_15, ta1_12, ta1_13, ta1_14, ta1_15, ta2_12, ta2_13, ta2_14, ta2_15, ta3_12, ta3_13, ta3_14, ta3_15, tb00_12, tb00_13, tb00_14, tb00_15, tb01_12, tb01_13, tb01_14, tb01_15, tb10_12, tb10_13, tb10_14, tb10_15, tb11_12, tb11_13, tb11_14, tb11_15, tb20_12, tb20_13, tb20_14, tb20_15, tb21_12, tb21_13, tb21_14, tb21_15, tb30_12, tb30_13, tb30_14, tb30_15, tb31_12, tb31_13, tb31_14, tb31_15;
        f32x4 ks4, kb4;
        f32x2 ks0, kb0, ks3, kb3;
        float f30_00, f30_10, f30_20, f30_30, f30_01, f30_11, f30_21, f30_31, f31_00, f31_10, f31_20, f31_30, f31_01, f31_11, f31_21, f31_31, f00_00, f00_10, f00_20, f00_30, f00_01, f00_11, f00_21, f00_31, f01_00, f01_10, f01_20, f01_30, f01_01, f01_11, f01_21, f01_31, f02_00, f02_10, f02_20, f02_30, f02_01, f02_11, f02_21, f02_31, f03_00, f03_10, f03_20, f03_30, f03_01, f03_11, f03_21, f03_31, f10_00, f10_10, f10_20, f10_30, f10_01, f10_11, f10_21, f10_31, f20_00, f20_10, f20_20, f20_30, f20_01, f20_11, f20_21, f20_31, f11_00, f11_10, f11_20, f11_30, f11_01, f11_11, f11_21, f11_31, f21_00, f21_10, f21_20, f21_30, f21_01, f21_11, f21_21, f21_31, f12_00, f12_10, f12_20, f12_30, f12_01, f12_11, f12_21, f12_31, f22_00, f22_10, f22_20, f22_30, f22_01, f22_11, f22_21, f22_31, f13_00, f13_10, f13_20, f13_30, f13_01, f13_11, f13_21, f13_31, f23_00, f23_10, f23_20, f23_30, f23_01, f23_11, f23_21, f23_31;
        float r30_00, r30_10, r30_20, r30_30, r30_01, r30_11, r30_21, r30_31, r31_00, r31_10, r31_20, r31_30, r31_01, r31_11, r31_21, r31_31, r00_00, r00_10, r00_20, r00_30, r00_01, r00_11, r00_21, r00_31, r01_00, r01_10, r01_20, r01_30, r01_01, r01_11, r01_21, r01_31, r02_00, r02_10, r02_20, r02_30, r02_01, r02_11, r02_21, r02_31, r03_00, r03_10, r03_20, r03_30, r03_01, r03_11, r03_21, r03_31, r10_00, r10_10, r10_20, r10_30, r10_01, r10_11, r10_21, r10_31, r20_00, r20_10, r20_20, r20_30, r20_01, r20_11, r20_21, r20_31, r11_00, r11_10, r11_20, r11_30, r11_01, r11_11, r11_21, r11_31, r21_00, r21_10, r21_20, r21_30, r21_01, r21_11, r21_21, r21_31, r12_00, r12_10, r12_20, r12_30, r12_01, r12_11, r12_21, r12_31, r22_00, r22_10, r22_20, r22_30, r22_01, r22_11, r22_21, r22_31, r13_00, r13_10, r13_20, r13_30, r13_01, r13_11, r13_21, r13_31, r23_00, r23_10, r23_20, r23_30, r23_01, r23_11, r23_21, r23_31;
        unsigned o30_0, o30_1, o30_2, o30_3, o31_0, o31_1, o31_2, o31_3, o00_0, o00_1, o00_2, o00_3, o01_0, o01_1, o01_2, o01_3, o02_0, o02_1, o02_2, o02_3, o03_0, o03_1, o03_2, o03_3, o10_0, o10_1, o10_2, o10_3, o20_0, o20_1, o20_2, o20_3, o11_0, o11_1, o11_2, o11_3, o21_0, o21_1, o21_2, o21_3, o12_0, o12_1, o12_2, o12_3, o22_0, o22_1, o22_2, o22_3, o13_0, o13_1, o13_2, o13_3, o23_0, o23_1, o23_2, o23_3;
        MF(0, 0, 1);
        MF(1, 0, 1);
        MF(2, 0, 1);
        MF(3, 0, 1);
        MF(0, 1, 1);
        MF(1, 1, 1);
        MF(2, 1, 1);
        MF(3, 1, 1);
        MF(0, 2, 1);
        MF(1, 2, 1);
        MF(2, 2, 1);
        MF(3, 2, 1);
        MF(0, 3, 1);
        MF(1, 3, 1);
        MF(2, 3, 1);
        asm volatile("ds_read_b96 %0, %1" : "=v"(fq) : "v"(fullv) : "memory");
        MF(3, 3, 1);
        MF(0, 4, 1);
        MF(1, 4, 1);
        MF(2, 4, 1);
        MF(3, 4, 1);
        asm volatile("s_waitcnt lgkmcnt(0)" : "+v"(fq)::"memory");
        known = max(known, (unsigned)__builtin_amdgcn_readfirstlane(min(fq[0], fq[1])));
        NEED(12);
        SB();
        asm volatile("ds_read_b96 %0, %1" : "=v"(fq) : "v"(fullv) : "memory");
        DSR(ta0_12, aA0[SU(12)], SO(12) + 0);
        DSR(tb30_12, aM[SU(12)], SO(12) + 0);
        MF(0, 5, 1);
        DSR(ta1_12, aA0[SU(12)], SO(12) + 1024);
        MF(1, 5, 1);
        DSR(tb31_12, aM[SU(12)], SO(12) + 1792);
        MF(2, 5, 1);
        MF(3, 5, 1);
        asm volatile("s_waitcnt lgkmcnt(4)" : "+v"(fq)::"memory");
        known = max(known, (unsigned)__builtin_amdgcn_readfirstlane(min(fq[0], fq[1])));
        NEED(13);
        SB();
        asm volatile("ds_read_b96 %0, %1" : "=v"(fq) : "v"(fullv) : "memory");
        DSR(ta0_13, aA0[SU(13)], SO(13) + 0);
        DSR(tb30_13, aM[SU(13)], SO(13) + 0);
        MM(0, 0, 1);
        DSR(ta1_13, aA0[SU(13)], SO(13) + 1024);
        MM(1, 0, 1);
        DSR(tb31_13, aM[SU(13)], SO(13) + 1792);
        MM(0, 1, 1);
        MM(1, 1, 1);
        TS(7);
        asm volatile("s_waitcnt lgkmcnt(4)" : "+v"(fq)::"memory");
        known = max(known, (unsigned)__builtin_amdgcn_readfirstlane(min(fq[0], fq[1])));
        NEED(14);
        SB();
        asm volatile("ds_read_b96 %0, %1" : "=v"(fq) : "v"(fullv) : "memory");
        DSR(ta0_14, aA0[SU(14)], SO(14) + 0);
        DSR(tb30_14, aM[SU(14)], SO(14) + 0);
        MFX(acm[0][0], tb30_12, ta0_12);
        DSR(ta1_14, aA0[SU(14)], SO(14) + 1024);
        MFX(acm[1][0], tb30_12, ta1_12);
        DSR(tb31_14, aM[SU(14)], SO(14) + 1792);
        MFX(acm[0][1], tb31_12, ta0_12);
        MFX(acm[1][1], tb31_12, ta1_12);
        asm volatile("s_waitcnt lgkmcnt(4)" : "+v"(fq)::"memory");
        known = max(known, (unsigned)__builtin_amdgcn_readfirstlane(min(fq[0], fq[1])));
        NEED(15);
        SB();
        asm volatile("ds_read_b96 %0, %1" : "=v"(fq) : "v"(fullv) : "memory");
        DSR(ta0_15, aA0[SU(15)], SO(15) + 0);
        DSR(tb30_15, aM[SU(15)], SO(15) + 0);
        MFX(acm[0][0], tb30_13, ta0_13);
        DSR(ta1_15, aA0[SU(15)], SO(15) + 1024);
        MFX(acm[1][0], tb30_13, ta1_13);
        DSR(tb31_15, aM[SU(15)], SO(15) + 1792);
        MFX(acm[0][1], tb31_13, ta0_13);
        MFX(acm[1][1], tb31_13, ta1_13);
        asm volatile("s_waitcnt lgkmcnt(2)" ::: "memory");
        DSR(tb00_12, aG2[SU(12)], SO(12) + 0);
        MFX(acm[0][0], tb30_14, ta0_14);
        asm volatile("s_waitcnt lgkmcnt(3)" : "+v"(fq)::"memory");
        known = max(known, (unsigned)__builtin_amdgcn_readfirstlane(min(fq[0], fq[1]))); crdy = __builtin_amdgcn_readfirstlane(fq[2]);
        CORRW();
        SB();
        DSR2(ks3, cad3, 0);
        DSR(tb01_12, aG2[SU(12)], SO(12) + 1792);
        MFX(acm[1][0], tb30_14, ta1_14);
        DSR2(kb3, cad3, 896);
        DSR(tb00_13, aG2[SU(13)], SO(13) + 0);
        MFX(acm[0][1], tb31_14, ta0_14);
        DSR(tb01_13, aG2[SU(13)], SO(13) + 1792);
        MFX(acm[1][1], tb31_14, ta1_14);
        DSR(tb00_14, aG2[SU(14)], SO(14) + 0);
        MFX(acm[0][0], tb30_15, ta0_15);
        asm volatile("s_waitcnt lgkmcnt(4)" ::: "memory");
        DSR(tb01_14, aG2[SU(14)], SO(14) + 1792);
        MFX(acm[1][0], tb30_15, ta1_15);
        DSR(tb00_15, aG2[SU(15)], SO(15) + 0);
        MFX(acm[0][1], tb31_15, ta0_15);
        DSR(tb01_15, aG2[SU(15)], SO(15) + 1792);
        MFX(acm[1][1], tb31_15, ta1_15);
        CVF(f30_00, acm[0][0][0]);
        CVF(f30_10, acm[0][0][1]);
        CVF(f30_20, acm[0][0][2]);
        CVF(f30_30, acm[0][0][3]);
        MFX(acc[0][0], tb00_12, ta0_12);
        asm volatile("s_waitcnt lgkmcnt(2)" ::: "memory");
        FMA(r30_00, f30_00, ks3[0], kb3[0]);
        MFX(acc[0][1], tb01_12, ta0_12);
        CVF(f30_01, acm[0][1][0]);
        FMA(r30_10, f30_10, ks3[0], kb3[0]);
        CVF(f30_11, acm[0][1][1]);
        FMA(r30_20, f30_20, ks3[0], kb3[0]);
        MFX(acc[0][0], tb00_13, ta0_13);
        CVF(f30_21, acm[0][1][2]);
        FMA(r30_30, f30_30, ks3[0], kb3[0]);
        CVF(f30_31, acm[0][1][3]);
        FMA(r30_01, f30_01, ks3[1], kb3[1]);
        MFX(acc[0][1], tb01_13, ta0_13);
        DSR2(ks0, cad0, 0);
        FMA(r30_11, f30_11, ks3[1], kb3[1]);
        FMA(r30_21, f30_21, ks3[1], kb3[1]);
        MFX(acc[0][0], tb00_14, ta0_14);
        DSR2(kb0, cad0, 896);
        FMA(r30_31, f30_31, ks3[1], kb3[1]);
        MFX(acc[0][1], tb01_14, ta0_14);
        asm volatile("s_waitcnt lgkmcnt(2)" ::: "memory");
        DSR(ks4, cad4, 0);
        CPK(o30_0, r30_00, r30_01);
        MFX(acc[0][0], tb00_15, ta0_15);
        DSR(kb4, cad4, 896);
        TS(9);
        ST1(o30_0, vM, 0, 0);
        CPK(o30_1, r30_10, r30_11);
        MFX(acc[0][1], tb01_15, ta0_15);
        DSR(ta2_12, aA2[SU(12)], SO(12) + 0);
        ST1(o30_1, vM, 0, 1);
        CPK(o30_2, r30_20, r30_21);
        MFX(acc[1][0], tb00_12, ta1_12);
        CPK(o30_3, r30_30, r30_31);
        CVF(f31_00, acm[1][0][0]);
        CVF(f31_10, acm[1][0][1]);
        MFX(acc[1][1], tb01_12, ta1_12);
        DSR(ta2_13, aA2[SU(13)], SO(13) + 0);
        ST1(o30_2, vM, 0, 2);
        ST1(o30_3, vM, 0, 3);
        CVF(f31_20, acm[1][0][2]);
        CVF(f31_30, acm[1][0][3]);
        MFX(acc[1][0], tb00_13, ta1_13);
        FMA(r31_00, f31_00, ks3[0], kb3[0]);
        CVF(f31_01, acm[1][1][0]);
        FMA(r31_10, f31_10, ks3[0], kb3[0]);
        CVF(f31_11, acm[1][1][1]);
        MFX(acc[1][1], tb01_13, ta1_13);
        DSR(ta2_14, aA2[SU(14)], SO(14) + 0);
        FMA(r31_20, f31_20, ks3[0], kb3[0]);
        CVF(f31_21, acm[1][1][2]);
        MFX(acc[1][0], tb00_14, ta1_14);
        FMA(r31_30, f31_30, ks3[0], kb3[0]);
        CVF(f31_31, acm[1][1][3]);
        FMA(r31_01, f31_01, ks3[1], kb3[1]);
        FMA(r31_11, f31_11, ks3[1], kb3[1]);
        MFX(acc[1][1], tb01_14, ta1_14);
        DSR(ta2_15, aA2[SU(15)], SO(15) + 0);
        FMA(r31_21, f31_21, ks3[1], kb3[1]);
        FMA(r31_31, f31_31, ks3[1], kb3[1]);
        MFX(acc[1][0], tb00_15, ta1_15);
        CPK(o31_0, r31_00, r31_01);
        CPK(o31_1, r31_10, r31_11);
        MFX(acc[1][1], tb01_15, ta1_15);
        asm volatile("s_waitcnt lgkmcnt(1)" ::: "memory");
        DSR(ta3_12, aA2[SU(12)], SO(12) + 1024);
        ST1(o31_0, vM, 1, 0);
        ST1(o31_1, vM, 1, 1);
        CPK(o31_2, r31_20, r31_21);
        MFX(acc[2][0], tb00_12, ta2_12);
        CPK(o31_3, r31_30, r31_31);
        CVF(f00_00, acc[0][0][0]);
        CVF(f00_10, acc[0][0][1]);
        MFX(acc[2][1], tb01_12, ta2_12);
        DSR(ta3_13, aA2[SU(13)], SO(13) + 1024);
        ST1(o31_2, vM, 1, 2);
        ST1(o31_3, vM, 1, 3);
        CVF(f00_20, acc[0][0][2]);
        CVF(f00_30, acc[0][0][3]);
        MFX(acc[2][0], tb00_13, ta2_13);
        FMA(r00_00, f00_00, ks0[0], kb0[0]);
        CVF(f00_01, acc[0][1][0]);
        FMA(r00_10, f00_10, ks0[0], kb0[0]);
        CVF(f00_11, acc[0][1][1]);
        MFX(acc[2][1], tb01_13, ta2_13);
        DSR(ta3_14, aA2[SU(14)], SO(14) + 1024);
        FMA(r00_20, f00_20, ks0[0], kb0[0]);
        CVF(f00_21, acc[0][1][2]);
        MFX(acc[2][0], tb00_14, ta2_14);
        FMA(r00_30, f00_30, ks0[0], kb0[0]);
        CVF(f00_31, acc[0][1][3]);
        FMA(r00_01, f00_01, ks0[1], kb0[1]);
        FMA(r00_11, f00_11, ks0[1], kb0[1]);
        MFX(acc[2][1], tb01_14, ta2_14);
        asm volatile("s_waitcnt lgkmcnt(1)" ::: "memory");
        DSR(ta3_15, aA2[SU(15)], SO(15) + 1024);
        FMA(r00_21, f00_21, ks0[1], kb0[1]);
        FMA(r00_31, f00_31, ks0[1], kb0[1]);
        MFX(acc[2][0], tb00_15, ta2_15);
        CPK(o00_0, r00_00, r00_01);
        CPK(o00_1, r00_10, r00_11);
        MFX(acc[2][1], tb01_15, ta2_15);
        DSR(tb10_12, aG4[SU(12)], SO(12) + 0);
        ST1(o00_0, vG2, 0, 0);
        ST1(o00_1, vG2, 0, 1);
        CPK(o00_2, r00_20, r00_21);
        MFX(acc[3][0], tb00_12, ta3_12);
        DSR(tb11_12, aG4[SU(12)], SO(12) + 1792);
        ST1(o00_2, vG2, 0, 2);
        CPK(o00_3, r00_30, r00_31);
        MFX(acc[3][1], tb01_12, ta3_12);
        DSR(tb10_13, aG4[SU(13)], SO(13) + 0);
        ST1(o00_3, vG2, 0, 3);
        CVF(f01_00, acc[1][0][0]);
        CVF(f01_10, acc[1][0][1]);
        MFX(acc[3][0], tb00_13, ta3_13);
        DSR(tb11_13, aG4[SU(13)], SO(13) + 1792);
        CVF(f01_20, acc[1][0][2]);
        CVF(f01_30, acc[1][0][3]);
        MFX(acc[3][1], tb01_13, ta3_13);
        asm volatile("s_waitcnt lgkmcnt(3)" ::: "memory");
        DSR(tb10_14, aG4[SU(14)], SO(14) + 0);
        FMA(r01_00, f01_00, ks0[0], kb0[0]);
        CVF(f01_01, acc[1][1][0]);
        MFX(acc[3][0], tb00_14, ta3_14);
        DSR(tb11_14, aG4[SU(14)], SO(14) + 1792);
        FMA(r01_10, f01_10, ks0[0], kb0[0]);
        CVF(f01_11, acc[1][1][1]);
        MFX(acc[3][1], tb01_14, ta3_14);
        DSR(tb10_15, aG4[SU(15)], SO(15) + 0);
        FMA(r01_20, f01_20, ks0[0], kb0[0]);
        CVF(f01_21, acc[1][1][2]);
        MFX(acc[3][0], tb00_15, ta3_15);
        DSR(tb11_15, aG4[SU(15)], SO(15) + 1792);
        FMA(r01_30, f01_30, ks0[0], kb0[0]);
        CVF(f01_31, acc[1][1][3]);
        MFX(acc[3][1], tb01_15, ta3_15);
        DSR(tb20_12, aG4[SU(12)], SO(12) + 896);
        FMA(r01_01, f01_01, ks0[1], kb0[1]);
        FMA(r01_11, f01_11, ks0[1], kb0[1]);
        MFX(acc[0][2], tb10_12, ta0_12);
        asm volatile("s_waitcnt lgkmcnt(3)" ::: "memory");
        DSR(tb21_12, aG4[SU(12)], SO(12) + 2688);
        FMA(r01_21, f01_21, ks0[1], kb0[1]);
        FMA(r01_31, f01_31, ks0[1], kb0[1]);
        MFX(acc[0][3], tb11_12, ta0_12);
        DSR(tb20_13, aG4[SU(13)], SO(13) + 896);
        CPK(o01_0, r01_00, r01_01);
        MFX(acc[0][2], tb10_13, ta0_13);
        DSR(tb21_13, aG4[SU(13)], SO(13) + 2688);
        ST1(o01_0, vG2, 1, 0);
        CPK(o01_1, r01_10, r01_11);
        MFX(acc[0][3], tb11_13, ta0_13);
        DSR(tb20_14, aG4[SU(14)], SO(14) + 896);
        ST1(o01_1, vG2, 1, 1);
        CPK(o01_2, r01_20, r01_21);
        MFX(acc[0][2], tb10_14, ta0_14);
        DSR(tb21_14, aG4[SU(14)], SO(14) + 2688);
        ST1(o01_2, vG2, 1, 2);
        CPK(o01_3, r01_30, r01_31);
        MFX(acc[0][3], tb11_14, ta0_14);
        asm volatile("s_waitcnt lgkmcnt(3)" ::: "memory");
        DSR(tb20_15, aG4[SU(15)], SO(15) + 896);
        ST1(o01_3, vG2, 1, 3);
        CVF(f02_00, acc[2][0][0]);
        CVF(f02_10, acc[2][0][1]);
        MFX(acc[0][2], tb10_15, ta0_15);
        DSR(tb21_15, aG4[SU(15)], SO(15) + 2688);
        CVF(f02_20, acc[2][0][2]);
        CVF(f02_30, acc[2][0][3]);
        MFX(acc[0][3], tb11_15, ta0_15);
        FMA(r02_00, f02_00, ks0[0], kb0[0]);
        CVF(f02_01, acc[2][1][0]);
        FMA(r02_10, f02_10, ks0[0], kb0[0]);
        CVF(f02_11, acc[2][1][1]);
        MFX(acc[0][4], tb20_12, ta0_12);
        FMA(r02_20, f02_20, ks0[0], kb0[0]);
        CVF(f02_21, acc[2][1][2]);
        FMA(r02_30, f02_30, ks0[0], kb0[0]);
        CVF(f02_31, acc[2][1][3]);
        MFX(acc[0][5], tb21_12, ta0_12);
        FMA(r02_01, f02_01, ks0[1], kb0[1]);
        FMA(r02_11, f02_11, ks0[1], kb0[1]);
        FMA(r02_21, f02_21, ks0[1], kb0[1]);
        FMA(r02_31, f02_31, ks0[1], kb0[1]);
        MFX(acc[0][4], tb20_13, ta0_13);
        asm volatile("s_waitcnt lgkmcnt(0)" ::: "memory");
        CPK(o02_0, r02_00, r02_01);
        CPK(o02_1, r02_10, r02_11);
        MFX(acc[0][5], tb21_13, ta0_13);
        ST1(o02_0, vG2, 2, 0);
        ST1(o02_1, vG2, 2, 1);
        CPK(o02_2, r02_20, r02_21);
        MFX(acc[0][4], tb20_14, ta0_14);
        CPK(o02_3, r02_30, r02_31);
        CVF(f03_00, acc[3][0][0]);
        CVF(f03_10, acc[3][0][1]);
        MFX(acc[0][5], tb21_14, ta0_14);
        ST1(o02_2, vG2, 2, 2);
        ST1(o02_3, vG2, 2, 3);
        CVF(f03_20, acc[3][0][2]);
        CVF(f03_30, acc[3][0][3]);
        MFX(acc[0][4], tb20_15, ta0_15);
        FMA(r03_00, f03_00, ks0[0], kb0[0]);
        CVF(f03_01, acc[3][1][0]);
        FMA(r03_10, f03_10, ks0[0], kb0[0]);
        CVF(f03_11, acc[3][1][1]);
        MFX(acc[0][5], tb21_15, ta0_15);
        FMA(r03_20, f03_20, ks0[0], kb0[0]);
        CVF(f03_21, acc[3][1][2]);
        FMA(r03_30, f03_30, ks0[0], kb0[0]);
        CVF(f03_31, acc[3][1][3]);
        MFX(acc[1][2], tb10_12, ta1_12);
        FMA(r03_01, f03_01, ks0[1], kb0[1]);
        FMA(r03_11, f03_11, ks0[1], kb0[1]);
        FMA(r03_21, f03_21, ks0[1], kb0[1]);
        FMA(r03_31, f03_31, ks0[1], kb0[1]);
        MFX(acc[1][3], tb11_12, ta1_12);
        CPK(o03_0, r03_00, r03_01);
        CPK(o03_1, r03_10, r03_11);
        MFX(acc[1][2], tb10_13, ta1_13);
        ST1(o03_0, vG2, 3, 0);
        ST1(o03_1, vG2, 3, 1);
        CPK(o03_2, r03_20, r03_21);
        MFX(acc[1][3], tb11_13, ta1_13);
        CPK(o03_3, r03_30, r03_31);
        CVF(f10_00, acc[0][2][0]);
        CVF(f10_10, acc[0][2][1]);
        MFX(acc[1][2], tb10_14, ta1_14);
        ST1(o03_2, vG2, 3, 2);
        ST1(o03_3, vG2, 3, 3);
        CVF(f10_20, acc[0][2][2]);
        CVF(f10_30, acc[0][2][3]);
        MFX(acc[1][3], tb11_14, ta1_14);
        FMA(r10_00, f10_00, ks4[0], kb4[0]);
        CVF(f10_01, acc[0][3][0]);
        FMA(r10_10, f10_10, ks4[0], kb4[0]);
        CVF(f10_11, acc[0][3][1]);
        MFX(acc[1][2], tb10_15, ta1_15);
        FMA(r10_20, f10_20, ks4[0], kb4[0]);
        CVF(f10_21, acc[0][3][2]);
        FMA(r10_30, f10_30, ks4[0], kb4[0]);
        CVF(f10_31, acc[0][3][3]);
        MFX(acc[1][3], tb11_15, ta1_15);
        FMA(r10_01, f10_01, ks4[1], kb4[1]);
        FMA(r10_11, f10_11, ks4[1], kb4[1]);
        FMA(r10_21, f10_21, ks4[1], kb4[1]);
        FMA(r10_31, f10_31, ks4[1], kb4[1]);
        MFX(acc[1][4], tb20_12, ta1_12);
        CPK(o10_0, r10_00, r10_01);
        CPK(o10_1, r10_10, r10_11);
        MFX(acc[1][5], tb21_12, ta1_12);
        CPK(o10_2, r10_20, r10_21);
        CPK(o10_3, r10_30, r10_31);
        MFX(acc[1][4], tb20_13, ta1_13);
        CVF(f20_00, acc[0][4][0]);
        CVF(f20_10, acc[0][4][1]);
        CVF(f20_20, acc[0][4][2]);
        CVF(f20_30, acc[0][4][3]);
        MFX(acc[1][5], tb21_13, ta1_13);
        FMA(r20_00, f20_00, ks4[2], kb4[2]);
        CVF(f20_01, acc[0][5][0]);
        FMA(r20_10, f20_10, ks4[2], kb4[2]);
        CVF(f20_11, acc[0][5][1]);
        MFX(acc[1][4], tb20_14, ta1_14);
        FMA(r20_20, f20_20, ks4[2], kb4[2]);
        CVF(f20_21, acc[0][5][2]);
        FMA(r20_30, f20_30, ks4[2], kb4[2]);
        CVF(f20_31, acc[0][5][3]);
        MFX(acc[1][5], tb21_14, ta1_14);
        FMA(r20_01, f20_01, ks4[3], kb4[3]);
        FMA(r20_11, f20_11, ks4[3], kb4[3]);
        FMA(r20_21, f20_21, ks4[3], kb4[3]);
        FMA(r20_31, f20_31, ks4[3], kb4[3]);
        MFX(acc[1][4], tb20_15, ta1_15);
        CPK(o20_0, r20_00, r20_01);
        CPK(o20_1, r20_10, r20_11);
        MFX(acc[1][5], tb21_15, ta1_15);
        ST2(o10_0, o20_0, 0, 0);
        ST2(o10_1, o20_1, 0, 1);
        CPK(o20_2, r20_20, r20_21);
        MFX(acc[2][2], tb10_12, ta2_12);
        CPK(o20_3, r20_30, r20_31);
        CVF(f11_00, acc[1][2][0]);
        CVF(f11_10, acc[1][2][1]);
        MFX(acc[2][3], tb11_12, ta2_12);
        ST2(o10_2, o20_2, 0, 2);
        ST2(o10_3, o20_3, 0, 3);
        CVF(f11_20, acc[1][2][2]);
        CVF(f11_30, acc[1][2][3]);
        MFX(acc[2][2], tb10_13, ta2_13);
        FMA(r11_00, f11_00, ks4[0], kb4[0]);
        CVF(f11_01, acc[1][3][0]);
        FMA(r11_10, f11_10, ks4[0], kb4[0]);
        CVF(f11_11, acc[1][3][1]);
        MFX(acc[2][3], tb11_13, ta2_13);
        FMA(r11_20, f11_20, ks4[0], kb4[0]);
        CVF(f11_21, acc[1][3][2]);
        FMA(r11_30, f11_30, ks4[0], kb4[0]);
        CVF(f11_31, acc[1][3][3]);
        MFX(acc[2][2], tb10_14, ta2_14);
        FMA(r11_01, f11_01, ks4[1], kb4[1]);
        FMA(r11_11, f11_11, ks4[1], kb4[1]);
        FMA(r11_21, f11_21, ks4[1], kb4[1]);
        FMA(r11_31, f11_31, ks4[1], kb4[1]);
        MFX(acc[2][3], tb11_14, ta2_14);
        CPK(o11_0, r11_00, r11_01);
        CPK(o11_1, r11_10, r11_11);
        MFX(acc[2][2], tb10_15, ta2_15);
        CPK(o11_2, r11_20, r11_21);
        CPK(o11_3, r11_30, r11_31);
        MFX(acc[2][3], tb11_15, ta2_15);
        CVF(f21_00, acc[1][4][0]);
        CVF(f21_10, acc[1][4][1]);
        CVF(f21_20, acc[1][4][2]);
        CVF(f21_30, acc[1][4][3]);
        MFX(acc[2][4], tb20_12, ta2_12);
        FMA(r21_00, f21_00, ks4[2], kb4[2]);
        CVF(f21_01, acc[1][5][0]);
        FMA(r21_10, f21_10, ks4[2], kb4[2]);
        CVF(f21_11, acc[1][5][1]);
        MFX(acc[2][5], tb21_12, ta2_12);
        FMA(r21_20, f21_20, ks4[2], kb4[2]);
        CVF(f21_21, acc[1][5][2]);
        FMA(r21_30, f21_30, ks4[2], kb4[2]);
        CVF(f21_31, acc[1][5][3]);
        MFX(acc[2][4], tb20_13, ta2_13);
        FMA(r21_01, f21_01, ks4[3], kb4[3]);
        FMA(r21_11, f21_11, ks4[3], kb4[3]);
        FMA(r21_21, f21_21, ks4[3], kb4[3]);
        FMA(r21_31, f21_31, ks4[3], kb4[3]);
        MFX(acc[2][5], tb21_13, ta2_13);
        CPK(o21_0, r21_00, r21_01);
        CPK(o21_1, r21_10, r21_11);
        MFX(acc[2][4], tb20_14, ta2_14);
        ST2(o11_0, o21_0, 1, 0);
        ST2(o11_1, o21_1, 1, 1);
        CPK(o21_2, r21_20, r21_21);
        MFX(acc[2][5], tb21_14, ta2_14);
        CPK(o21_3, r21_30, r21_31);
        CVF(f12_00, acc[2][2][0]);
        CVF(f12_10, acc[2][2][1]);
        MFX(acc[2][4], tb20_15, ta2_15);
        ST2(o11_2, o21_2, 1, 2);
        ST2(o11_3, o21_3, 1, 3);
        CVF(f12_20, acc[2][2][2]);
        CVF(f12_30, acc[2][2][3]);
        MFX(acc[2][5], tb21_15, ta2_15);
        FMA(r12_00, f12_00, ks4[0], kb4[0]);
        CVF(f12_01, acc[2][3][0]);
        FMA(r12_10, f12_10, ks4[0], kb4[0]);
        CVF(f12_11, acc[2][3][1]);
        MFX(acc[3][2], tb10_12, ta3_12);
        FMA(r12_20, f12_20, ks4[0], kb4[0]);
        CVF(f12_21, acc[2][3][2]);
        FMA(r12_30, f12_30, ks4[0], kb4[0]);
        CVF(f12_31, acc[2][3][3]);
        MFX(acc[3][3], tb11_12, ta3_12);
        FMA(r12_01, f12_01, ks4[1], kb4[1]);
        FMA(r12_11, f12_11, ks4[1], kb4[1]);
        FMA(r12_21, f12_21, ks4[1], kb4[1]);
        FMA(r12_31, f12_31, ks4[1], kb4[1]);
        MFX(acc[3][2], tb10_13, ta3_13);
        CPK(o12_0, r12_00, r12_01);
        CPK(o12_1, r12_10, r12_11);
        MFX(acc[3][3], tb11_13, ta3_13);
        CPK(o12_2, r12_20, r12_21);
        CPK(o12_3, r12_30, r12_31);
        MFX(acc[3][2], tb10_14, ta3_14);
        CVF(f22_00, acc[2][4][0]);
        CVF(f22_10, acc[2][4][1]);
        CVF(f22_20, acc[2][4][2]);
        CVF(f22_30, acc[2][4][3]);
        MFX(acc[3][3], tb11_14, ta3_14);
        FMA(r22_00, f22_00, ks4[2], kb4[2]);
        CVF(f22_01, acc[2][5][0]);
        FMA(r22_10, f22_10, ks4[2], kb4[2]);
        CVF(f22_11, acc[2][5][1]);
        MFX(acc[3][2], tb10_15, ta3_15);
        FMA(r22_20, f22_20, ks4[2], kb4[2]);
        CVF(f22_21, acc[2][5][2]);
        FMA(r22_30, f22_30, ks4[2], kb4[2]);
        CVF(f22_31, acc[2][5][3]);
        MFX(acc[3][3], tb11_15, ta3_15);
        FMA(r22_01, f22_01, ks4[3], kb4[3]);
        FMA(r22_11, f22_11, ks4[3], kb4[3]);
        FMA(r22_21, f22_21, ks4[3], kb4[3]);
        FMA(r22_31, f22_31, ks4[3], kb4[3]);
        MFX(acc[3][4], tb20_12, ta3_12);
        CPK(o22_0, r22_00, r22_01);
        CPK(o22_1, r22_10, r22_11);
        MFX(acc[3][5], tb21_12, ta3_12);
        ST2(o12_0, o22_0, 2, 0);
        ST2(o12_1, o22_1, 2, 1);
        CPK(o22_2, r22_20, r22_21);
        MFX(acc[3][4], tb20_13, ta3_13);
        CPK(o22_3, r22_30, r22_31);
        CVF(f13_00, acc[3][2][0]);
        CVF(f13_10, acc[3][2][1]);
        MFX(acc[3][5], tb21_13, ta3_13);
        ST2(o12_2, o22_2, 2, 2);
        ST2(o12_3, o22_3, 2, 3);
        CVF(f13_20, acc[3][2][2]);
        CVF(f13_30, acc[3][2][3]);
        MFX(acc[3][4], tb20_14, ta3_14);
        FMA(r13_00, f13_00, ks4[0], kb4[0]);
        CVF(f13_01, acc[3][3][0]);
        FMA(r13_10, f13_10, ks4[0], kb4[0]);
        CVF(f13_11, acc[3][3][1]);
        MFX(acc[3][5], tb21_14, ta3_14);
        FMA(r13_20, f13_20, ks4[0], kb4[0]);
        CVF(f13_21, acc[3][3][2]);
        FMA(r13_30, f13_30, ks4[0], kb4[0]);
        CVF(f13_31, acc[3][3][3]);
        MFX(acc[3][4], tb20_15, ta3_15);
        FMA(r13_01, f13_01, ks4[1], kb4[1]);
        FMA(r13_11, f13_11, ks4[1], kb4[1]);
        FMA(r13_21, f13_21, ks4[1], kb4[1]);
        FMA(r13_31, f13_31, ks4[1], kb4[1]);
        MFX(acc[3][5], tb21_15, ta3_15);
        TS(8);
        asm volatile("s_nop 7" ::: "memory");
        CPK(o13_0, r13_00, r13_01);
        CPK(o13_1, r13_10, r13_11);
        CPK(o13_2, r13_20, r13_21);
        CPK(o13_3, r13_30, r13_31);
        CVF(f23_00, acc[3][4][0]);
        CVF(f23_10, acc[3][4][1]);
        CVF(f23_20, acc[3][4][2]);
        CVF(f23_30, acc[3][4][3]);
        FMA(r23_00, f23_00, ks4[2], kb4[2]);
        CVF(f23_01, acc[3][5][0]);
        FMA(r23_10, f23_10, ks4[2], kb4[2]);
        CVF(f23_11, acc[3][5][1]);
        FMA(r23_20, f23_20, ks4[2], kb4[2]);
        CVF(f23_21, acc[3][5][2]);
        FMA(r23_30, f23_30, ks4[2], kb4[2]);
        CVF(f23_31, acc[3][5][3]);
        FMA(r23_01, f23_01, ks4[3], kb4[3]);
        FMA(r23_11, f23_11, ks4[3], kb4[3]);
        FMA(r23_21, f23_21, ks4[3], kb4[3]);
        FMA(r23_31, f23_31, ks4[3], kb4[3]);
        CPK(o23_0, r23_00, r23_01);
        CPK(o23_1, r23_10, r23_11);
        ST2(o13_0, o23_0, 3, 0);
        ST2(o13_1, o23_1, 3, 1);
        CPK(o23_2, r23_20, r23_21);
        CPK(o23_3, r23_30, r23_31);
        ST2(o13_2, o23_2, 3, 2);
        ST2(o13_3, o23_3, 3, 3);
    }
    asm volatile(".Lpf_end:");
    TS(10);
}

extern "C" void kernel_launch(void* const* d_in, const int* in_sizes, int n_in,
                              void* d_out, int out_size, void* d_ws, size_t ws_size,
                              hipStream_t stream) {
    const int* qx = (const int*)d_in[0];
    const int* qw = (const int*)d_in[1];
    const __hip_bfloat16* scale_x = (const __hip_bfloat16*)d_in[2];
    const int* zero_x = (const int*)d_in[3];
    const __hip_bfloat16* scale_w = (const __hip_bfloat16*)d_in[4];
    const __hip_bfloat16* bias = (const __hip_bfloat16*)d_in[5];
    (void)hipFuncSetAttribute((const void*)fused_kernel, hipFuncAttributeMaxDynamicSharedMemorySize, SMEM_BYTES);
    fused_kernel<<<256, 512, SMEM_BYTES, stream>>>(qx, qw, scale_x, zero_x, scale_w, bias, (__hip_bfloat16*)d_out,
                                                   (uint8_t*)d_ws);
}
```

```cpp
#include <hip/hip_runtime.h>
#include <hip/hip_bf16.h>
#include <stdint.h>

typedef int i32x4 __attribute__((ext_vector_type(4)));
typedef unsigned int u32x4 __attribute__((ext_vector_type(4)));
typedef unsigned int u32x2 __attribute__((ext_vector_type(2)));
typedef float f32x2 __attribute__((ext_vector_type(2)));
typedef float f32x4 __attribute__((ext_vector_type(4)));
typedef __bf16 bf16x2_t __attribute__((ext_vector_type(2)));
typedef unsigned long long u64;
#define GAS __attribute__((address_space(1)))
#define LAS __attribute__((address_space(3)))

constexpr int M_ = 2048, K_ = 1024, N_ = 3584;
constexpr int BM = 128, BN = 224;
constexpr int NSTAGE = 7;
constexpr int UNIT = 3584;
constexpr int A_STAGE = 8192, B_STAGE = 4 * UNIT;
constexpr int STAGE_BYTES = A_STAGE + B_STAGE;
constexpr int RING_BYTES = NSTAGE * STAGE_BYTES;
constexpr int OFF_CTL = RING_BYTES;
constexpr int CT_FULL = 0, CT_ARR = 48, CT_GO = 56, CT_SIG = 64;
constexpr int OFF_SCALE = OFF_CTL + 128;
constexpr int OFF_BIAS = OFF_SCALE + 896;
constexpr int OFF_FREE = OFF_BIAS + 896 + 4;
constexpr int SMEM_BYTES = OFF_FREE + 1024 + 4;
constexpr size_t WS_EPOCH = 0;
constexpr size_t WS_FLAGS = 8u << 20;
constexpr size_t WS_A8 = 65536;
constexpr size_t WS_B8 = WS_A8 + (size_t)M_ * K_;
constexpr size_t WS_CP = WS_B8 + (size_t)N_ * K_;
constexpr size_t WS_REG = 16u << 20;
constexpr int AUX_SC1 = 16;
constexpr int RSRC_W3 = 0x00020000;
#define NCA 1
#define NCB 1
#define FL 128
#define NREG 1
#define CPH (16 * (NCA + NCB) * FL)
#define SGATE 1
#define CPF 10
#define SLAG 2
#define ATR 0
#define FMASK 0xffff
#define CPRIO 0
#define PW 6
#define FI 1
#define ORD 0
#define TG (23 - PW)
#define RSIG 0
#define RDP 8
#define DMA_AUX 16
#define OUT_AUX 18
#define RAW_AUX 2

constexpr int styp(int t) {
    constexpr int o[3][6] = {{4, 5, 0, 1, 2, 3}, {0, 1, 2, 3, 4, 5}, {0, 4, 1, 2, 5, 3}};
    return o[ORD][t % 6];
}
constexpr int ops_after(int p, int t) {
    int n = 0;
    for (int s = 6 * p + 5; s <= t; ++s) {
        if (s > 6 * p + 5) n += styp(s) >= 4 ? 1 : styp(s) == 3 ? 4 : 0;
        if (s + PW < 24) n += 4;
    }
    return n;
}
__device__ __forceinline__ unsigned pack4(int a, int b, int c, int d) {
    const unsigned lo = __builtin_amdgcn_perm((unsigned)b, (unsigned)a, 0x0c0c0400u);
    const unsigned hi = __builtin_amdgcn_perm((unsigned)d, (unsigned)c, 0x0c0c0400u);
    return __builtin_amdgcn_perm(hi, lo, 0x05040100u);
}
__device__ __forceinline__ u32x4 lds_rd128(unsigned addr) {
    u32x4 v;
    asm volatile("ds_read_b128 %0, %1\n\ts_waitcnt lgkmcnt(0)" : "=v"(v) : "v"(addr) : "memory");
    return v;
}
__device__ __forceinline__ void lds_wr32(unsigned addr, unsigned val) {
    asm volatile("ds_write_b32 %0, %1" ::"v"(addr), "v"(val) : "memory");
}
#define SPIN for (;;)

__global__ __launch_bounds__(512, 2) void fused_kernel(const int* __restrict__ qx, const int* __restrict__ qw,
                                                       const __hip_bfloat16* __restrict__ scale_x,
                                                       const int* __restrict__ zero_x,
                                                       const __hip_bfloat16* __restrict__ scale_w,
                                                       const __hip_bfloat16* __restrict__ bias,
                                                       __hip_bfloat16* __restrict__ out, uint8_t* __restrict__ ws) {
    extern __shared__ __attribute__((aligned(16))) uint8_t smem[];
    const int tid = threadIdx.x, lane = tid & 63;
    const int wid = __builtin_amdgcn_readfirstlane(tid >> 6);
    const int bid = blockIdx.x;
    const int tm = (bid & 1) * 8 + ((bid >> 3) & 7);
    const int tn = ((bid >> 1) & 3) * 4 + (bid >> 6);
    const int tmA = bid >> 4, subA = bid & 15;
    const int tnB = bid & 15, subB = bid >> 4;
    const unsigned smem_base = (unsigned)(uintptr_t)smem;
    const unsigned ctl = smem_base + OFF_CTL;
    uint8_t* wr;
    u64 tag;
    unsigned pfd = 0;
    i32x4 rsT = {0, 0, 0, 0};
#define SIG(ADDR, V) asm volatile("s_mov_b64 exec, 1\n\ts_mov_b32 m0, %0\n\ts_nop 0\n\tbuffer_load_dword off, %1, %2 lds\n\ts_mov_b64 exec, -1" \
                                  ::"s"(ADDR), "s"(rsT), "n"((V) * 4) : "memory", "m0")
#define TAGWAIT()                                                                                                        \
    do {                                                                                                                 \
        asm volatile("s_load_dwordx2 %0, %1, 0x0\n\ts_waitcnt lgkmcnt(0)" : "=s"(tag) : "s"(ws + WS_EPOCH) : "memory"); \
        tag += 0x9E3779B97F4A7C15ull;                                                                                    \
        wr = ws + ((unsigned)tag & (NREG - 1)) * WS_REG;                                                                 \
    } while (0)

    if (wid == 4) {
        const __amdgpu_buffer_rsrc_t rsW = __builtin_amdgcn_make_buffer_rsrc((void*)qw, 0, K_ * N_ * 4, RSRC_W3);
        const __amdgpu_buffer_rsrc_t rsX = __builtin_amdgcn_make_buffer_rsrc((void*)qx, 0, M_ * K_ * 4, RSRC_W3);
        const int q = lane < 56 ? lane : lane - 8;
        const unsigned voB = (unsigned)((tnB * 224 + q * 4) * 4);
        const int soB = ((subB >> 2) * 64 + (subB & 3) * 4) * (N_ * 4);
        const int kb0 = 2 * (subA & 1);
        const unsigned voA = (unsigned)((((tmA * 8 + (subA >> 1)) * 16 + (lane >> 2)) * K_ + kb0 * 64 + ((lane & 3) ^ ((lane >> 5) * 2)) * 4) * 4);
        i32x4 st[PW][4];
        auto ld = [&](int t) {
            const int p = t / 6, j = styp(t);
#pragma unroll
            for (int x = 0; x < 4; ++x)
                st[t % PW][x] = __builtin_bit_cast(i32x4, j < 4 ? __builtin_amdgcn_raw_buffer_load_b128(rsW, voB, soB + (p * 256 + j * 16 + x) * (N_ * 4), RAW_AUX)
                                                                : __builtin_amdgcn_raw_buffer_load_b128(rsX, voA + p * 1024 + (j - 4) * 256 + x * 64, 0, RAW_AUX));
        };
#pragma unroll
        for (int t = 0; t < PW; ++t) ld(t);
        __builtin_amdgcn_sched_barrier(0);
        const unsigned wB = lane < 56 ? (unsigned)(M_ * K_ + (tnB * 64 + subB) * UNIT + lane * 16) : 0x80000000u;
        const unsigned wA = (unsigned)(((tmA * 16 + kb0) * 8 + (subA >> 1)) * 1024 + lane * 16);
        const unsigned vf0 = lane < NCB ? (unsigned)((16 * NCA + tnB * NCB + lane) * FL + subB * 8)
                           : (lane & 48) == 16 && (lane & 15) < NCA ? (unsigned)((tmA * NCA + (lane & 15)) * FL + (subA & 1) * 64 + (subA >> 1) * 8) : 0x80000000u;
        TAGWAIT();
        __builtin_amdgcn_s_barrier();
        __builtin_amdgcn_sched_barrier(0);
        const u32x2 tg = {(unsigned)tag, (unsigned)(tag >> 32)};
        const __amdgpu_buffer_rsrc_t rs8 = __builtin_amdgcn_make_buffer_rsrc((void*)(wr + WS_A8), 0, (M_ + N_) * K_ + 16 * 16 * 512, RSRC_W3);
        const __amdgpu_buffer_rsrc_t rsF = __builtin_amdgcn_make_buffer_rsrc((void*)(wr + WS_FLAGS), 0, 4 * CPH, RSRC_W3);
        int cs[4] = {0, 0, 0, 0};
        u32x4 pk[4];
#pragma unroll
        for (int t = 0; t < 24; ++t) {
            const int p = t / 6, j = styp(t);
            if (j < 4) {
#pragma unroll
                for (int c = 0; c < 4; ++c) {
                    pk[c][j] = pack4(st[t % PW][0][c], st[t % PW][1][c], st[t % PW][2][c], st[t % PW][3][c]);
                    cs[c] = __builtin_amdgcn_sdot4((int)pk[c][j], 0x01010101, cs[c], false);
                }
                if (j == 3) {
#pragma unroll
                    for (int c = 0; c < 4; ++c) __builtin_amdgcn_raw_buffer_store_b128(pk[c], rs8, wB + p * 16 * UNIT + ((c & 1) * 2 + (c >> 1)) * 896, 0, AUX_SC1);
                    if (p == 3) {
                        const u32x2 cp = {__builtin_amdgcn_perm((unsigned)cs[1], (unsigned)cs[0], 0x05040100u),
                                          __builtin_amdgcn_perm((unsigned)cs[3], (unsigned)cs[2], 0x05040100u)};
                        __builtin_amdgcn_raw_buffer_store_b64(cp, rs8, (unsigned)((M_ + N_) * K_ + (tnB * 16 + subB) * 512 + lane * 8), 0, AUX_SC1);
                    }
                }
            } else {
                u32x4 o;
#pragma unroll
                for (int x = 0; x < 4; ++x) o[x] = pack4(st[t % PW][x][0], st[t % PW][x][1], st[t % PW][x][2], st[t % PW][x][3]);
                __builtin_amdgcn_raw_buffer_store_b128(o, rs8, wA + (p * 4 + j - 4) * 8192, 0, AUX_SC1);
            }
            __builtin_amdgcn_sched_barrier(0);
            if (t + PW < 24) ld(t + PW);
            if (t == TG) lds_wr32(ctl + CT_SIG + 4, 1u);
            __builtin_amdgcn_sched_barrier(0);
            if (t % 6 == FI && t >= 6) {
                asm volatile("s_waitcnt vmcnt(%0)" ::"n"(ops_after(p - 1, t)) : "memory");
                __builtin_amdgcn_raw_buffer_store_b64(tg, rsF, vf0 + (p - 1) * CPH, 0, AUX_SC1);
                if (p == 1) lds_wr32(ctl + CT_GO, 1u);
                __builtin_amdgcn_sched_barrier(0);
            }
            if (t == 23) {
                asm volatile("s_waitcnt vmcnt(0)" ::: "memory");
                __builtin_amdgcn_raw_buffer_store_b64(tg, rsF, vf0 + 3 * CPH, 0, AUX_SC1);
            }
        }
    } else {
        u64 pc;
        int clen, toff;
        asm volatile(".Lpf_start:\n\ts_getpc_b64 %0\n\ts_mov_b32 %1, .Lpf_end-.Lpf_start\n\ts_mov_b32 %2, .Ltab-.Lpf_start-4\n\ts_branch .Ltab_end\n\t"
                     ".p2align 7\n.Ltab:\n\t.long 0, 1, 2, 3, 4, 5, 6, 7, 8, 9, 10, 11, 12, 13, 14, 15, 16, 0\n.Ltab_end:"
                     : "=s"(pc), "=s"(clen), "=s"(toff));
        {
            const u64 tp = pc + (unsigned)toff;
            rsT = (i32x4){(int)(unsigned)tp, (int)(unsigned)(tp >> 32), 68, RSRC_W3};
            asm volatile("" : "+s"(rsT));
        }
        if (CPF && wid < 4) {
            const __amdgpu_buffer_rsrc_t rsP = __builtin_amdgcn_make_buffer_rsrc((void*)pc, 0, max(clen, 4) - 4, RSRC_W3);
            const int idx = (bid >> 3) * 4 + wid;
            pfd = __builtin_amdgcn_raw_buffer_load_b32(rsP, idx < 3 * CPF ? (unsigned)((idx % CPF) * 4096 + lane * 64) : 0x80000000u, 0, 0);
        }
        if (wid == 6) lds_wr32(lane < 32 ? ctl + lane * 4 : smem_base + OFF_FREE + 252 + (lane & 1) * 4 + (lane & 2) * 256, 0u);
        TAGWAIT();
        __builtin_amdgcn_s_barrier();
    }

    if (wid >= 6) {
        const int lw = wid - 6;
        const __amdgpu_buffer_rsrc_t rsL = __builtin_amdgcn_make_buffer_rsrc((void*)(wr + WS_A8), 0, (M_ + N_) * K_, RSRC_W3);
        const int soA = tm * 131072 + lw * 1024, soB = M_ * K_ + tn * (64 * UNIT) + lw * 1024;
        const unsigned full = ctl + CT_FULL + lw * 4;
        unsigned rdy = 0, kfree = 0, aR = ctl + CT_SIG, aF = smem_base + OFF_FREE + 252;
        asm volatile("" : "+v"(aR), "+v"(aF));
        u32x2 vr;
        u32x4 vf;
#define LOOK() asm volatile("ds_read_b64 %0, %2\n\tds_read2st64_b64 %1, %3 offset1:1" : "=&v"(vr), "=&v"(vf) : "v"(aR), "v"(aF) : "memory")
#define SEEN()                                                                                     \
    do {                                                                                           \
        asm volatile("s_waitcnt lgkmcnt(0)" : "+v"(vr), "+v"(vf)::"memory");                       \
        rdy = __builtin_amdgcn_readfirstlane(vr[1] ? vr[0] : 0u);                                  \
        kfree = __builtin_amdgcn_readfirstlane(min(min(vf[0], vf[1]), min(vf[2], vf[3])));         \
    } while (0)
#pragma unroll
        for (int kb = 0; kb < 16; ++kb) {
            if (__builtin_expect(rdy <= (unsigned)(kb >> 1) || (kb >= NSTAGE && kfree <= (unsigned)(kb - NSTAGE)), 0)) {
                SPIN {
                    LOOK();
                    SEEN();
                    if (rdy > (unsigned)(kb >> 1) && (kb < NSTAGE || kfree > (unsigned)(kb - NSTAGE))) break;
                    __builtin_amdgcn_s_sleep(1);
                }
            }
            LAS uint8_t* base = (LAS uint8_t*)(uintptr_t)(smem_base + lw * 1024 + (kb % NSTAGE) * STAGE_BYTES);
#pragma unroll
            for (int i = 0; i < 11; ++i) {
                if (i == RDP && kb < 15) LOOK();
                const int so = i < 4 ? soA + kb * 8192 + i * 2048 : soB + kb * B_STAGE + (i - 4) * 2048;
                __builtin_amdgcn_raw_ptr_buffer_load_lds(rsL, base + i * 2048, 16, lane * 16, so, 0, DMA_AUX);
            }
            SIG(full, kb + 1);
            if (kb < 15) SEEN();
            asm volatile("s_waitcnt vmcnt(%0)" ::"n"(12 * SLAG) : "memory");
        }
        asm volatile("s_waitcnt vmcnt(0)" ::: "memory");
    } else if (wid == 5) {
        const int l8 = lane < 28 ? lane : 0;
        const u32x4 sw8 = reinterpret_cast<const u32x4*>(scale_w)[tn * (BN / 8) + l8];
        const u32x4 bi8 = reinterpret_cast<const u32x4*>(bias)[tn * (BN / 8) + l8];
        const float sx = __uint_as_float((unsigned)reinterpret_cast<const unsigned short*>(scale_x)[0] << 16);
        const float nzx = -(float)zero_x[0];
        f32x4 sc[2], bi[2];
#pragma unroll
        for (int e = 0; e < 4; ++e) {
            const unsigned w = sw8[e], c = bi8[e];
            sc[e >> 1][(e & 1) * 2] = sx * __uint_as_float(w << 16);
            sc[e >> 1][(e & 1) * 2 + 1] = sx * __uint_as_float(w & 0xffff0000u);
            bi[e >> 1][(e & 1) * 2] = __uint_as_float(c << 16);
            bi[e >> 1][(e & 1) * 2 + 1] = __uint_as_float(c & 0xffff0000u);
        }
        if (lane < 28) asm volatile("ds_write_b128 %0, %1\n\tds_write_b128 %0, %2 offset:16" ::"v"(smem_base + OFF_SCALE + lane * 32), "v"(sc[0]), "v"(sc[1]) : "memory");
        if (SGATE) SPIN {
            const u32x4 v = lds_rd128(ctl + CT_ARR);
            if (__builtin_amdgcn_readfirstlane(v[2])) break;
            __builtin_amdgcn_s_sleep(1);
        }
        GAS u64* fa = (GAS u64*)(wr + WS_FLAGS + (tm * NCA + tn / (16 / NCA)) * FL);
        GAS u64* fb = (GAS u64*)(wr + WS_FLAGS + (16 * NCA + tn * NCB + tm / (16 / NCB)) * FL);
        typedef u64 u64x8 __attribute__((ext_vector_type(8)));
#define FD8(D, F)                                                                                                                       \
    do {                                                                                                                                \
        u64 t_;                                                                                                                         \
        const u64 a0_ = F[0], a1_ = F[1], a2_ = F[2], a3_ = F[3], a4_ = F[4], a5_ = F[5], a6_ = F[6], a7_ = F[7];                       \
        asm("s_xor_b64 %1, %3, %2\n\ts_or_b64 %0, %0, %1\n\ts_xor_b64 %1, %4, %2\n\ts_or_b64 %0, %0, %1\n\t"                            \
            "s_xor_b64 %1, %5, %2\n\ts_or_b64 %0, %0, %1\n\ts_xor_b64 %1, %6, %2\n\ts_or_b64 %0, %0, %1\n\t"                            \
            "s_xor_b64 %1, %7, %2\n\ts_or_b64 %0, %0, %1\n\ts_xor_b64 %1, %8, %2\n\ts_or_b64 %0, %0, %1\n\t"                            \
            "s_xor_b64 %1, %9, %2\n\ts_or_b64 %0, %0, %1\n\ts_xor_b64 %1, %10, %2\n\ts_or_b64 %0, %0, %1"                               \
            : "+s"(D), "=&s"(t_) : "s"(tag), "s"(a0_), "s"(a1_), "s"(a2_), "s"(a3_), "s"(a4_), "s"(a5_), "s"(a6_), "s"(a7_) : "scc");     \
    } while (0)
#pragma unroll
        for (int q = 0; q < 4; ++q) {
            int it = 0;
            bool half = false;
            SPIN {
                if ((++it & 63) == 0) {
                    u64 x;
                    asm volatile("global_load_dwordx2 %0, %1, off sc1\n\ts_waitcnt vmcnt(0)" : "=v"(x) : "v"(((lane & 16) ? fb : fa) + q * (CPH / 8) + (lane & 15)) : "memory");
                    if (__all(x == tag)) break;
                } else {
                    u64x8 f0, f1, f2, f3;
                    asm volatile("s_load_dwordx16 %0, %4, %6 glc\n\ts_load_dwordx16 %1, %5, %6 glc\n\ts_load_dwordx16 %2, %4, %7 glc\n\ts_load_dwordx16 %3, %5, %7 glc\n\ts_waitcnt lgkmcnt(0)"
                                 : "=&s"(f0), "=&s"(f1), "=&s"(f2), "=&s"(f3)
                                 : "s"(fa), "s"(fb), "n"(q * CPH), "n"(q * CPH + 64) : "memory");
                    u64 dl = 0, dh = 0;
                    FD8(dl, f0);
                    FD8(dl, f1);
                    if (dl == 0) {
                        FD8(dh, f2);
                        FD8(dh, f3);
                        if (dh == 0) break;
                        if (!half) {
                            if (RSIG && q) SIG(ctl + CT_SIG, 2 * q + 1);
                            else lds_wr32(ctl + CT_SIG, 2 * q + 1);
                            half = true;
                        }
                    }
                }
            }
            if (RSIG && q) SIG(ctl + CT_SIG, 2 * q + 2);
            else lds_wr32(ctl + CT_SIG, 2 * q + 2);
        }
        const __amdgpu_buffer_rsrc_t rsC = __builtin_amdgcn_make_buffer_rsrc((void*)(wr + WS_CP), 0, 16 * 16 * 512, RSRC_W3);
        typedef short i16x8 __attribute__((ext_vector_type(8)));
        i16x8 cp[8];
#pragma unroll
        for (int x = 0; x < 8; ++x)
            cp[x] = __builtin_bit_cast(i16x8, __builtin_amdgcn_raw_buffer_load_b128(rsC, lane * 16, tn * 8192 + x * 1024, AUX_SC1));
        const i16x8 u0 = (cp[0] + cp[1]) + (cp[2] + cp[3]), u1 = (cp[4] + cp[5]) + (cp[6] + cp[7]);
#pragma unroll
        for (int e = 0; e < 8; ++e) {
            const short x0 = u0[e], x1 = u1[e];
            const unsigned t = (unsigned)((int)x0 + (int)x1);
            const u32x2 sw = __builtin_amdgcn_permlane32_swap(t, t, false, false);
            const unsigned lo = sw[0], hi = sw[1];
            const float cf = nzx * (float)(int)(lo + hi);
            bi[e >> 2][e & 3] = __builtin_fmaf(cf, sc[e >> 2][e & 3], bi[e >> 2][e & 3]);
        }
        if (lane < 28)
            asm volatile("ds_write_b128 %0, %1\n\tds_write_b128 %0, %2 offset:16\n\tds_write_b32 %3, %4" ::"v"(smem_base + OFF_BIAS + lane * 32),
                         "v"(bi[0]), "v"(bi[1]), "v"(ctl + CT_FULL + 8), "v"(1u) : "memory");
    } else if (wid == 4) {
        if (bid == 0) {
            SPIN {
                const u32x4 v = lds_rd128(ctl + CT_FULL);
                if (__builtin_amdgcn_readfirstlane(min(v[0], v[1])) >= 16) break;
                __builtin_amdgcn_s_sleep(8);
            }
            SPIN {
                bool ok = true;
#pragma unroll
                for (int i = 0; i < 4; ++i)
                    ok &= __hip_atomic_load((GAS u64*)(wr + WS_FLAGS + 3 * CPH + (i * 4 + (lane >> 4)) * NCA * FL) + (lane & 15), __ATOMIC_RELAXED, __HIP_MEMORY_SCOPE_AGENT) == tag;
                if (__all(ok)) break;
                __builtin_amdgcn_s_sleep(8);
            }
            if (lane == 0) __hip_atomic_store((GAS u64*)(ws + WS_EPOCH), tag, __ATOMIC_RELAXED, __HIP_MEMORY_SCOPE_AGENT);
        }
    } else {
        const int wm = wid >> 1, wn = wid & 1;
        const int qG4 = wn * 32 + (tn & 1) * 8, qG2 = wn * 32 + 16 - (tn & 1) * 16;
        i32x4 acc[4][6], acm[2][2];
        i32x4 af[2][4], bfr[2][8];
#pragma unroll
        for (int i = 0; i < 4; ++i)
#pragma unroll
            for (int j = 0; j < 6; ++j) {
                acc[i][j] = (i32x4){0, 0, 0, 0};
                asm volatile("" : "+v"(acc[i][j]));
            }
#pragma unroll
        for (int i = 0; i < 2; ++i)
#pragma unroll
            for (int j = 0; j < 2; ++j) {
                acm[i][j] = (i32x4){0, 0, 0, 0};
                asm volatile("" : "+v"(acm[i][j]));
            }
        constexpr int NU = (NSTAGE + 2) / 3;
        unsigned aA0[NU], aA2[NU], aG4[NU], aG2[NU], aM[NU];
        {
            const unsigned fA = smem_base + wm * 4096 + (ATR ? lane * 16 : (lane & 15) * 64 + ((lane >> 4) ^ ((lane >> 2) & 2)) * 16);
            const unsigned fB = smem_base + A_STAGE + (lane >> 4) * UNIT, f2 = fB + (lane & 1) * 896 + ((lane >> 1) & 7) * 16;
#pragma unroll
            for (int u = 0; u < NU; ++u) {
                aA0[u] = fA + wn * 2048 + u * 3 * STAGE_BYTES;
                aA2[u] = fA + (wn ^ 1) * 2048 + u * 3 * STAGE_BYTES;
                aG4[u] = fB + (qG4 + (lane & 15)) * 16 + u * 3 * STAGE_BYTES;
                aG2[u] = f2 + qG2 * 16 + u * 3 * STAGE_BYTES;
                aM[u] = f2 + 384 + u * 3 * STAGE_BYTES;
                asm volatile("" : "+v"(aA0[u]), "+v"(aA2[u]), "+v"(aG4[u]), "+v"(aG2[u]), "+v"(aM[u]));
            }
        }
#define FSIG(V) do { if ((FMASK >> (V)) & 1) asm volatile("buffer_load_dword off, %0, %1 lds" ::"s"(rsT), "n"((V) * 4) : "memory"); } while (0)
        unsigned fmin;
        unsigned known = 0;
#define DSR(DST, ADDR, OFF) asm volatile("ds_read_b128 %0, %1 offset:%2" : "=v"(DST) : "v"(ADDR), "n"(OFF) : "memory")
#define MF(I, J, SET) asm volatile("v_mfma_i32_16x16x64_i8 %0, %1, %2, %0" : "+v"(acc[I][J]) : "v"(af[SET][I]), "v"(bfr[SET][J]))
#define MM(I, J, SET) asm volatile("v_mfma_i32_16x16x64_i8 %0, %1, %2, %0" : "+v"(acm[I][J]) : "v"(af[SET][I]), "v"(bfr[SET][6 + (J)]))
#define NEED(X)                                                                                                \
    do {                                                                                                       \
        if (__builtin_expect(known <= (unsigned)(X), 0)) {                                                     \
            SPIN {                                                                                             \
                const u32x4 v_ = lds_rd128(ctl + CT_FULL);                                                     \
                known = __builtin_amdgcn_readfirstlane(min(v_[0], v_[1]));                                     \
                if (known > (unsigned)(X)) break;                                                              \
                __builtin_amdgcn_s_sleep(1);                                                                   \
            }                                                                                                  \
        }                                                                                                      \
    } while (0)
#define SU(KB) (((KB) % NSTAGE) / 3)
#define SO(KB) ((((KB) % NSTAGE) % 3) * STAGE_BYTES)
#define RA(SET, KB, I) DSR(af[SET][I], (I) < 2 ? aA0[SU(KB)] : aA2[SU(KB)], SO(KB) + ((I) & 1) * 1024)
#define RB(SET, KB, J) DSR(bfr[SET][J], (J) >= 6 ? aM[SU(KB)] : (J) < 2 ? aG2[SU(KB)] : aG4[SU(KB)], SO(KB) + ((J) & 1) * 1792 + ((J) >> 1 == 2 ? 896 : 0))
#define STEP(SET, KB)                                                                        \
    do {                                                                                     \
        MF(0, 0, SET);                                                                       \
        if ((KB) + 1 < 16) {                                                                 \
            NEED((KB) + 1);                                                                  \
            __builtin_amdgcn_sched_barrier(0);                                               \
            MF(1, 0, SET);                                                                   \
            RB((SET) ^ 1, (KB) + 1, 0);   MF(2, 0, SET);                                     \
            RA((SET) ^ 1, (KB) + 1, 0);   MF(3, 0, SET);                                     \
            RA((SET) ^ 1, (KB) + 1, 1);   MF(0, 1, SET);                                     \
            RA((SET) ^ 1, (KB) + 1, 2);   MF(1, 1, SET);                                     \
            RA((SET) ^ 1, (KB) + 1, 3);   MF(2, 1, SET);                                     \
            RB((SET) ^ 1, (KB) + 1, 1);   MF(3, 1, SET);                                     \
            RB((SET) ^ 1, (KB) + 1, 2);   MF(0, 2, SET);                                     \
            RB((SET) ^ 1, (KB) + 1, 3);   MF(1, 2, SET);                                     \
            RB((SET) ^ 1, (KB) + 1, 4);   MF(2, 2, SET);                                     \
            RB((SET) ^ 1, (KB) + 1, 5);   MF(3, 2, SET);                                     \
            RB((SET) ^ 1, (KB) + 1, 6);   MF(0, 3, SET);                                     \
            RB((SET) ^ 1, (KB) + 1, 7);                                                      \
            MF(1, 3, SET); MF(2, 3, SET); MF(3, 3, SET);                                     \
            MF(0, 4, SET); MF(1, 4, SET);                                                    \
            if ((KB) + 2 < 16) asm volatile("ds_read_b64 %0, %1" : "=v"(fv) : "v"(fullv) : "memory"); \
            MF(2, 4, SET); MF(3, 4, SET);                                                    \
            MF(0, 5, SET); MF(1, 5, SET); MF(2, 5, SET); MF(3, 5, SET);                      \
            MM(0, 0, SET);                                                                   \
            asm volatile("s_waitcnt lgkmcnt(0)" : "+v"(fv)::"memory");                       \
            if ((KB) + 1 + NSTAGE < 16) FSIG((KB) + 2);                                      \
            MM(1, 0, SET);                                                                   \
            { const unsigned f0_ = fv[0], f1_ = fv[1]; asm volatile("v_min_u32 %0, %1, %2" : "=v"(fmin) : "v"(f0_), "v"(f1_)); } \
            MM(0, 1, SET);                                                                   \
            asm volatile("v_readfirstlane_b32 %0, %1" : "=s"(known) : "v"(fmin));            \
            MM(1, 1, SET);                                                                   \
        } else {                                                                             \
            MF(1, 0, SET); MF(2, 0, SET); MF(3, 0, SET);                                     \
            _Pragma("unroll") for (int j = 1; j < 6; ++j)                                    \
            _Pragma("unroll") for (int i = 0; i < 4; ++i) MF(i, j, SET);                     \
            MM(0, 0, SET); MM(1, 0, SET); MM(0, 1, SET); MM(1, 1, SET);                      \
        }                                                                                    \
        __builtin_amdgcn_sched_barrier(0);                                                   \
    } while (0)
        u32x2 fv = {0, 0};
        unsigned fullv = ctl + CT_FULL;
        asm volatile("" : "+v"(fullv));
        u64 outp = (u64)out;
        asm volatile("" : "+s"(outp));
        const __amdgpu_buffer_rsrc_t rsO = __builtin_amdgcn_make_buffer_rsrc((void*)outp, 0, M_ * N_ * 2, RSRC_W3);
        unsigned vG4, vG2, vM, cad4, cad0, cad3, crdy = 0;
        int so[4][4];
        typedef unsigned u32x3 __attribute__((ext_vector_type(3)));
        u32x3 fq;
        {
            const unsigned ovo = (unsigned)(((tm * BM + wm * 64 + (lane >> 4) * 4) * N_ + tn * BN) * 2), l = lane & 15;
            vG4 = ovo + (qG4 + l) * 8;
            vG2 = ovo + qG2 * 8 + l * 4;
            vM = ovo + 192 + l * 4;
            cad4 = smem_base + OFF_SCALE + (qG4 + l) * 16;
            cad0 = smem_base + OFF_SCALE + qG2 * 16 + l * 8;
            cad3 = smem_base + OFF_SCALE + 384 + l * 8;
            asm volatile("" : "+v"(vG4), "+v"(vG2), "+v"(vM), "+v"(cad4), "+v"(cad0), "+v"(cad3));
#pragma unroll
            for (int i = 0; i < 4; ++i)
#pragma unroll
                for (int r = 0; r < 4; ++r) {
                    so[i][r] = (((i ^ (wn * 2)) * 16 + r) * N_) * 2;
                    asm volatile("" : "+s"(so[i][r]));
                }
        }
#define SB() __builtin_amdgcn_sched_barrier(0)
#define MFX(ACC, B, A) asm volatile("v_mfma_i32_16x16x64_i8 %0, %1, %2, %0" : "+v"(ACC) : "v"(A), "v"(B))
#define CVF(F, X) do { SB(); F = (float)(X); SB(); } while (0)
#define FMA(R, F, S, B) do { const float s_ = S, b_ = B; asm volatile("v_fma_f32 %0, %1, %2, %3" : "=v"(R) : "v"(F), "v"(s_), "v"(b_)); } while (0)
#define CPK(O, R0, R1) do { SB(); const f32x2 v_ = {R0, R1}; O = __builtin_bit_cast(unsigned, __builtin_convertvector(v_, bf16x2_t)); SB(); } while (0)
#define DSR2(DST, ADDR, OFF) asm volatile("ds_read_b64 %0, %1 offset:%2" : "=v"(DST) : "v"(ADDR), "n"(OFF) : "memory")
#define ST1(X, ADDR, I, R) do { SB(); __builtin_amdgcn_raw_buffer_store_b32(X, rsO, ADDR, so[I][R], OUT_AUX); SB(); } while (0)
#define ST2(X, Y, I, R) do { SB(); __builtin_amdgcn_raw_buffer_store_b64((u32x2){X, Y}, rsO, vG4, so[I][R], OUT_AUX); SB(); } while (0)
#define CORRW()                                                                                                \
    do {                                                                                                       \
        if (__builtin_expect(!crdy, 0)) {                                                                      \
            SPIN {                                                                                             \
                const u32x4 v_ = lds_rd128(ctl + CT_FULL);                                                     \
                if (__builtin_amdgcn_readfirstlane(v_[2])) break;                                              \
                __builtin_amdgcn_s_sleep(1);                                                                   \
            }                                                                                                  \
        }                                                                                                      \
    } while (0)
        asm volatile("s_mov_b32 m0, %1" ::"v"(pfd), "s"(smem_base + OFF_FREE + wid * 256) : "memory");
        if (CPRIO) __builtin_amdgcn_s_setprio(CPRIO);
        NEED(0);
        RB(0, 0, 0); RA(0, 0, 0); RA(0, 0, 1); RA(0, 0, 2); RA(0, 0, 3);
        RB(0, 0, 1); RB(0, 0, 2); RB(0, 0, 3); RB(0, 0, 4); RB(0, 0, 5); RB(0, 0, 6); RB(0, 0, 7);
        asm volatile("s_waitcnt lgkmcnt(0)" ::: "memory");
        FSIG(1);
        __builtin_amdgcn_sched_barrier(0);
        STEP(0, 0);
        STEP(1, 1);
        STEP(0, 2);
        STEP(1, 3);
        STEP(0, 4);
        STEP(1, 5);
        STEP(0, 6);
        STEP(1, 7);
        STEP(0, 8);
        STEP(1, 9);
        STEP(0, 10);
        i32x4 ta0_12, ta0_13, ta0_14, ta0_15, ta1_12, ta1_13, ta1_14, ta1_15, ta2_12, ta2_13, ta2_14, ta2_15, ta3_12, ta3_13, ta3_14, ta3_15, tb00_12, tb00_13, tb00_14, tb00_15, tb01_12, tb01_13, tb01_14, tb01_15, tb10_12, tb10_13, tb10_14, tb10_15, tb11_12, tb11_13, tb11_14, tb11_15, tb20_12, tb20_13, tb20_14, tb20_15, tb21_12, tb21_13, tb21_14, tb21_15, tb30_12, tb30_13, tb30_14, tb30_15, tb31_12, tb31_13, tb31_14, tb31_15;
        f32x4 ks4, kb4;
        f32x2 ks0, kb0, ks3, kb3;
        float f30_00, f30_10, f30_20, f30_30, f30_01, f30_11, f30_21, f30_31, f31_00, f31_10, f31_20, f31_30, f31_01, f31_11, f31_21, f31_31, f00_00, f00_10, f00_20, f00_30, f00_01, f00_11, f00_21, f00_31, f01_00, f01_10, f01_20, f01_30, f01_01, f01_11, f01_21, f01_31, f02_00, f02_10, f02_20, f02_30, f02_01, f02_11, f02_21, f02_31, f03_00, f03_10, f03_20, f03_30, f03_01, f03_11, f03_21, f03_31, f10_00, f10_10, f10_20, f10_30, f10_01, f10_11, f10_21, f10_31, f20_00, f20_10, f20_20, f20_30, f20_01, f20_11, f20_21, f20_31, f11_00, f11_10, f11_20, f11_30, f11_01, f11_11, f11_21, f11_31, f21_00, f21_10, f21_20, f21_30, f21_01, f21_11, f21_21, f21_31, f12_00, f12_10, f12_20, f12_30, f12_01, f12_11, f12_21, f12_31, f22_00, f22_10, f22_20, f22_30, f22_01, f22_11, f22_21, f22_31, f13_00, f13_10, f13_20, f13_30, f13_01, f13_11, f13_21, f13_31, f23_00, f23_10, f23_20, f23_30, f23_01, f23_11, f23_21, f23_31;
        float r30_00, r30_10, r30_20, r30_30, r30_01, r30_11, r30_21, r30_31, r31_00, r31_10, r31_20, r31_30, r31_01, r31_11, r31_21, r31_31, r00_00, r00_10, r00_20, r00_30, r00_01, r00_11, r00_21, r00_31, r01_00, r01_10, r01_20, r01_30, r01_01, r01_11, r01_21, r01_31, r02_00, r02_10, r02_20, r02_30, r02_01, r02_11, r02_21, r02_31, r03_00, r03_10, r03_20, r03_30, r03_01, r03_11, r03_21, r03_31, r10_00, r10_10, r10_20, r10_30, r10_01, r10_11, r10_21, r10_31, r20_00, r20_10, r20_20, r20_30, r20_01, r20_11, r20_21, r20_31, r11_00, r11_10, r11_20, r11_30, r11_01, r11_11, r11_21, r11_31, r21_00, r21_10, r21_20, r21_30, r21_01, r21_11, r21_21, r21_31, r12_00, r12_10, r12_20, r12_30, r12_01, r12_11, r12_21, r12_31, r22_00, r22_10, r22_20, r22_30, r22_01, r22_11, r22_21, r22_31, r13_00, r13_10, r13_20, r13_30, r13_01, r13_11, r13_21, r13_31, r23_00, r23_10, r23_20, r23_30, r23_01, r23_11, r23_21, r23_31;
        unsigned o30_0, o30_1, o30_2, o30_3, o31_0, o31_1, o31_2, o31_3, o00_0, o00_1, o00_2, o00_3, o01_0, o01_1, o01_2, o01_3, o02_0, o02_1, o02_2, o02_3, o03_0, o03_1, o03_2, o03_3, o10_0, o10_1, o10_2, o10_3, o20_0, o20_1, o20_2, o20_3, o11_0, o11_1, o11_2, o11_3, o21_0, o21_1, o21_2, o21_3, o12_0, o12_1, o12_2, o12_3, o22_0, o22_1, o22_2, o22_3, o13_0, o13_1, o13_2, o13_3, o23_0, o23_1, o23_2, o23_3;
        MF(0, 0, 1);
        MF(1, 0, 1);
        MF(2, 0, 1);
        MF(3, 0, 1);
        MF(0, 1, 1);
        MF(1, 1, 1);
        MF(2, 1, 1);
        MF(3, 1, 1);
        MF(0, 2, 1);
        MF(1, 2, 1);
        MF(2, 2, 1);
        MF(3, 2, 1);
        MF(0, 3, 1);
        MF(1, 3, 1);
        MF(2, 3, 1);
        asm volatile("ds_read_b96 %0, %1" : "=v"(fq) : "v"(fullv) : "memory");
        MF(3, 3, 1);
        MF(0, 4, 1);
        MF(1, 4, 1);
        MF(2, 4, 1);
        MF(3, 4, 1);
        asm volatile("s_waitcnt lgkmcnt(0)" : "+v"(fq)::"memory");
        known = max(known, (unsigned)__builtin_amdgcn_readfirstlane(min(fq[0], fq[1])));
        NEED(12);
        SB();
        asm volatile("ds_read_b96 %0, %1" : "=v"(fq) : "v"(fullv) : "memory");
        DSR(ta0_12, aA0[SU(12)], SO(12) + 0);
        DSR(tb30_12, aM[SU(12)], SO(12) + 0);
        MF(0, 5, 1);
        DSR(ta1_12, aA0[SU(12)], SO(12) + 1024);
        MF(1, 5, 1);
        DSR(tb31_12, aM[SU(12)], SO(12) + 1792);
        MF(2, 5, 1);
        MF(3, 5, 1);
        asm volatile("s_waitcnt lgkmcnt(4)" : "+v"(fq)::"memory");
        known = max(known, (unsigned)__builtin_amdgcn_readfirstlane(min(fq[0], fq[1])));
        NEED(13);
        SB();
        asm volatile("ds_read_b96 %0, %1" : "=v"(fq) : "v"(fullv) : "memory");
        DSR(ta0_13, aA0[SU(13)], SO(13) + 0);
        DSR(tb30_13, aM[SU(13)], SO(13) + 0);
        MM(0, 0, 1);
        DSR(ta1_13, aA0[SU(13)], SO(13) + 1024);
        MM(1, 0, 1);
        DSR(tb31_13, aM[SU(13)], SO(13) + 1792);
        MM(0, 1, 1);
        MM(1, 1, 1);
        asm volatile("s_waitcnt lgkmcnt(4)" : "+v"(fq)::"memory");
        known = max(known, (unsigned)__builtin_amdgcn_readfirstlane(min(fq[0], fq[1])));
        NEED(14);
        SB();
        asm volatile("ds_read_b96 %0, %1" : "=v"(fq) : "v"(fullv) : "memory");
        DSR(ta0_14, aA0[SU(14)], SO(14) + 0);
        DSR(tb30_14, aM[SU(14)], SO(14) + 0);
        MFX(acm[0][0], tb30_12, ta0_12);
        DSR(ta1_14, aA0[SU(14)], SO(14) + 1024);
        MFX(acm[1][0], tb30_12, ta1_12);
        DSR(tb31_14, aM[SU(14)], SO(14) + 1792);
        MFX(acm[0][1], tb31_12, ta0_12);
        MFX(acm[1][1], tb31_12, ta1_12);
        asm volatile("s_waitcnt lgkmcnt(4)" : "+v"(fq)::"memory");
        known = max(known, (unsigned)__builtin_amdgcn_readfirstlane(min(fq[0], fq[1])));
        NEED(15);
        SB();
        asm volatile("ds_read_b96 %0, %1" : "=v"(fq) : "v"(fullv) : "memory");
        DSR(ta0_15, aA0[SU(15)], SO(15) + 0);
        DSR(tb30_15, aM[SU(15)], SO(15) + 0);
        MFX(acm[0][0], tb30_13, ta0_13);
        DSR(ta1_15, aA0[SU(15)], SO(15) + 1024);
        MFX(acm[1][0], tb30_13, ta1_13);
        DSR(tb31_15, aM[SU(15)], SO(15) + 1792);
        MFX(acm[0][1], tb31_13, ta0_13);
        MFX(acm[1][1], tb31_13, ta1_13);
        asm volatile("s_waitcnt lgkmcnt(2)" ::: "memory");
        DSR(tb00_12, aG2[SU(12)], SO(12) + 0);
        MFX(acm[0][0], tb30_14, ta0_14);
        asm volatile("s_waitcnt lgkmcnt(3)" : "+v"(fq)::"memory");
        known = max(known, (unsigned)__builtin_amdgcn_readfirstlane(min(fq[0], fq[1]))); crdy = __builtin_amdgcn_readfirstlane(fq[2]);
        CORRW();
        SB();
        DSR2(ks3, cad3, 0);
        DSR(tb01_12, aG2[SU(12)], SO(12) + 1792);
        MFX(acm[1][0], tb30_14, ta1_14);
        DSR2(kb3, cad3, 896);
        DSR(tb00_13, aG2[SU(13)], SO(13) + 0);
        MFX(acm[0][1], tb31_14, ta0_14);
        DSR(tb01_13, aG2[SU(13)], SO(13) + 1792);
        MFX(acm[1][1], tb31_14, ta1_14);
        DSR(tb00_14, aG2[SU(14)], SO(14) + 0);
        MFX(acm[0][0], tb30_15, ta0_15);
        asm volatile("s_waitcnt lgkmcnt(4)" ::: "memory");
        DSR(tb01_14, aG2[SU(14)], SO(14) + 1792);
        MFX(acm[1][0], tb30_15, ta1_15);
        DSR(tb00_15, aG2[SU(15)], SO(15) + 0);
        MFX(acm[0][1], tb31_15, ta0_15);
        DSR(tb01_15, aG2[SU(15)], SO(15) + 1792);
        MFX(acm[1][1], tb31_15, ta1_15);
        CVF(f30_00, acm[0][0][0]);
        CVF(f30_10, acm[0][0][1]);
        CVF(f30_20, acm[0][0][2]);
        CVF(f30_30, acm[0][0][3]);
        MFX(acc[0][0], tb00_12, ta0_12);
        asm volatile("s_waitcnt lgkmcnt(2)" ::: "memory");
        FMA(r30_00, f30_00, ks3[0], kb3[0]);
        MFX(acc[0][1], tb01_12, ta0_12);
        CVF(f30_01, acm[0][1][0]);
        FMA(r30_10, f30_10, ks3[0], kb3[0]);
        CVF(f30_11, acm[0][1][1]);
        FMA(r30_20, f30_20, ks3[0], kb3[0]);
        MFX(acc[0][0], tb00_13, ta0_13);
        CVF(f30_21, acm[0][1][2]);
        FMA(r30_30, f30_30, ks3[0], kb3[0]);
        CVF(f30_31, acm[0][1][3]);
        FMA(r30_01, f30_01, ks3[1], kb3[1]);
        MFX(acc[0][1], tb01_13, ta0_13);
        DSR2(ks0, cad0, 0);
        FMA(r30_11, f30_11, ks3[1], kb3[1]);
        FMA(r30_21, f30_21, ks3[1], kb3[1]);
        MFX(acc[0][0], tb00_14, ta0_14);
        DSR2(kb0, cad0, 896);
        FMA(r30_31, f30_31, ks3[1], kb3[1]);
        MFX(acc[0][1], tb01_14, ta0_14);
        asm volatile("s_waitcnt lgkmcnt(2)" ::: "memory");
        DSR(ks4, cad4, 0);
        CPK(o30_0, r30_00, r30_01);
        MFX(acc[0][0], tb00_15, ta0_15);
        DSR(kb4, cad4, 896);
        ST1(o30_0, vM, 0, 0);
        CPK(o30_1, r30_10, r30_11);
        MFX(acc[0][1], tb01_15, ta0_15);
        DSR(ta2_12, aA2[SU(12)], SO(12) + 0);
        ST1(o30_1, vM, 0, 1);
        CPK(o30_2, r30_20, r30_21);
        MFX(acc[1][0], tb00_12, ta1_12);
        CPK(o30_3, r30_30, r30_31);
        CVF(f31_00, acm[1][0][0]);
        CVF(f31_10, acm[1][0][1]);
        MFX(acc[1][1], tb01_12, ta1_12);
        DSR(ta2_13, aA2[SU(13)], SO(13) + 0);
        ST1(o30_2, vM, 0, 2);
        ST1(o30_3, vM, 0, 3);
        CVF(f31_20, acm[1][0][2]);
        CVF(f31_30, acm[1][0][3]);
        MFX(acc[1][0], tb00_13, ta1_13);
        FMA(r31_00, f31_00, ks3[0], kb3[0]);
        CVF(f31_01, acm[1][1][0]);
        FMA(r31_10, f31_10, ks3[0], kb3[0]);
        CVF(f31_11, acm[1][1][1]);
        MFX(acc[1][1], tb01_13, ta1_13);
        DSR(ta2_14, aA2[SU(14)], SO(14) + 0);
        FMA(r31_20, f31_20, ks3[0], kb3[0]);
        CVF(f31_21, acm[1][1][2]);
        MFX(acc[1][0], tb00_14, ta1_14);
        FMA(r31_30, f31_30, ks3[0], kb3[0]);
        CVF(f31_31, acm[1][1][3]);
        FMA(r31_01, f31_01, ks3[1], kb3[1]);
        FMA(r31_11, f31_11, ks3[1], kb3[1]);
        MFX(acc[1][1], tb01_14, ta1_14);
        DSR(ta2_15, aA2[SU(15)], SO(15) + 0);
        FMA(r31_21, f31_21, ks3[1], kb3[1]);
        FMA(r31_31, f31_31, ks3[1], kb3[1]);
        MFX(acc[1][0], tb00_15, ta1_15);
        CPK(o31_0, r31_00, r31_01);
        CPK(o31_1, r31_10, r31_11);
        MFX(acc[1][1], tb01_15, ta1_15);
        asm volatile("s_waitcnt lgkmcnt(1)" ::: "memory");
        DSR(ta3_12, aA2[SU(12)], SO(12) + 1024);
        ST1(o31_0, vM, 1, 0);
        ST1(o31_1, vM, 1, 1);
        CPK(o31_2, r31_20, r31_21);
        MFX(acc[2][0], tb00_12, ta2_12);
        CPK(o31_3, r31_30, r31_31);
        CVF(f00_00, acc[0][0][0]);
        CVF(f00_10, acc[0][0][1]);
        MFX(acc[2][1], tb01_12, ta2_12);
        DSR(ta3_13, aA2[SU(13)], SO(13) + 1024);
        ST1(o31_2, vM, 1, 2);
        ST1(o31_3, vM, 1, 3);
        CVF(f00_20, acc[0][0][2]);
        CVF(f00_30, acc[0][0][3]);
        MFX(acc[2][0], tb00_13, ta2_13);
        FMA(r00_00, f00_00, ks0[0], kb0[0]);
        CVF(f00_01, acc[0][1][0]);
        FMA(r00_10, f00_10, ks0[0], kb0[0]);
        CVF(f00_11, acc[0][1][1]);
        MFX(acc[2][1], tb01_13, ta2_13);
        DSR(ta3_14, aA2[SU(14)], SO(14) + 1024);
        FMA(r00_20, f00_20, ks0[0], kb0[0]);
        CVF(f00_21, acc[0][1][2]);
        MFX(acc[2][0], tb00_14, ta2_14);
        FMA(r00_30, f00_30, ks0[0], kb0[0]);
        CVF(f00_31, acc[0][1][3]);
        FMA(r00_01, f00_01, ks0[1], kb0[1]);
        FMA(r00_11, f00_11, ks0[1], kb0[1]);
        MFX(acc[2][1], tb01_14, ta2_14);
        asm volatile("s_waitcnt lgkmcnt(1)" ::: "memory");
        DSR(ta3_15, aA2[SU(15)], SO(15) + 1024);
        FMA(r00_21, f00_21, ks0[1], kb0[1]);
        FMA(r00_31, f00_31, ks0[1], kb0[1]);
        MFX(acc[2][0], tb00_15, ta2_15);
        CPK(o00_0, r00_00, r00_01);
        CPK(o00_1, r00_10, r00_11);
        MFX(acc[2][1], tb01_15, ta2_15);
        DSR(tb10_12, aG4[SU(12)], SO(12) + 0);
        ST1(o00_0, vG2, 0, 0);
        ST1(o00_1, vG2, 0, 1);
        CPK(o00_2, r00_20, r00_21);
        MFX(acc[3][0], tb00_12, ta3_12);
        DSR(tb11_12, aG4[SU(12)], SO(12) + 1792);
        ST1(o00_2, vG2, 0, 2);
        CPK(o00_3, r00_30, r00_31);
        MFX(acc[3][1], tb01_12, ta3_12);
        DSR(tb10_13, aG4[SU(13)], SO(13) + 0);
        ST1(o00_3, vG2, 0, 3);
        CVF(f01_00, acc[1][0][0]);
        CVF(f01_10, acc[1][0][1]);
        MFX(acc[3][0], tb00_13, ta3_13);
        DSR(tb11_13, aG4[SU(13)], SO(13) + 1792);
        CVF(f01_20, acc[1][0][2]);
        CVF(f01_30, acc[1][0][3]);
        MFX(acc[3][1], tb01_13, ta3_13);
        asm volatile("s_waitcnt lgkmcnt(3)" ::: "memory");
        DSR(tb10_14, aG4[SU(14)], SO(14) + 0);
        FMA(r01_00, f01_00, ks0[0], kb0[0]);
        CVF(f01_01, acc[1][1][0]);
        MFX(acc[3][0], tb00_14, ta3_14);
        DSR(tb11_14, aG4[SU(14)], SO(14) + 1792);
        FMA(r01_10, f01_10, ks0[0], kb0[0]);
        CVF(f01_11, acc[1][1][1]);
        MFX(acc[3][1], tb01_14, ta3_14);
        DSR(tb10_15, aG4[SU(15)], SO(15) + 0);
        FMA(r01_20, f01_20, ks0[0], kb0[0]);
        CVF(f01_21, acc[1][1][2]);
        MFX(acc[3][0], tb00_15, ta3_15);
        DSR(tb11_15, aG4[SU(15)], SO(15) + 1792);
        FMA(r01_30, f01_30, ks0[0], kb0[0]);
        CVF(f01_31, acc[1][1][3]);
        MFX(acc[3][1], tb01_15, ta3_15);
        DSR(tb20_12, aG4[SU(12)], SO(12) + 896);
        FMA(r01_01, f01_01, ks0[1], kb0[1]);
        FMA(r01_11, f01_11, ks0[1], kb0[1]);
        MFX(acc[0][2], tb10_12, ta0_12);
        asm volatile("s_waitcnt lgkmcnt(3)" ::: "memory");
        DSR(tb21_12, aG4[SU(12)], SO(12) + 2688);
        FMA(r01_21, f01_21, ks0[1], kb0[1]);
        FMA(r01_31, f01_31, ks0[1], kb0[1]);
        MFX(acc[0][3], tb11_12, ta0_12);
        DSR(tb20_13, aG4[SU(13)], SO(13) + 896);
        CPK(o01_0, r01_00, r01_01);
        MFX(acc[0][2], tb10_13, ta0_13);
        DSR(tb21_13, aG4[SU(13)], SO(13) + 2688);
        ST1(o01_0, vG2, 1, 0);
        CPK(o01_1, r01_10, r01_11);
        MFX(acc[0][3], tb11_13, ta0_13);
        DSR(tb20_14, aG4[SU(14)], SO(14) + 896);
        ST1(o01_1, vG2, 1, 1);
        CPK(o01_2, r01_20, r01_21);
        MFX(acc[0][2], tb10_14, ta0_14);
        DSR(tb21_14, aG4[SU(14)], SO(14) + 2688);
        ST1(o01_2, vG2, 1, 2);
        CPK(o01_3, r01_30, r01_31);
        MFX(acc[0][3], tb11_14, ta0_14);
        asm volatile("s_waitcnt lgkmcnt(3)" ::: "memory");
        DSR(tb20_15, aG4[SU(15)], SO(15) + 896);
        ST1(o01_3, vG2, 1, 3);
        CVF(f02_00, acc[2][0][0]);
        CVF(f02_10, acc[2][0][1]);
        MFX(acc[0][2], tb10_15, ta0_15);
        DSR(tb21_15, aG4[SU(15)], SO(15) + 2688);
        CVF(f02_20, acc[2][0][2]);
        CVF(f02_30, acc[2][0][3]);
        MFX(acc[0][3], tb11_15, ta0_15);
        FMA(r02_00, f02_00, ks0[0], kb0[0]);
        CVF(f02_01, acc[2][1][0]);
        FMA(r02_10, f02_10, ks0[0], kb0[0]);
        CVF(f02_11, acc[2][1][1]);
        MFX(acc[0][4], tb20_12, ta0_12);
        FMA(r02_20, f02_20, ks0[0], kb0[0]);
        CVF(f02_21, acc[2][1][2]);
        FMA(r02_30, f02_30, ks0[0], kb0[0]);
        CVF(f02_31, acc[2][1][3]);
        MFX(acc[0][5], tb21_12, ta0_12);
        FMA(r02_01, f02_01, ks0[1], kb0[1]);
        FMA(r02_11, f02_11, ks0[1], kb0[1]);
        FMA(r02_21, f02_21, ks0[1], kb0[1]);
        FMA(r02_31, f02_31, ks0[1], kb0[1]);
        MFX(acc[0][4], tb20_13, ta0_13);
        asm volatile("s_waitcnt lgkmcnt(0)" ::: "memory");
        CPK(o02_0, r02_00, r02_01);
        CPK(o02_1, r02_10, r02_11);
        MFX(acc[0][5], tb21_13, ta0_13);
        ST1(o02_0, vG2, 2, 0);
        ST1(o02_1, vG2, 2, 1);
        CPK(o02_2, r02_20, r02_21);
        MFX(acc[0][4], tb20_14, ta0_14);
        CPK(o02_3, r02_30, r02_31);
        CVF(f03_00, acc[3][0][0]);
        CVF(f03_10, acc[3][0][1]);
        MFX(acc[0][5], tb21_14, ta0_14);
        ST1(o02_2, vG2, 2, 2);
        ST1(o02_3, vG2, 2, 3);
        CVF(f03_20, acc[3][0][2]);
        CVF(f03_30, acc[3][0][3]);
        MFX(acc[0][4], tb20_15, ta0_15);
        FMA(r03_00, f03_00, ks0[0], kb0[0]);
        CVF(f03_01, acc[3][1][0]);
        FMA(r03_10, f03_10, ks0[0], kb0[0]);
        CVF(f03_11, acc[3][1][1]);
        MFX(acc[0][5], tb21_15, ta0_15);
        FMA(r03_20, f03_20, ks0[0], kb0[0]);
        CVF(f03_21, acc[3][1][2]);
        FMA(r03_30, f03_30, ks0[0], kb0[0]);
        CVF(f03_31, acc[3][1][3]);
        MFX(acc[1][2], tb10_12, ta1_12);
        FMA(r03_01, f03_01, ks0[1], kb0[1]);
        FMA(r03_11, f03_11, ks0[1], kb0[1]);
        FMA(r03_21, f03_21, ks0[1], kb0[1]);
        FMA(r03_31, f03_31, ks0[1], kb0[1]);
        MFX(acc[1][3], tb11_12, ta1_12);
        CPK(o03_0, r03_00, r03_01);
        CPK(o03_1, r03_10, r03_11);
        MFX(acc[1][2], tb10_13, ta1_13);
        ST1(o03_0, vG2, 3, 0);
        ST1(o03_1, vG2, 3, 1);
        CPK(o03_2, r03_20, r03_21);
        MFX(acc[1][3], tb11_13, ta1_13);
        CPK(o03_3, r03_30, r03_31);
        CVF(f10_00, acc[0][2][0]);
        CVF(f10_10, acc[0][2][1]);
        MFX(acc[1][2], tb10_14, ta1_14);
        ST1(o03_2, vG2, 3, 2);
        ST1(o03_3, vG2, 3, 3);
        CVF(f10_20, acc[0][2][2]);
        CVF(f10_30, acc[0][2][3]);
        MFX(acc[1][3], tb11_14, ta1_14);
        FMA(r10_00, f10_00, ks4[0], kb4[0]);
        CVF(f10_01, acc[0][3][0]);
        FMA(r10_10, f10_10, ks4[0], kb4[0]);
        CVF(f10_11, acc[0][3][1]);
        MFX(acc[1][2], tb10_15, ta1_15);
        FMA(r10_20, f10_20, ks4[0], kb4[0]);
        CVF(f10_21, acc[0][3][2]);
        FMA(r10_30, f10_30, ks4[0], kb4[0]);
        CVF(f10_31, acc[0][3][3]);
        MFX(acc[1][3], tb11_15, ta1_15);
        FMA(r10_01, f10_01, ks4[1], kb4[1]);
        FMA(r10_11, f10_11, ks4[1], kb4[1]);
        FMA(r10_21, f10_21, ks4[1], kb4[1]);
        FMA(r10_31, f10_31, ks4[1], kb4[1]);
        MFX(acc[1][4], tb20_12, ta1_12);
        CPK(o10_0, r10_00, r10_01);
        CPK(o10_1, r10_10, r10_11);
        MFX(acc[1][5], tb21_12, ta1_12);
        CPK(o10_2, r10_20, r10_21);
        CPK(o10_3, r10_30, r10_31);
        MFX(acc[1][4], tb20_13, ta1_13);
        CVF(f20_00, acc[0][4][0]);
        CVF(f20_10, acc[0][4][1]);
        CVF(f20_20, acc[0][4][2]);
        CVF(f20_30, acc[0][4][3]);
        MFX(acc[1][5], tb21_13, ta1_13);
        FMA(r20_00, f20_00, ks4[2], kb4[2]);
        CVF(f20_01, acc[0][5][0]);
        FMA(r20_10, f20_10, ks4[2], kb4[2]);
        CVF(f20_11, acc[0][5][1]);
        MFX(acc[1][4], tb20_14, ta1_14);
        FMA(r20_20, f20_20, ks4[2], kb4[2]);
        CVF(f20_21, acc[0][5][2]);
        FMA(r20_30, f20_30, ks4[2], kb4[2]);
        CVF(f20_31, acc[0][5][3]);
        MFX(acc[1][5], tb21_14, ta1_14);
        FMA(r20_01, f20_01, ks4[3], kb4[3]);
        FMA(r20_11, f20_11, ks4[3], kb4[3]);
        FMA(r20_21, f20_21, ks4[3], kb4[3]);
        FMA(r20_31, f20_31, ks4[3], kb4[3]);
        MFX(acc[1][4], tb20_15, ta1_15);
        CPK(o20_0, r20_00, r20_01);
        CPK(o20_1, r20_10, r20_11);
        MFX(acc[1][5], tb21_15, ta1_15);
        ST2(o10_0, o20_0, 0, 0);
        ST2(o10_1, o20_1, 0, 1);
        CPK(o20_2, r20_20, r20_21);
        MFX(acc[2][2], tb10_12, ta2_12);
        CPK(o20_3, r20_30, r20_31);
        CVF(f11_00, acc[1][2][0]);
        CVF(f11_10, acc[1][2][1]);
        MFX(acc[2][3], tb11_12, ta2_12);
        ST2(o10_2, o20_2, 0, 2);
        ST2(o10_3, o20_3, 0, 3);
        CVF(f11_20, acc[1][2][2]);
        CVF(f11_30, acc[1][2][3]);
        MFX(acc[2][2], tb10_13, ta2_13);
        FMA(r11_00, f11_00, ks4[0], kb4[0]);
        CVF(f11_01, acc[1][3][0]);
        FMA(r11_10, f11_10, ks4[0], kb4[0]);
        CVF(f11_11, acc[1][3][1]);
        MFX(acc[2][3], tb11_13, ta2_13);
        FMA(r11_20, f11_20, ks4[0], kb4[0]);
        CVF(f11_21, acc[1][3][2]);
        FMA(r11_30, f11_30, ks4[0], kb4[0]);
        CVF(f11_31, acc[1][3][3]);
        MFX(acc[2][2], tb10_14, ta2_14);
        FMA(r11_01, f11_01, ks4[1], kb4[1]);
        FMA(r11_11, f11_11, ks4[1], kb4[1]);
        FMA(r11_21, f11_21, ks4[1], kb4[1]);
        FMA(r11_31, f11_31, ks4[1], kb4[1]);
        MFX(acc[2][3], tb11_14, ta2_14);
        CPK(o11_0, r11_00, r11_01);
        CPK(o11_1, r11_10, r11_11);
        MFX(acc[2][2], tb10_15, ta2_15);
        CPK(o11_2, r11_20, r11_21);
        CPK(o11_3, r11_30, r11_31);
        MFX(acc[2][3], tb11_15, ta2_15);
        CVF(f21_00, acc[1][4][0]);
        CVF(f21_10, acc[1][4][1]);
        CVF(f21_20, acc[1][4][2]);
        CVF(f21_30, acc[1][4][3]);
        MFX(acc[2][4], tb20_12, ta2_12);
        FMA(r21_00, f21_00, ks4[2], kb4[2]);
        CVF(f21_01, acc[1][5][0]);
        FMA(r21_10, f21_10, ks4[2], kb4[2]);
        CVF(f21_11, acc[1][5][1]);
        MFX(acc[2][5], tb21_12, ta2_12);
        FMA(r21_20, f21_20, ks4[2], kb4[2]);
        CVF(f21_21, acc[1][5][2]);
        FMA(r21_30, f21_30, ks4[2], kb4[2]);
        CVF(f21_31, acc[1][5][3]);
        MFX(acc[2][4], tb20_13, ta2_13);
        FMA(r21_01, f21_01, ks4[3], kb4[3]);
        FMA(r21_11, f21_11, ks4[3], kb4[3]);
        FMA(r21_21, f21_21, ks4[3], kb4[3]);
        FMA(r21_31, f21_31, ks4[3], kb4[3]);
        MFX(acc[2][5], tb21_13, ta2_13);
        CPK(o21_0, r21_00, r21_01);
        CPK(o21_1, r21_10, r21_11);
        MFX(acc[2][4], tb20_14, ta2_14);
        ST2(o11_0, o21_0, 1, 0);
        ST2(o11_1, o21_1, 1, 1);
        CPK(o21_2, r21_20, r21_21);
        MFX(acc[2][5], tb21_14, ta2_14);
        CPK(o21_3, r21_30, r21_31);
        CVF(f12_00, acc[2][2][0]);
        CVF(f12_10, acc[2][2][1]);
        MFX(acc[2][4], tb20_15, ta2_15);
        ST2(o11_2, o21_2, 1, 2);
        ST2(o11_3, o21_3, 1, 3);
        CVF(f12_20, acc[2][2][2]);
        CVF(f12_30, acc[2][2][3]);
        MFX(acc[2][5], tb21_15, ta2_15);
        FMA(r12_00, f12_00, ks4[0], kb4[0]);
        CVF(f12_01, acc[2][3][0]);
        FMA(r12_10, f12_10, ks4[0], kb4[0]);
        CVF(f12_11, acc[2][3][1]);
        MFX(acc[3][2], tb10_12, ta3_12);
        FMA(r12_20, f12_20, ks4[0], kb4[0]);
        CVF(f12_21, acc[2][3][2]);
        FMA(r12_30, f12_30, ks4[0], kb4[0]);
        CVF(f12_31, acc[2][3][3]);
        MFX(acc[3][3], tb11_12, ta3_12);
        FMA(r12_01, f12_01, ks4[1], kb4[1]);
        FMA(r12_11, f12_11, ks4[1], kb4[1]);
        FMA(r12_21, f12_21, ks4[1], kb4[1]);
        FMA(r12_31, f12_31, ks4[1], kb4[1]);
        MFX(acc[3][2], tb10_13, ta3_13);
        CPK(o12_0, r12_00, r12_01);
        CPK(o12_1, r12_10, r12_11);
        MFX(acc[3][3], tb11_13, ta3_13);
        CPK(o12_2, r12_20, r12_21);
        CPK(o12_3, r12_30, r12_31);
        MFX(acc[3][2], tb10_14, ta3_14);
        CVF(f22_00, acc[2][4][0]);
        CVF(f22_10, acc[2][4][1]);
        CVF(f22_20, acc[2][4][2]);
        CVF(f22_30, acc[2][4][3]);
        MFX(acc[3][3], tb11_14, ta3_14);
        FMA(r22_00, f22_00, ks4[2], kb4[2]);
        CVF(f22_01, acc[2][5][0]);
        FMA(r22_10, f22_10, ks4[2], kb4[2]);
        CVF(f22_11, acc[2][5][1]);
        MFX(acc[3][2], tb10_15, ta3_15);
        FMA(r22_20, f22_20, ks4[2], kb4[2]);
        CVF(f22_21, acc[2][5][2]);
        FMA(r22_30, f22_30, ks4[2], kb4[2]);
        CVF(f22_31, acc[2][5][3]);
        MFX(acc[3][3], tb11_15, ta3_15);
        FMA(r22_01, f22_01, ks4[3], kb4[3]);
        FMA(r22_11, f22_11, ks4[3], kb4[3]);
        FMA(r22_21, f22_21, ks4[3], kb4[3]);
        FMA(r22_31, f22_31, ks4[3], kb4[3]);
        MFX(acc[3][4], tb20_12, ta3_12);
        CPK(o22_0, r22_00, r22_01);
        CPK(o22_1, r22_10, r22_11);
        MFX(acc[3][5], tb21_12, ta3_12);
        ST2(o12_0, o22_0, 2, 0);
        ST2(o12_1, o22_1, 2, 1);
        CPK(o22_2, r22_20, r22_21);
        MFX(acc[3][4], tb20_13, ta3_13);
        CPK(o22_3, r22_30, r22_31);
        CVF(f13_00, acc[3][2][0]);
        CVF(f13_10, acc[3][2][1]);
        MFX(acc[3][5], tb21_13, ta3_13);
        ST2(o12_2, o22_2, 2, 2);
        ST2(o12_3, o22_3, 2, 3);
        CVF(f13_20, acc[3][2][2]);
        CVF(f13_30, acc[3][2][3]);
        MFX(acc[3][4], tb20_14, ta3_14);
        FMA(r13_00, f13_00, ks4[0], kb4[0]);
        CVF(f13_01, acc[3][3][0]);
        FMA(r13_10, f13_10, ks4[0], kb4[0]);
        CVF(f13_11, acc[3][3][1]);
        MFX(acc[3][5], tb21_14, ta3_14);
        FMA(r13_20, f13_20, ks4[0], kb4[0]);
        CVF(f13_21, acc[3][3][2]);
        FMA(r13_30, f13_30, ks4[0], kb4[0]);
        CVF(f13_31, acc[3][3][3]);
        MFX(acc[3][4], tb20_15, ta3_15);
        FMA(r13_01, f13_01, ks4[1], kb4[1]);
        FMA(r13_11, f13_11, ks4[1], kb4[1]);
        FMA(r13_21, f13_21, ks4[1], kb4[1]);
        FMA(r13_31, f13_31, ks4[1], kb4[1]);
        MFX(acc[3][5], tb21_15, ta3_15);
        asm volatile("s_nop 7" ::: "memory");
        CPK(o13_0, r13_00, r13_01);
        CPK(o13_1, r13_10, r13_11);
        CPK(o13_2, r13_20, r13_21);
        CPK(o13_3, r13_30, r13_31);
        CVF(f23_00, acc[3][4][0]);
        CVF(f23_10, acc[3][4][1]);
        CVF(f23_20, acc[3][4][2]);
        CVF(f23_30, acc[3][4][3]);
        FMA(r23_00, f23_00, ks4[2], kb4[2]);
        CVF(f23_01, acc[3][5][0]);
        FMA(r23_10, f23_10, ks4[2], kb4[2]);
        CVF(f23_11, acc[3][5][1]);
        FMA(r23_20, f23_20, ks4[2], kb4[2]);
        CVF(f23_21, acc[3][5][2]);
        FMA(r23_30, f23_30, ks4[2], kb4[2]);
        CVF(f23_31, acc[3][5][3]);
        FMA(r23_01, f23_01, ks4[3], kb4[3]);
        FMA(r23_11, f23_11, ks4[3], kb4[3]);
        FMA(r23_21, f23_21, ks4[3], kb4[3]);
        FMA(r23_31, f23_31, ks4[3], kb4[3]);
        CPK(o23_0, r23_00, r23_01);
        CPK(o23_1, r23_10, r23_11);
        ST2(o13_0, o23_0, 3, 0);
        ST2(o13_1, o23_1, 3, 1);
        CPK(o23_2, r23_20, r23_21);
        CPK(o23_3, r23_30, r23_31);
        ST2(o13_2, o23_2, 3, 2);
        ST2(o13_3, o23_3, 3, 3);
    }
    asm volatile(".Lpf_end:");
}

extern "C" void kernel_launch(void* const* d_in, const int* in_sizes, int n_in,
                              void* d_out, int out_size, void* d_ws, size_t ws_size,
                              hipStream_t stream) {
    const int* qx = (const int*)d_in[0];
    const int* qw = (const int*)d_in[1];
    const __hip_bfloat16* scale_x = (const __hip_bfloat16*)d_in[2];
    const int* zero_x = (const int*)d_in[3];
    const __hip_bfloat16* scale_w = (const __hip_bfloat16*)d_in[4];
    const __hip_bfloat16* bias = (const __hip_bfloat16*)d_in[5];
    (void)hipFuncSetAttribute((const void*)fused_kernel, hipFuncAttributeMaxDynamicSharedMemorySize, SMEM_BYTES);
    fused_kernel<<<256, 512, SMEM_BYTES, stream>>>(qx, qw, scale_x, zero_x, scale_w, bias, (__hip_bfloat16*)d_out,
                                                   (uint8_t*)d_ws);
}
```
